```python
import torch

def _softmax(x, axis=-1):
    return torch.softmax(x, dim=axis)
def _gelu(x):
    return torch.nn.functional.gelu(x, approximate="tanh")
def _silu(x):
    return torch.nn.functional.silu(x)
def _sigmoid(x):
    return torch.sigmoid(x)
def _layernorm(x, axis=-1, eps=1e-5):
    m = x.mean(dim=axis, keepdim=True)
    v = x.var(dim=axis, keepdim=True, unbiased=False)
    return (x - m) / torch.sqrt(v + eps)
def _scatter_add(tgt, idx, src, axis=0):
    return tgt.clone().index_add_(axis, idx, src)
def _segment_sum(src, seg, n):
    out = torch.zeros((n,) + src.shape[1:], dtype=src.dtype)
    return out.index_add_(0, seg, src)
def _pad(x, pw):
    flat = tuple(v for pair in reversed(pw) for v in pair)
    return torch.nn.functional.pad(x, flat)

kernel_name = 'interleave_layers__ssm_conv_s4d__glu_gated_glu__prompt'

def setup_inputs():

    _w = (lambda a: (lambda t: t.to(torch.bfloat16) if t.is_floating_point() else t)(torch.as_tensor(a)))
    def _A_setup():
        Bsz, L, D, N = 1, 2048, 512, 128
        return {
            "u": _w((lambda *s: torch.randn(*s, dtype=torch.bfloat16) * 0.05)(Bsz, L, D)),
            "log_A_real": _w((lambda *s: torch.randn(*s, dtype=torch.bfloat16) * 3.2)(D, N) * 0.5),
            "A_imag": _w((lambda *s: torch.randn(*s, dtype=torch.bfloat16) * 0.1)(D, N) * 3.14),
            "C_real": _w((lambda *s: torch.randn(*s, dtype=torch.bfloat16) * 0.1)(D, N) / N ** 0.5),
            "C_imag": _w((lambda *s: torch.randn(*s, dtype=torch.bfloat16) * 0.1)(D, N) / N ** 0.5),
            "log_dt": _w((lambda *s: torch.randn(*s, dtype=torch.bfloat16) * 0.1)(D) * 0.5 - 4.0),
            "D_skip": _w((lambda *s: torch.randn(*s, dtype=torch.bfloat16) * 0.1)(D)),
        }
    _A_in = _A_setup()

    def _B_setup():
        B, S, D, F = 1, 2048, 512, 1376
        return {
            "x": _w((lambda *s: torch.randn(*s, dtype=torch.bfloat16) * 0.05)(B, S, D)),
            "w_gate": _w((lambda *s: torch.randn(*s, dtype=torch.bfloat16) * 0.05)(D, F)),
            "w_up": _w((lambda *s: torch.randn(*s, dtype=torch.bfloat16) * 0.05)(D, F)),
            "w_down": _w((lambda *s: torch.randn(*s, dtype=torch.bfloat16) * 0.05)(F, D)),
        }
    _B_in = _B_setup()

    return {
        "A_u": _A_in["u"],
        "A_log_A_real": _A_in["log_A_real"],
        "A_A_imag": _A_in["A_imag"],
        "A_C_real": _A_in["C_real"],
        "A_C_imag": _A_in["C_imag"],
        "A_log_dt": _A_in["log_dt"],
        "A_D_skip": _A_in["D_skip"],
        "B_x": _B_in["x"],
        "B_w_gate": _B_in["w_gate"],
        "B_w_up": _B_in["w_up"],
        "B_w_down": _B_in["w_down"],
    }

def reference(A_u, A_log_A_real, A_A_imag, A_C_real, A_C_imag, A_log_dt, A_D_skip, B_x, B_w_gate, B_w_up, B_w_down):
    def _A_ref(u, log_A_real, A_imag, C_real, C_imag, log_dt, D_skip):
        L = u.shape[1]
        dt = torch.exp(log_dt)
        A = -torch.exp(log_A_real) + 1j * A_imag
        C = (C_real + 1j * C_imag) * dt[:, None]
        pos = (lambda x, dt: x.to(dt))(torch.arange(L), log_dt.dtype)
        P = torch.exp((A * dt[:, None])[:, :, None] * pos[None, None, :])
        K = 2.0 * torch.einsum("dn,dnl->dl", C, P).real
        L2 = 2 * L
        u_t = (lambda x, *d: x.permute(*d))(u, 0, 2, 1)
        Kf = (lambda x, *a, **k: torch.fft.rfft(x.float(), *a, **k))(K, L2)
        Uf = (lambda x, *a, **k: torch.fft.rfft(x.float(), *a, **k))(u_t, L2)
        y = (lambda x, *a, **k: torch.fft.irfft(x, *a, **k).to(torch.bfloat16))(Uf * Kf[None, :, :], L2)[:, :, :L]
        y = (lambda x, *d: x.permute(*d))(y, 0, 2, 1)
        return y + u * D_skip[None, None, :]

    def _B_ref(x, w_gate, w_up, w_down):
        g = torch.einsum("bsd,df->bsf", x, w_gate)
        u = torch.einsum("bsd,df->bsf", x, w_up)
        h = _sigmoid(g) * u
        return torch.einsum("bsf,fd->bsd", h, w_down)

    x = A_u
    x = x + _A_ref(_layernorm(x, axis=-1), A_log_A_real, A_A_imag, A_C_real, A_C_imag, A_log_dt, A_D_skip)
    x = x + _B_ref(_layernorm(x, axis=-1), B_w_gate, B_w_up, B_w_down)
    x = x + _A_ref(_layernorm(x, axis=-1), A_log_A_real, A_A_imag, A_C_real, A_C_imag, A_log_dt, A_D_skip)
    x = x + _B_ref(_layernorm(x, axis=-1), B_w_gate, B_w_up, B_w_down)
    return x
```

```cpp
#include <hip/hip_runtime.h>
#include <hip/hip_bf16.h>
#include <stdint.h>

typedef unsigned short u16;
typedef unsigned int u32;
typedef __attribute__((ext_vector_type(8))) __bf16 bf16x8;
typedef __attribute__((ext_vector_type(2))) __bf16 bf16x2;
typedef __attribute__((ext_vector_type(8))) _Float16 f16x8;
typedef __attribute__((ext_vector_type(2))) _Float16 f16x2;
typedef __attribute__((ext_vector_type(4))) float f32x4;
typedef __attribute__((ext_vector_type(2))) float f32x2;
typedef __attribute__((ext_vector_type(4))) u32 u32x4;
typedef __attribute__((ext_vector_type(2))) u32 u32x2;
typedef __attribute__((ext_vector_type(4))) short s16x4;
typedef __attribute__((ext_vector_type(2))) double f64x2;
#define LAS __attribute__((address_space(3)))
#define DEV __device__ __forceinline__

constexpr int SL = 2048, SD = 512, SN = 128, SF = 1376;
constexpr int NKF = 43;
constexpr int NCW = 15;
constexpr int KIMG_CH = 8448;
constexpr int KIMG_C1 = 4224;

constexpr size_t OFF_WGU = 0;
constexpr size_t OFF_WD = OFF_WGU + (size_t)NCW * 4 * 16 * 3 * 1024;
constexpr size_t OFF_CSP = OFF_WD + (size_t)8 * NKF * 4096;
constexpr size_t OFF_XA = OFF_CSP + (size_t)8 * 180 * 16 * 4;
constexpr size_t OFF_XB = OFF_XA + (size_t)SL * SD * 2;
constexpr size_t OFF_XC = OFF_XB + (size_t)SL * SD * 2;
constexpr size_t OFF_XD = OFF_XC + (size_t)SL * SD * 2;
constexpr size_t OFF_H = OFF_XD + (size_t)SL * SD * 2;
constexpr size_t OFF_KIMG = OFF_H + (size_t)32 * NKF * 4096;
constexpr size_t OFF_ST0 = OFF_KIMG + (size_t)SD * KIMG_CH;
constexpr size_t OFF_ST2 = OFF_ST0 + (size_t)SL * 16;

DEV float bflo(u32 v) { return __uint_as_float(v << 16); }
DEV float bfhi(u32 v) { return __uint_as_float(v & 0xffff0000u); }
DEV float bf2f(u16 v) { return __uint_as_float((u32)v << 16); }
DEV u32 pk_bf16(float a, float b) {
  f32x2 v = {a, b};
  return __builtin_bit_cast(u32, __builtin_convertvector(v, bf16x2));
}
DEV u32 pk_f16(float a, float b) {
  f32x2 v = {a, b};
  return __builtin_bit_cast(u32, __builtin_convertvector(v, f16x2));
}
DEV float bfr(float a) { return bflo(pk_bf16(a, a)); }
DEV float dot2bf(u32 a, u32 b, float c) {
  return __builtin_amdgcn_fdot2_f32_bf16(__builtin_bit_cast(bf16x2, a), __builtin_bit_cast(bf16x2, b), c, false);
}
DEV f32x4 mfma_bf16(u32x4 a, u32x4 b, f32x4 c) {
  return __builtin_amdgcn_mfma_f32_16x16x32_bf16(__builtin_bit_cast(bf16x8, a), __builtin_bit_cast(bf16x8, b), c, 0, 0, 0);
}
DEV f32x4 mfma_f16(u32x4 a, u32x4 b, f32x4 c) {
  return __builtin_amdgcn_mfma_f32_16x16x32_f16(__builtin_bit_cast(f16x8, a), __builtin_bit_cast(f16x8, b), c, 0, 0, 0);
}
DEV u32x2 lds_tr(const char* p) {
  return __builtin_bit_cast(u32x2, __builtin_amdgcn_ds_read_tr16_b64_v4i16((LAS s16x4*)p));
}
DEV void ln_consts(float sum, float sumsq, float& m, float& s) {
  float mean = sum * (1.0f / SD);
  float var = fmaxf(sumsq * (1.0f / SD) - mean * mean, 0.0f);
  m = bfr(mean);
  s = bfr(__builtin_sqrtf(bfr(bfr(var) + 1e-5f)));
}

struct cpx { float r, i; };
DEV cpx cmul(cpx a, cpx b) { return {a.r * b.r - a.i * b.i, a.r * b.i + a.i * b.r}; }

constexpr int KG_ROW = 528;
constexpr int KG_CH = 64 * KG_ROW;
constexpr int KG_TAB = 2 * KG_CH;
constexpr int PREP_LDS = KG_TAB + 2 * 648 * 2;

__global__ __launch_bounds__(512, 2) void k_prep(
    const u16* __restrict__ x, const u16* __restrict__ logA, const u16* __restrict__ Aim, const u16* __restrict__ Cre,
    const u16* __restrict__ Cim, const u16* __restrict__ logdt, const u16* __restrict__ wg, const u16* __restrict__ wu,
    const u16* __restrict__ wd, char* __restrict__ ws) {
  extern __shared__ __attribute__((aligned(16))) char smem[];
  const int tid = threadIdx.x, lane = tid & 63, w = tid >> 6, b = blockIdx.x;
  if (w < 4) {
    const int ch = tid >> 7, n = tid & 127, d = 2 * b + ch;
    const float dt = bfr(expf(bf2f(logdt[d])));
    const float ar = -bfr(expf(bf2f(logA[d * SN + n]))) * dt;
    const float bi = bf2f(Aim[d * SN + n]) * dt;
    const float cr = bf2f(Cre[d * SN + n]), ci = bf2f(Cim[d * SN + n]);
    float mx = fmaxf(fmaxf(fabsf(cr), fabsf(ci)),
                     fmaxf(fabsf(bf2f(Cre[d * SN + (n ^ 64)])), fabsf(bf2f(Cim[d * SN + (n ^ 64)]))));
#pragma unroll
    for (int o = 1; o < 64; o <<= 1) mx = fmaxf(mx, __shfl_xor(mx, o));
    const float sc = __uint_as_float((__float_as_uint(fmaxf(mx, 1e-30f)) & 0x7f800000u) + 0x00800000u);
    const float isc = 1.0f / sc;
    const float a2 = ar * 1.44269504088896340736f;
    const float rh = bi * 0.15915494309189533577f;
    const float rl = fmaf(bi, 0.15915494309189533577f, -rh) + bi * 6.420638e-9f;
    cpx z[12];
#pragma unroll
    for (int p = 0; p < 12; ++p) {
      const float s = (float)(1 << p);
      const float mag = __builtin_amdgcn_exp2f(a2 * s);
      const float rev = __builtin_amdgcn_fractf(rh * s) + rl * s;
      z[p].r = mag * __builtin_amdgcn_cosf(rev);
      z[p].i = mag * __builtin_amdgcn_sinf(rev);
    }
    char* base = smem + ch * KG_CH + n * 4;
    {
      cpx X[32];
      X[0] = {1.0f, 0.0f};
#pragma unroll
      for (int q = 0; q < 5; ++q) {
        X[1 << q] = z[3 + q];
#pragma unroll
        for (int j = 1; j < (1 << q); ++j) X[(1 << q) + j] = cmul(X[j], z[3 + q]);
      }
#pragma unroll
      for (int i = 0; i < 32; ++i) *(u32*)(base + i * KG_ROW) = pk_f16(X[i].r, X[i].i);
    }
    {
      cpx Y[21];
      Y[0] = {cr * isc, ci * isc};
      Y[1] = cmul(Y[0], z[0]);
      Y[2] = cmul(Y[0], z[1]);
      Y[3] = cmul(Y[2], z[0]);
      Y[4] = cmul(Y[0], z[2]);
      Y[5] = cmul(Y[4], z[0]);
      Y[6] = cmul(Y[4], z[1]);
      Y[7] = cmul(Y[6], z[0]);
      Y[8] = cmul(Y[0], z[8]);
      Y[9] = cmul(Y[2], z[8]);
      Y[10] = cmul(Y[4], z[8]);
      Y[11] = cmul(Y[6], z[8]);
      Y[12] = cmul(Y[0], z[9]);
      Y[13] = cmul(Y[4], z[9]);
      Y[14] = cmul(Y[8], z[9]);
      Y[15] = cmul(Y[10], z[9]);
      Y[16] = cmul(Y[0], z[10]);
      Y[17] = cmul(Y[8], z[10]);
      Y[18] = cmul(Y[12], z[10]);
      Y[19] = cmul(Y[14], z[10]);
      Y[20] = cmul(Y[0], z[11]);
#pragma unroll
      for (int c = 0; c < 21; ++c) *(u32*)(base + (32 + c) * KG_ROW) = pk_f16(Y[c].r, -Y[c].i);
#pragma unroll
      for (int c = 21; c < 32; ++c) *(u32*)(base + (32 + c) * KG_ROW) = 0u;
    }
    __syncthreads();
    {
      const int xt = w & 1, r = lane & 15, g = lane >> 4;
      const char* pa = smem + ch * KG_CH + (16 * xt + r) * KG_ROW + g * 16;
      const char* pb = smem + ch * KG_CH + (32 + r) * KG_ROW + g * 16;
      f32x4 acc0 = {0, 0, 0, 0}, acc1 = {0, 0, 0, 0};
#pragma unroll
      for (int s = 0; s < 8; ++s) {
        const u32x4 a = *(const u32x4*)(pa + s * 64);
        acc0 = mfma_f16(a, *(const u32x4*)(pb + s * 64), acc0);
        acc1 = mfma_f16(a, *(const u32x4*)(pb + 16 * KG_ROW + s * 64), acc1);
      }
      const float fs = 2.0f * dt * sc;
      u16* tab = (u16*)(smem + KG_TAB) + ch * 648;
      int tb, ts;
      if (r < 8) { tb = r; ts = 8; }
      else if (r < 12) { tb = 256 + (r - 8); ts = 4; }
      else { tb = 384 + 64 * ((r - 12) >> 1) + ((r - 12) & 1); ts = 2; }
      const int i0 = 16 * xt + 4 * g;
#pragma unroll
      for (int e = 0; e < 4; ++e) {
        tab[tb + ts * (i0 + e)] = (u16)pk_bf16(acc0[e] * fs, 0.f);
        if (r < 4) tab[512 + 32 * r + i0 + e] = (u16)pk_bf16(acc1[e] * fs, 0.f);
        if (r == 4 && i0 + e == 0) tab[640] = (u16)pk_bf16(acc1[e] * fs, 0.f);
      }
    }
    __syncthreads();
    {
      const u16* tab = (const u16*)(smem + KG_TAB) + ch * 648;
      char* img = ws + OFF_KIMG + (size_t)d * KIMG_CH;
      for (int c = n; c < KIMG_CH / 16; c += 128) {
        const int cp = c >= KIMG_C1 / 16;
        const int f0 = (cp ? (c - KIMG_C1 / 16) * 8 - 14 : c * 8 - 16);
        u32x4 v;
#pragma unroll
        for (int j = 0; j < 4; ++j) {
          u32 pr = 0;
#pragma unroll
          for (int hh = 0; hh < 2; ++hh) {
            const int f = f0 + 2 * j + hh;
            const u32 bits = pk_bf16((float)f, 0.f) & 0xffffu;
            int idx = f < 128 ? f : (int)bits - 0x4300 + 128;
            const bool ok = (f >= 0) && (f < SL);
            idx = ok ? idx : 0;
            const u32 val = ok ? (u32)tab[idx] : 0u;
            pr |= val << (16 * hh);
          }
          v[j] = pr;
        }
        *(u32x4*)(img + c * 16) = v;
      }
    }
  } else if (w == 4) {
    u32x4 row[8];
#pragma unroll
    for (int r = 0; r < 8; ++r) row[r] = *(const u32x4*)(x + (size_t)(8 * b + r) * SD + lane * 8);
    float s1[8], s2[8];
#pragma unroll
    for (int r = 0; r < 8; ++r) {
      float a = 0.f, q = 0.f;
#pragma unroll
      for (int j = 0; j < 4; ++j) { a = dot2bf(row[r][j], 0x3f803f80u, a); q = dot2bf(row[r][j], row[r][j], q); }
#pragma unroll
      for (int o = 1; o < 64; o <<= 1) { a += __shfl_xor(a, o); q += __shfl_xor(q, o); }
      s1[r] = a; s2[r] = q;
    }
    u16* xT = (u16*)(ws + OFF_XA);
#pragma unroll
    for (int e = 0; e < 8; ++e) {
      u32x4 v;
#pragma unroll
      for (int j = 0; j < 4; ++j)
        v[j] = __builtin_amdgcn_perm(row[2 * j + 1][e >> 1], row[2 * j][e >> 1], (e & 1) ? 0x07060302u : 0x05040100u);
      *(u32x4*)(xT + (size_t)(lane * 8 + e) * SL + 8 * b) = v;
    }
    float a = s1[0], q = s2[0];
#pragma unroll
    for (int r = 1; r < 8; ++r) { a = (lane == r) ? s1[r] : a; q = (lane == r) ? s2[r] : q; }
    if (lane < 8) {
      f64x2 v = {(double)a, (double)q};
      ((f64x2*)(ws + OFF_ST0))[lane * 256 + b] = v;
      f64x2 zz = {0.0, 0.0};
      ((f64x2*)(ws + OFF_ST2))[lane * 256 + b] = zz;
    }
  } else {
    const int mat = w - 5;
    if (b >= 176) return;
    const int ko = lane >> 3, no = lane & 7;
    if (mat < 2) {
      const int kg = b & 7, ng = b >> 3;
      const int T = 8 * ng + no, koct = 8 * kg + ko;
      const bool ok = T < 172;
      const u16* src = (mat ? wu : wg) + (size_t)(8 * koct) * SF + 8 * (ok ? T : 0);
      u32x4 row[8];
#pragma unroll
      for (int r = 0; r < 8; ++r) row[r] = *(const u32x4*)(src + (size_t)r * SF);
      const int c = T / 12, wv = (T % 12) / 3, t = T % 3;
      char* dst = ws + OFF_WGU + ((((size_t)(c * 4 + wv) * 16 + (koct >> 2)) * 3 + t) << 10) + (koct & 3) * 256 + mat * 32;
      float cs[8];
#pragma unroll
      for (int e = 0; e < 8; ++e) {
        u32x4 v;
        float s = 0.f;
#pragma unroll
        for (int j = 0; j < 4; ++j) {
          v[j] = __builtin_amdgcn_perm(row[2 * j + 1][e >> 1], row[2 * j][e >> 1], (e & 1) ? 0x07060302u : 0x05040100u);
          s = dot2bf(v[j], 0x3f803f80u, s);
        }
        if (ok) *(u32x4*)(dst + (e >> 1) * 64 + (e & 1) * 16) = v;
        s += __shfl_xor(s, 8); s += __shfl_xor(s, 16); s += __shfl_xor(s, 32);
        cs[e] = s;
      }
      float a = cs[0];
#pragma unroll
      for (int e = 1; e < 8; ++e) a = (ko == e) ? cs[e] : a;
      if (ok) ((float*)(ws + OFF_CSP))[(kg * 180 + T) * 16 + (ko >> 1) * 4 + (ko & 1) + mat * 2] = a;
    } else {
      const int ng = b & 7, kg = b >> 3;
      const int koct = 8 * kg + ko, noct = 8 * ng + no;
      const bool ok = koct < 172;
      const u16* src = wd + (size_t)(8 * (ok ? koct : 0)) * SD + 8 * noct;
      u32x4 row[8];
#pragma unroll
      for (int r = 0; r < 8; ++r) row[r] = *(const u32x4*)(src + (size_t)r * SD);
      char* dst = ws + OFF_WD + (((size_t)(ng * NKF + (koct >> 2)) * 4 + (no >> 1)) << 10) + (koct & 3) * 256 + (no & 1) * 128;
#pragma unroll
      for (int e = 0; e < 8; ++e) {
        u32x4 v;
#pragma unroll
        for (int j = 0; j < 4; ++j)
          v[j] = __builtin_amdgcn_perm(row[2 * j + 1][e >> 1], row[2 * j][e >> 1], (e & 1) ? 0x07060302u : 0x05040100u);
        if (ok) *(u32x4*)(dst + e * 16) = v;
      }
    }
  }
}

constexpr int CV_U = 2 * KIMG_CH;
constexpr int CV_UROW = (128 + 1024) * 2;
constexpr int CV_P = CV_U + 4 * CV_UROW;
constexpr int CV_PROW = 2048 * 4 + 64 * 16 + 16;
constexpr int CONV_LDS = CV_P + 4 * CV_PROW;

__global__ __launch_bounds__(256, 1) void k_conv(const u16* __restrict__ xin, const f64x2* __restrict__ st,
                                                 const char* __restrict__ kimg, const u16* __restrict__ dskip,
                                                 u16* __restrict__ xout) {
  extern __shared__ __attribute__((aligned(16))) char smem[];
  const int tid = threadIdx.x, lane = tid & 63, w = tid >> 6, b = blockIdx.x;
  const int d0 = 2 * b;
  u32x4 xv[2];
  xv[0] = *(const u32x4*)(xin + (size_t)d0 * SL + 8 * tid);
  xv[1] = *(const u32x4*)(xin + (size_t)(d0 + 1) * SL + 8 * tid);
  f64x2 sv[8];
#pragma unroll
  for (int e = 0; e < 8; ++e) sv[e] = st[e * 256 + tid];
  u32x4 kv[5];
  const char* ksrc = kimg + (size_t)d0 * KIMG_CH;
#pragma unroll
  for (int j = 0; j < 4; ++j) kv[j] = *(const u32x4*)(ksrc + (j * 256 + tid) * 16);
  if (tid < 32) kv[4] = *(const u32x4*)(ksrc + (1024 + tid) * 16);
  const float dsk[2] = {bf2f(dskip[d0]), bf2f(dskip[d0 + 1])};
  if (tid < 64) *(u32x4*)(smem + CV_U + (tid >> 4) * CV_UROW + (tid & 15) * 16) = u32x4{0, 0, 0, 0};
  float un[2][8];
#pragma unroll
  for (int e = 0; e < 8; ++e) {
    const double mean = sv[e][0] * (1.0 / SD);
    const float var = fmaxf((float)(sv[e][1] * (1.0 / SD) - mean * mean), 0.0f);
    const float m = bfr((float)mean);
    const float s = bfr(__builtin_sqrtf(bfr(bfr(var) + 1e-5f)));
    const float rs = 1.0f / s;
#pragma unroll
    for (int c = 0; c < 2; ++c) {
      const float xx = (e & 1) ? bfhi(xv[c][e >> 1]) : bflo(xv[c][e >> 1]);
      un[c][e] = bfr(xx - m) * rs;
    }
  }
  u32x2 up[2][2];
#pragma unroll
  for (int c = 0; c < 2; ++c)
#pragma unroll
    for (int r = 0; r < 2; ++r) {
      up[c][r] = u32x2{pk_bf16(un[c][r], un[c][2 + r]), pk_bf16(un[c][4 + r], un[c][6 + r])};
      *(u32x2*)(smem + CV_U + (c * 2 + r) * CV_UROW + 256 + tid * 8) = up[c][r];
    }
#pragma unroll
  for (int j = 0; j < 4; ++j) *(u32x4*)(smem + (j * 256 + tid) * 16) = kv[j];
  if (tid < 32) *(u32x4*)(smem + (1024 + tid) * 16) = kv[4];
  __syncthreads();

  {
    const int ch = w >> 1;
    const int np = lane & 15, h = lane >> 4, q = (lane & 15) >> 2, p = lane & 3;
    const char* ka = smem + ch * KIMG_CH + ((q & 1) ? KIMG_C1 + 2 * 14 : 2 * 16) + 2 * (48 - 16 * h - 2 * q + 4 * p);
    const char* ua = smem + CV_U + w * CV_UROW + 256 + 2 * (8 * (np + h) - 24);
    f32x4 acc[8];
#pragma unroll
    for (int j = 0; j < 8; ++j) acc[j] = f32x4{0, 0, 0, 0};
#pragma unroll
    for (int dl = 0; dl < 4; ++dl) {
      u32x4 kf[8], uf[8];
#pragma unroll
      for (int i = 0; i < 8; ++i) {
        const u32x2 lo = lds_tr(ka + (4 * i + dl) * 128);
        const u32x2 hi = lds_tr(ka + (4 * i + dl) * 128 - 16);
        kf[i] = u32x4{lo[0], lo[1], hi[0], hi[1]};
        uf[i] = *(const u32x4*)(ua + i * 256 - dl * 64);
      }
#pragma unroll
      for (int i = 0; i < 8; ++i)
#pragma unroll
        for (int j = i; j < 8; ++j) acc[j] = mfma_bf16(kf[i], uf[j - i], acc[j]);
    }
    char* pp = smem + CV_P + w * CV_PROW + 16 + np * 64 + h * 16 + (np >> 1) * 16;
#pragma unroll
    for (int j = 0; j < 8; ++j) *(f32x4*)(pp + j * (1024 + 128)) = acc[j];
    if (lane == 0) *(float*)(smem + CV_P + w * CV_PROW + 12) = 0.f;
  }
  __syncthreads();
#pragma unroll
  for (int c = 0; c < 2; ++c) {
    const int off = 16 + tid * 32 + (tid >> 2) * 16;
    const char* p0 = smem + CV_P + (2 * c) * CV_PROW + off;
    const char* p1 = smem + CV_P + (2 * c + 1) * CV_PROW + off;
    const f32x4 a0 = *(const f32x4*)p0, a1 = *(const f32x4*)(p0 + 16);
    const f32x4 b0 = *(const f32x4*)p1, b1 = *(const f32x4*)(p1 + 16);
    const float bm = *(const float*)(p1 - (((tid & 3) == 0 && tid > 0) ? 20 : 4));
    float y[8] = {a0[0] + bm, a0[1] + b0[0], a0[2] + b0[1], a0[3] + b0[2],
                  a1[0] + b0[3], a1[1] + b1[0], a1[2] + b1[1], a1[3] + b1[2]};
    float o[8];
#pragma unroll
    for (int e = 0; e < 8; ++e) {
      const u32 uw = up[c][e & 1][e >> 2];
      const float u = (e & 2) ? bfhi(uw) : bflo(uw);
      const float xx = (e & 1) ? bfhi(xv[c][e >> 1]) : bflo(xv[c][e >> 1]);
      o[e] = xx + bfr(bfr(y[e]) + bfr(u * dsk[c]));
    }
    const u32x4 ov = {pk_bf16(o[0], o[1]), pk_bf16(o[2], o[3]), pk_bf16(o[4], o[5]), pk_bf16(o[6], o[7])};
    *(u32x4*)(xout + (size_t)(d0 + c) * SL + 8 * tid) = ov;
  }
}

constexpr int G1_ST = 512 * 256;
constexpr int G1_LDS = G1_ST + 128 * 8;

DEV void glds16(const void* gsrc, u32 lds_dst) {
  u32 keep;
  asm volatile("s_mov_b32 %0, m0\n\ts_mov_b32 m0, %2\n\ts_nop 0\n\tglobal_load_lds_dwordx4 %1, off\n\ts_mov_b32 m0, %0"
               : "=&s"(keep) : "v"(gsrc), "s"(lds_dst) : "memory");
}
template <int OFF>
DEV void gload16(u32x4& dst, const void* p) {
  asm volatile("global_load_dwordx4 %0, %1, off offset:%2" : "=v"(dst) : "v"(p), "n"(OFF) : "memory");
}

__global__ __launch_bounds__(256, 1) void k_gemm1(const u16* __restrict__ xT, const char* __restrict__ wgu,
                                                  const float* __restrict__ csp, char* __restrict__ hbuf) {
  extern __shared__ __attribute__((aligned(16))) char smem[];
  const int tid = threadIdx.x, lane = tid & 63;
  const int w = __builtin_amdgcn_readfirstlane(tid >> 6);
  const int xcd = blockIdx.x & 7, idx = blockIdx.x >> 3;
  const int rb = (xcd & 1) * 8 + (idx & 7), c = (xcd >> 1) * 4 + (idx >> 3);
  if (c >= NCW) return;
  const int col = lane & 15, g = lane >> 4;

  f32x4 cs[3];
#pragma unroll
  for (int t = 0; t < 3; ++t) cs[t] = *(const f32x4*)(csp + ((lane & 7) * 180 + c * 12 + w * 3 + t) * 16 + g * 4);
  asm volatile("" ::: "memory");

  const u32 lds0 = (u32)(uintptr_t)smem;
  const char* xsrc[2];
#pragma unroll
  for (int o = 0; o < 2; ++o) {
    const int key = (lane >> 4) | (o << 2);
    const int seg = ((lane & 15) >> 1) ^ key;
    xsrc[o] = (const char*)xT + (size_t)(lane >> 4) * (SL * 2) + rb * 256 + seg * 32 + (lane & 1) * 16;
  }
  const char* wsrc = wgu + ((size_t)(c * 4 + w) * 48 << 10) + lane * 16;

  u32x4 wf[16][3];
#define G1_ISSUE(gi)                                                                                  \
  {                                                                                                   \
    _Pragma("unroll") for (int pc = 0; pc < 4; ++pc) {                                                \
      const int k0 = (gi) * 64 + w * 16 + pc * 4;                                                     \
      glds16(xsrc[pc >> 1] + (size_t)k0 * (SL * 2), lds0 + k0 * 256);                                 \
    }                                                                                                 \
    const char* wp = wsrc + (gi) * 6144;                                                              \
    gload16<0>(wf[2 * (gi)][0], wp); gload16<1024>(wf[2 * (gi)][1], wp); gload16<2048>(wf[2 * (gi)][2], wp); \
    gload16<3072>(wf[2 * (gi) + 1][0], wp);                                                           \
    gload16<0>(wf[2 * (gi) + 1][1], wp + 4096); gload16<1024>(wf[2 * (gi) + 1][2], wp + 4096);        \
  }
#define G1_WAIT(gi, N)                                                                                \
  asm volatile("s_waitcnt vmcnt(" #N ")"                                                              \
               : "+v"(wf[2 * (gi)][0]), "+v"(wf[2 * (gi)][1]), "+v"(wf[2 * (gi)][2]), "+v"(wf[2 * (gi) + 1][0]), \
                 "+v"(wf[2 * (gi) + 1][1]), "+v"(wf[2 * (gi) + 1][2])::"memory");                     \
  __builtin_amdgcn_s_barrier();                                                                       \
  asm volatile("" ::: "memory");

  G1_ISSUE(0) G1_ISSUE(1) G1_ISSUE(2) G1_ISSUE(3) G1_ISSUE(4) G1_ISSUE(5)

  const int q = (lane & 15) >> 2, p = lane & 3;
  const int key = q | ((g & 1) << 2);
  const char* xa[8];
#pragma unroll
  for (int mt = 0; mt < 8; ++mt) xa[mt] = smem + (8 * g + q) * 256 + ((mt ^ key) * 32) + 8 * p;

  f32x4 acc[8][3];
#pragma unroll
  for (int mt = 0; mt < 8; ++mt)
#pragma unroll
    for (int t = 0; t < 3; ++t) acc[mt][t] = f32x4{0, 0, 0, 0};
  float s1[2] = {0.f, 0.f}, s2[2] = {0.f, 0.f};
  const char* xs[2] = {smem + (8 * g + q) * 256 + (((2 * w) ^ key) * 32) + 8 * p,
                       smem + (8 * g + q) * 256 + (((2 * w + 1) ^ key) * 32) + 8 * p};

#define G1_STEP(s)                                                                                    \
  {                                                                                                   \
    _Pragma("unroll") for (int mt = 0; mt < 8; ++mt) {                                                \
      const u32x2 lo = lds_tr(xa[mt] + (s) * 8192), hi = lds_tr(xa[mt] + (s) * 8192 + 1024);          \
      const u32x4 xf = {lo[0], lo[1], hi[0], hi[1]};                                                  \
      _Pragma("unroll") for (int t = 0; t < 3; ++t) acc[mt][t] = mfma_bf16(wf[s][t], xf, acc[mt][t]); \
    }                                                                                                 \
    _Pragma("unroll") for (int j = 0; j < 2; ++j) {                                                   \
      const u32x2 lo = lds_tr(xs[j] + (s) * 8192), hi = lds_tr(xs[j] + (s) * 8192 + 1024);            \
      s1[j] = dot2bf(lo[0], 0x3f803f80u, s1[j]); s2[j] = dot2bf(lo[0], lo[0], s2[j]);                 \
      s1[j] = dot2bf(lo[1], 0x3f803f80u, s1[j]); s2[j] = dot2bf(lo[1], lo[1], s2[j]);                 \
      s1[j] = dot2bf(hi[0], 0x3f803f80u, s1[j]); s2[j] = dot2bf(hi[0], hi[0], s2[j]);                 \
      s1[j] = dot2bf(hi[1], 0x3f803f80u, s1[j]); s2[j] = dot2bf(hi[1], hi[1], s2[j]);                 \
    }                                                                                                 \
  }

  G1_WAIT(0, 50) G1_STEP(0) G1_STEP(1) G1_ISSUE(6)
  G1_WAIT(1, 50) G1_STEP(2) G1_STEP(3) G1_ISSUE(7)
  G1_WAIT(2, 50) G1_STEP(4) G1_STEP(5)
  G1_WAIT(3, 40) G1_STEP(6) G1_STEP(7)
  G1_WAIT(4, 30) G1_STEP(8) G1_STEP(9)
  G1_WAIT(5, 20) G1_STEP(10) G1_STEP(11)
  G1_WAIT(6, 10) G1_STEP(12) G1_STEP(13)
  G1_WAIT(7, 0) G1_STEP(14) G1_STEP(15)

#pragma unroll
  for (int j = 0; j < 2; ++j) {
    s1[j] += __shfl_xor(s1[j], 16); s1[j] += __shfl_xor(s1[j], 32);
    s2[j] += __shfl_xor(s2[j], 16); s2[j] += __shfl_xor(s2[j], 32);
    float m, s;
    ln_consts(s1[j], s2[j], m, s);
    if (g == 0) *(f32x2*)(smem + G1_ST + ((2 * w + j) * 16 + col) * 8) = f32x2{m, 1.0f / s};
  }
#pragma unroll
  for (int t = 0; t < 3; ++t)
#pragma unroll
    for (int e = 0; e < 4; ++e) {
      cs[t][e] += __shfl_xor(cs[t][e], 1); cs[t][e] += __shfl_xor(cs[t][e], 2); cs[t][e] += __shfl_xor(cs[t][e], 4);
    }
  __syncthreads();
  u32 hv[8][3];
#pragma unroll
  for (int mt = 0; mt < 8; ++mt) {
    const f32x2 mr = *(const f32x2*)(smem + G1_ST + (mt * 16 + col) * 8);
    const float m = mr[0], rs = mr[1];
#pragma unroll
    for (int t = 0; t < 3; ++t) {
      float hh[2];
#pragma unroll
      for (int e = 0; e < 2; ++e) {
        const float gg = bfr((acc[mt][t][e] - m * cs[t][e]) * rs);
        const float uu = bfr((acc[mt][t][2 + e] - m * cs[t][2 + e]) * rs);
        const float sg = bfr(__builtin_amdgcn_rcpf(1.0f + __builtin_amdgcn_exp2f(-1.44269504088896340736f * gg)));
        hh[e] = sg * uu;
      }
      hv[mt][t] = pk_bf16(hh[0], hh[1]);
    }
  }
#pragma unroll
  for (int t = 0; t < 3; ++t) {
    const int T = c * 12 + w * 3 + t;
    if (T < 172) {
#pragma unroll
      for (int mq = 0; mq < 2; ++mq) {
        u32 r0 = hv[4 * mq][t], r1 = hv[4 * mq + 1][t], r2 = hv[4 * mq + 2][t], r3 = hv[4 * mq + 3][t];
        auto a = __builtin_amdgcn_permlane32_swap(r0, r2, false, false); r0 = a[0]; r2 = a[1];
        auto bb = __builtin_amdgcn_permlane32_swap(r1, r3, false, false); r1 = bb[0]; r3 = bb[1];
        auto cc = __builtin_amdgcn_permlane16_swap(r0, r1, false, false); r0 = cc[0]; r1 = cc[1];
        auto dd = __builtin_amdgcn_permlane16_swap(r2, r3, false, false); r2 = dd[0]; r3 = dd[1];
        char* dst = hbuf + ((((size_t)(2 * rb + mq) * NKF + (T >> 2)) * 4 + g) << 10) + (T & 3) * 256 + col * 16;
        *(u32x4*)dst = u32x4{r0, r1, r2, r3};
      }
    }
  }
}

constexpr int G2_LDS = 4 * 3 * 4 * 1024;

template <bool FINAL>
__global__ __launch_bounds__(256, 1) void k_gemm2(const char* __restrict__ hbuf, const char* __restrict__ wdp,
                                                  const u16* __restrict__ xres, u16* __restrict__ xout,
                                                  double* __restrict__ st) {
  extern __shared__ __attribute__((aligned(16))) char smem[];
  const int tid = threadIdx.x, lane = tid & 63;
  const int w = __builtin_amdgcn_readfirstlane(tid >> 6);
  const int xcd = blockIdx.x & 7, idx = blockIdx.x >> 3;
  const int rb = xcd * 4 + (idx & 3), ct = idx >> 2;
  const int col = lane & 15, g = lane >> 4;
  const int l0 = rb * 64 + w * 16, d0 = ct * 64;

  u32x2 rv[4];
  u16 rf[4][4];
  if (!FINAL) {
#pragma unroll
    for (int nt = 0; nt < 4; ++nt) rv[nt] = *(const u32x2*)(xres + (size_t)(d0 + nt * 16 + col) * SL + l0 + 4 * g);
  } else {
#pragma unroll
    for (int nt = 0; nt < 4; ++nt)
#pragma unroll
      for (int e = 0; e < 4; ++e) rf[nt][e] = xres[(size_t)(d0 + nt * 16 + 4 * g + e) * SL + l0 + col];
  }

  const int k0 = 11 * w;
  const char* hp = hbuf + ((size_t)(rb * NKF + k0) << 12) + lane * 16;
  const char* wp = wdp + ((size_t)(ct * NKF + k0) << 12) + lane * 16;
  const int ho[4] = {(w & 3) * 1024, ((w + 1) & 3) * 1024, ((w + 2) & 3) * 1024, ((w + 3) & 3) * 1024};
  constexpr int S = 6;
  u32x4 ha[11][4], wb[11][4];
  f32x4 acc[4][4];
#pragma unroll
  for (int i = 0; i < 4; ++i)
#pragma unroll
    for (int j = 0; j < 4; ++j) acc[i][j] = f32x4{0, 0, 0, 0};
  const int last = (w == 3) ? 9 : 10;
#pragma unroll
  for (int s = 0; s < 11 + S; ++s) {
    if (s >= S) {
      const int u = s - S;
      if (u == 10 && w == 3) {
      } else {
#pragma unroll
        for (int i = 0; i < 4; ++i)
#pragma unroll
          for (int j = 0; j < 4; ++j)
            acc[i][j] = FINAL ? mfma_bf16(wb[u][j], ha[u][i], acc[i][j]) : mfma_bf16(ha[u][i], wb[u][j], acc[i][j]);
      }
    }
    if (s < 11) {
      const int ss = s < last ? s : last;
#pragma unroll
      for (int i = 0; i < 4; ++i) ha[s][i] = *(const u32x4*)(hp + ss * 4096 + ho[i]);
#pragma unroll
      for (int j = 0; j < 4; ++j) wb[s][j] = *(const u32x4*)(wp + ss * 4096 + j * 1024);
    }
    __builtin_amdgcn_sched_barrier(0);
  }
#pragma unroll
  for (int i = 1; i < 4; ++i)
#pragma unroll
    for (int j = 0; j < 4; ++j)
      *(f32x4*)(smem + (((((w + i) & 3) * 3 + (i - 1)) * 4 + j) << 10) + lane * 16) = acc[i][j];
  __syncthreads();
#pragma unroll
  for (int i = 0; i < 3; ++i)
#pragma unroll
    for (int j = 0; j < 4; ++j) acc[0][j] += *(const f32x4*)(smem + (((w * 3 + i) * 4 + j) << 10) + lane * 16);

  if (!FINAL) {
    float s1[4] = {0, 0, 0, 0}, s2[4] = {0, 0, 0, 0};
#pragma unroll
    for (int nt = 0; nt < 4; ++nt) {
      const u32 p0 = pk_bf16(bflo(rv[nt][0]) + bfr(acc[0][nt][0]), bfhi(rv[nt][0]) + bfr(acc[0][nt][1]));
      const u32 p1 = pk_bf16(bflo(rv[nt][1]) + bfr(acc[0][nt][2]), bfhi(rv[nt][1]) + bfr(acc[0][nt][3]));
      *(u32x2*)(xout + (size_t)(d0 + nt * 16 + col) * SL + l0 + 4 * g) = u32x2{p0, p1};
      const float v[4] = {bflo(p0), bfhi(p0), bflo(p1), bfhi(p1)};
#pragma unroll
      for (int e = 0; e < 4; ++e) { s1[e] += v[e]; s2[e] = fmaf(v[e], v[e], s2[e]); }
    }
#pragma unroll
    for (int e = 0; e < 4; ++e)
#pragma unroll
      for (int o = 1; o < 16; o <<= 1) { s1[e] += __shfl_xor(s1[e], o); s2[e] += __shfl_xor(s2[e], o); }
    float v = s1[0];
#pragma unroll
    for (int e = 1; e < 4; ++e) v = (col == e) ? s1[e] : v;
#pragma unroll
    for (int e = 0; e < 4; ++e) v = (col == 4 + e) ? s2[e] : v;
    if (col < 8) {
      const int l = l0 + 4 * g + (col & 3);
      unsafeAtomicAdd(st + ((l & 7) * 256 + (l >> 3)) * 2 + (col >> 2), (double)v);
    }
  } else {
#pragma unroll
    for (int nt = 0; nt < 4; ++nt) {
      const u32 p0 = pk_bf16(bf2f(rf[nt][0]) + bfr(acc[0][nt][0]), bf2f(rf[nt][1]) + bfr(acc[0][nt][1]));
      const u32 p1 = pk_bf16(bf2f(rf[nt][2]) + bfr(acc[0][nt][2]), bf2f(rf[nt][3]) + bfr(acc[0][nt][3]));
      *(u32x2*)(xout + (size_t)(l0 + col) * SD + d0 + nt * 16 + 4 * g) = u32x2{p0, p1};
    }
  }
}

extern "C" void kernel_launch(void* const* d_in, const int* in_sizes, int n_in, void* d_out, int out_size, void* d_ws,
                              size_t ws_size, hipStream_t stream) {
  const u16* x = (const u16*)d_in[0];
  const u16* logA = (const u16*)d_in[1];
  const u16* Aim = (const u16*)d_in[2];
  const u16* Cre = (const u16*)d_in[3];
  const u16* Cim = (const u16*)d_in[4];
  const u16* logdt = (const u16*)d_in[5];
  const u16* dskip = (const u16*)d_in[6];
  const u16* wg = (const u16*)d_in[8];
  const u16* wu = (const u16*)d_in[9];
  const u16* wd = (const u16*)d_in[10];
  char* ws = (char*)d_ws;
  (void)hipFuncSetAttribute((const void*)k_prep, hipFuncAttributeMaxDynamicSharedMemorySize, PREP_LDS);
  (void)hipFuncSetAttribute((const void*)k_conv, hipFuncAttributeMaxDynamicSharedMemorySize, CONV_LDS);
  (void)hipFuncSetAttribute((const void*)k_gemm1, hipFuncAttributeMaxDynamicSharedMemorySize, G1_LDS);

  u16 *xa = (u16*)(ws + OFF_XA), *xb = (u16*)(ws + OFF_XB), *xc = (u16*)(ws + OFF_XC), *xd = (u16*)(ws + OFF_XD);
  k_prep<<<256, 512, PREP_LDS, stream>>>(x, logA, Aim, Cre, Cim, logdt, wg, wu, wd, ws);
  k_conv<<<256, 256, CONV_LDS, stream>>>(xa, (const f64x2*)(ws + OFF_ST0), ws + OFF_KIMG, dskip, xb);
  k_gemm1<<<256, 256, G1_LDS, stream>>>(xb, ws + OFF_WGU, (const float*)(ws + OFF_CSP), ws + OFF_H);
  k_gemm2<false><<<256, 256, G2_LDS, stream>>>(ws + OFF_H, ws + OFF_WD, xb, xc, (double*)(ws + OFF_ST2));
  k_conv<<<256, 256, CONV_LDS, stream>>>(xc, (const f64x2*)(ws + OFF_ST2), ws + OFF_KIMG, dskip, xd);
  k_gemm1<<<256, 256, G1_LDS, stream>>>(xd, ws + OFF_WGU, (const float*)(ws + OFF_CSP), ws + OFF_H);
  k_gemm2<true><<<256, 256, G2_LDS, stream>>>(ws + OFF_H, ws + OFF_WD, xd, (u16*)d_out, nullptr);
}
```

```cpp
#include <hip/hip_runtime.h>
#include <hip/hip_bf16.h>
#include <stdint.h>
#include <type_traits>

typedef unsigned short u16;
typedef unsigned int u32;
typedef __attribute__((ext_vector_type(8))) __bf16 bf16x8;
typedef __attribute__((ext_vector_type(2))) __bf16 bf16x2;
typedef __attribute__((ext_vector_type(8))) _Float16 f16x8;
typedef __attribute__((ext_vector_type(2))) _Float16 f16x2;
typedef __attribute__((ext_vector_type(4))) float f32x4;
typedef __attribute__((ext_vector_type(2))) float f32x2;
typedef __attribute__((ext_vector_type(4))) u32 u32x4;
typedef __attribute__((ext_vector_type(2))) u32 u32x2;
typedef __attribute__((ext_vector_type(4))) short s16x4;
typedef __attribute__((ext_vector_type(2))) double f64x2;
#define LAS __attribute__((address_space(3)))
#define DEV __device__ __forceinline__

#define KIDP
#define KIDA(n)
#define TSP
#define TSA
#define TS_DECL
#define TS(i)
#define TS_FLUSH

constexpr int SL = 2048, SD = 512, SN = 128, SF = 1376;
constexpr int NKF = 43;
constexpr int NCW = 16;
constexpr int XS = SL + 64;
constexpr int KIMG_CH = 8448;
constexpr int KIMG_C1 = 4224;

constexpr size_t OFF_WGU = 0;
constexpr size_t OFF_WD = OFF_WGU + (size_t)NCW * 16 * 11 * 1024;
constexpr size_t OFF_CSP = OFF_WD + (size_t)8 * NKF * 4096;
constexpr size_t OFF_XB = OFF_CSP + (size_t)16 * 176 * 16 * 4;
constexpr size_t OFF_XC = OFF_XB + (size_t)XS * SD * 2;
constexpr size_t OFF_XD = OFF_XC + (size_t)XS * SD * 2;
constexpr size_t OFF_H = OFF_XD + (size_t)XS * SD * 2;
constexpr size_t OFF_H2 = OFF_H + (size_t)32 * NKF * 4096;
constexpr size_t OFF_KIMG = OFF_H2 + (size_t)32 * NKF * 4096;
constexpr size_t OFF_GRAN = OFF_KIMG + (size_t)SD * KIMG_CH;
constexpr int XG_ROW = SL * 8 + 256;
constexpr size_t OFF_XG = OFF_GRAN + (size_t)SL * 8;
constexpr size_t OFF_ST2 = OFF_XG + (size_t)256 * XG_ROW;
constexpr size_t OFF_CNT = OFF_ST2 + (size_t)SL * 16;
constexpr size_t OFF_EPOCH = OFF_CNT + 64 * 256;
constexpr size_t WS_USED = OFF_EPOCH + 256;
#define PACK_AUX 16

DEV float bflo(u32 v) { return __uint_as_float(v << 16); }
DEV float bfhi(u32 v) { return __uint_as_float(v & 0xffff0000u); }
DEV float bf2f(u16 v) { return __uint_as_float((u32)v << 16); }
DEV u32 pk_bf16(float a, float b) {
  f32x2 v = {a, b};
  return __builtin_bit_cast(u32, __builtin_convertvector(v, bf16x2));
}
DEV u32 pk_f16(float a, float b) {
  f32x2 v = {a, b};
  return __builtin_bit_cast(u32, __builtin_convertvector(v, f16x2));
}
DEV float bfr(float a) { return bflo(pk_bf16(a, a)); }
DEV float dot2bf(u32 a, u32 b, float c) {
  return __builtin_amdgcn_fdot2_f32_bf16(__builtin_bit_cast(bf16x2, a), __builtin_bit_cast(bf16x2, b), c, false);
}
DEV f32x4 mfma_bf16(u32x4 a, u32x4 b, f32x4 c) {
  return __builtin_amdgcn_mfma_f32_16x16x32_bf16(__builtin_bit_cast(bf16x8, a), __builtin_bit_cast(bf16x8, b), c, 0, 0, 0);
}
DEV f32x4 mfma_f16(u32x4 a, u32x4 b, f32x4 c) {
  return __builtin_amdgcn_mfma_f32_16x16x32_f16(__builtin_bit_cast(f16x8, a), __builtin_bit_cast(f16x8, b), c, 0, 0, 0);
}
DEV u32x2 lds_tr(const char* p) {
  return __builtin_bit_cast(u32x2, __builtin_amdgcn_ds_read_tr16_b64_v4i16((LAS s16x4*)p));
}
DEV u32x2 lds_tr32(u32 a) {
  return __builtin_bit_cast(u32x2, __builtin_amdgcn_ds_read_tr16_b64_v4i16((LAS s16x4*)a));
}
DEV u32x4 lds_rd128(u32 a) { return *(const LAS u32x4*)a; }
template <int CTRL>
DEV float dpp_f(float v) {
  return __builtin_bit_cast(float, __builtin_amdgcn_update_dpp(0, __builtin_bit_cast(int, v), CTRL, 0xf, 0xf, true));
}
DEV float sum8(float v) {
  v += dpp_f<0xB1>(v);
  v += dpp_f<0x4E>(v);
  v += dpp_f<0x141>(v);
  return v;
}
DEV float sum16(float v) { v = sum8(v); return v + dpp_f<0x140>(v); }
DEV void ln_consts(float sum, float sumsq, float& m, float& s) {
  float mean = sum * (1.0f / SD);
  float var = fmaxf(sumsq * (1.0f / SD) - mean * mean, 0.0f);
  m = bfr(mean);
  s = bfr(__builtin_amdgcn_sqrtf(bfr(bfr(var) + 1e-5f)));
}

typedef f32x2 cpx;
DEV cpx cmul(cpx a, cpx b) {
  const f32x2 t = a * f32x2{b[0], b[0]};
  return __builtin_elementwise_fma(f32x2{a[1], a[0]}, f32x2{-b[1], b[1]}, t);
}
DEV float exp_acc(float x) {
  const float t = x * 1.44269504088896340736f;
  const float e = fmaf(x, 1.44269504088896340736f, -t) + x * 1.92596299e-8f;
  const float r = __builtin_amdgcn_exp2f(t);
  return fmaf(r, e * 0.69314718056f, r);
}

typedef __attribute__((address_space(1))) u32 gu32;
typedef __attribute__((address_space(1))) unsigned long long gu64;
template <int N>
DEV void wait_vm() { asm volatile("s_waitcnt vmcnt(%0)" ::"n"(N) : "memory"); }
DEV void wait_lgkm0() { __builtin_amdgcn_s_waitcnt(0xC07F); }
DEV void bar() {
  asm volatile("" ::: "memory");
  __builtin_amdgcn_s_barrier();
  asm volatile("" ::: "memory");
}
DEV void lds_bar() { wait_lgkm0(); bar(); }
template <int I, int N, class Fn>
DEV void static_for(Fn&& f) {
  if constexpr (I < N) { f(std::integral_constant<int, I>{}); static_for<I + 1, N>(f); }
}
DEV int wgu_off(int c, int s, int t) { return (c * 176 + (t < 6 ? s * 6 + t : 96 + (t - 6) * 16 + s)) << 10; }
DEV int tile_of_oct(int oct) { return oct < 96 ? (oct / 6) * 11 + oct % 6 : ((oct - 96) / 5) * 11 + 6 + (oct - 96) % 5; }

constexpr int CV_U = 2 * KIMG_CH;
constexpr int CV_UROW = (128 + 1024) * 2;
constexpr int CV_P = CV_U + 4 * CV_UROW;
constexpr int CV_PROW = 2048 * 4 + 64 * 16 + 16;
constexpr int CONV_LDS = CV_P + 4 * CV_PROW;

DEV void conv_mfma(char* smem, int lane, int w) {
  const int np = lane & 15, h = lane >> 4, q = (lane & 15) >> 2, p = lane & 3;
  const u32 lds0 = (u32)(uintptr_t)smem;
  u32 ka = lds0 + (w >> 1) * KIMG_CH + ((q & 1) ? KIMG_C1 + 2 * 18 : 2 * 16) + 2 * (48 - 16 * h - 2 * q + 4 * p) - 16;
  u32 ua = lds0 + CV_U + w * CV_UROW + 256 + 2 * (8 * (np + h) - 24) - 192;
  asm volatile("" : "+v"(ka), "+v"(ua));
  f32x4 acc[8];
#pragma unroll
  for (int j = 0; j < 8; ++j) acc[j] = f32x4{0, 0, 0, 0};
  u32x4 kf[2][8], uf[2][8];
  auto rd = [&](auto D) {
    constexpr int dl = decltype(D)::value;
#pragma unroll
    for (int i = 0; i < 8; ++i) {
      const u32x2 lo = lds_tr32(ka + (4 * i + dl) * 128 + 16), hi = lds_tr32(ka + (4 * i + dl) * 128);
      kf[dl & 1][i] = u32x4{lo[0], lo[1], hi[0], hi[1]};
      uf[dl & 1][i] = lds_rd128(ua + i * 256 + (3 - dl) * 64);
    }
  };
  rd(std::integral_constant<int, 0>{});
  __builtin_amdgcn_sched_barrier(0);
  static_for<0, 4>([&](auto D) {
    constexpr int dl = decltype(D)::value;
    if constexpr (dl < 3) rd(std::integral_constant<int, dl + 1>{});
#pragma unroll
    for (int mx = 0; mx < 8; ++mx) {
#pragma unroll
      for (int o = 0; o <= mx; ++o)
        if (mx + o < 8) acc[mx + o] = mfma_bf16(kf[dl & 1][mx], uf[dl & 1][o], acc[mx + o]);
#pragma unroll
      for (int o = 0; o < mx; ++o)
        if (mx + o < 8) acc[mx + o] = mfma_bf16(kf[dl & 1][o], uf[dl & 1][mx], acc[mx + o]);
    }
    if constexpr (dl < 3) {
#pragma unroll
      for (int k = 0; k < 24; ++k) {
        __builtin_amdgcn_sched_group_barrier(0x008, 1, 0);
        __builtin_amdgcn_sched_group_barrier(0x100, 1, 0);
      }
      __builtin_amdgcn_sched_group_barrier(0x008, 12, 0);
    }
    __builtin_amdgcn_sched_barrier(0);
  });
  char* pp = smem + CV_P + w * CV_PROW + 16 + np * 64 + h * 16 + (np >> 1) * 16;
#pragma unroll
  for (int j = 0; j < 8; ++j) *(f32x4*)(pp + j * (1024 + 128)) = acc[j];
}

DEV void conv_body(char* smem, int tid, const float (&xf)[2][8], const float (&m)[8], const float (&sd)[8],
                   const float (&dsk)[2], u16* xout0 KIDP TSP) {
  const int lane = tid & 63, w = __builtin_amdgcn_readfirstlane(tid >> 6);
  float base[2][8];
#pragma unroll
  for (int c = 0; c < 2; ++c) {
    u32 up[2][2];
#pragma unroll
    for (int r = 0; r < 2; ++r)
#pragma unroll
      for (int j = 0; j < 2; ++j) {
        const int e0 = 4 * j + r, e1 = e0 + 2;
        const f32x2 rs = {__builtin_amdgcn_rcpf(sd[e0]), __builtin_amdgcn_rcpf(sd[e1])};
        const f32x2 xx = {xf[c][e0], xf[c][e1]};
        const f32x2 u = (xx - f32x2{m[e0], m[e1]}) * rs;
        up[r][j] = pk_bf16(u[0], u[1]);
        const f32x2 bs = __builtin_elementwise_fma(u, f32x2{dsk[c], dsk[c]}, xx);
        base[c][e0] = bs[0]; base[c][e1] = bs[1];
      }
#pragma unroll
    for (int r = 0; r < 2; ++r)
      *(u32x2*)(smem + CV_U + (c * 2 + r) * CV_UROW + 256 + tid * 8) = u32x2{up[r][0], up[r][1]};
  }
  if (tid < 64) *(u32x4*)(smem + CV_U + (tid >> 4) * CV_UROW + (tid & 15) * 16) = u32x4{0, 0, 0, 0};
  if (tid < 4) *(float*)(smem + CV_P + tid * CV_PROW + 12) = 0.f;
  lds_bar();
  TS(8)
  conv_mfma(smem, lane, w);
  TS(9)
  lds_bar();
  TS(10)
  const int off = CV_P + 16 + tid * 32 + (tid >> 2) * 16;
  const int offm = off + CV_PROW - (((tid & 3) == 0 && tid > 0) ? 20 : 4);
#pragma unroll
  for (int c = 0; c < 2; ++c) {
    const char* p0 = smem + off + (2 * c) * CV_PROW;
    const f32x4 a0 = *(const f32x4*)p0, a1 = *(const f32x4*)(p0 + 16);
    const f32x4 b0 = *(const f32x4*)(p0 + CV_PROW), b1 = *(const f32x4*)(p0 + CV_PROW + 16);
    const float bm = *(const float*)(smem + offm + (2 * c) * CV_PROW);
    const float o[8] = {base[c][0] + a0[0] + bm,    base[c][1] + a0[1] + b0[0], base[c][2] + a0[2] + b0[1],
                        base[c][3] + a0[3] + b0[2], base[c][4] + a1[0] + b0[3], base[c][5] + a1[1] + b1[0],
                        base[c][6] + a1[2] + b1[1], base[c][7] + a1[3] + b1[2]};
    const u32x4 ov = {pk_bf16(o[0], o[1]), pk_bf16(o[2], o[3]), pk_bf16(o[4], o[5]), pk_bf16(o[6], o[7])};
    *(u32x4*)(xout0 + (size_t)c * XS + 8 * tid) = ov;
  }
  TS(11)
}

DEV int chan_pair(int b) { return (b & 7) * 32 + (b >> 3); }

__global__ __launch_bounds__(320, 2) void k_conv(char* ws, const u16* __restrict__ dskip KIDP) {
  extern __shared__ __attribute__((aligned(16))) char smem[];
  TS_DECL
  const int tid = threadIdx.x, lane = tid & 63, w = __builtin_amdgcn_readfirstlane(tid >> 6);
  const int d0 = 2 * chan_pair(blockIdx.x);
  if (w == 4) {
    u32 vo = (u32)OFF_KIMG + d0 * KIMG_CH + lane * 16;
    const u32 lds0 = (u32)(uintptr_t)smem;
#pragma unroll
    for (int i = 0; i < 4; ++i)
      asm volatile("s_mov_b32 m0, %2\n\ts_nop 0\n\tglobal_load_lds_dwordx4 %0, %1\n\tglobal_load_lds_dwordx4 %0, %1 offset:1024\n\t"
                   "global_load_lds_dwordx4 %0, %1 offset:2048\n\tglobal_load_lds_dwordx4 %0, %1 offset:3072"
                   ::"v"(vo + i * 4096), "s"(ws), "s"(lds0 + i * 4096) : "memory");
    if (lane < 32)
      asm volatile("s_mov_b32 m0, %2\n\ts_nop 0\n\tglobal_load_lds_dwordx4 %0, %1"
                   ::"v"(vo + 16384), "s"(ws), "s"(lds0 + 16384) : "memory");
    wait_vm<0>();
    bar();
    return;
  }
  f64x2 sv[8];
#pragma unroll
  for (int e = 0; e < 8; ++e)
    sv[e] = ((const f64x2*)(ws + OFF_ST2))[e * 256 + tid];
  const u16* xin = (const u16*)(ws + OFF_XC) + (size_t)d0 * XS + 8 * tid;
  const u32x4 xr[2] = {*(const u32x4*)xin, *(const u32x4*)(xin + XS)};
  const float dsk[2] = {bf2f(dskip[d0]), bf2f(dskip[d0 + 1])};
  float m[8], sd[8];
#pragma unroll
  for (int e = 0; e < 8; e += 2) {
    float mean[2], var[2];
#pragma unroll
    for (int j = 0; j < 2; ++j) {
      const double mu = sv[e + j][0] * (1.0 / SD);
      mean[j] = (float)mu;
      var[j] = (float)fma(-mu, sv[e + j][0], sv[e + j][1]);
    }
    const u32 mp = pk_bf16(mean[0], mean[1]);
    m[e] = bflo(mp); m[e + 1] = bfhi(mp);
    const u32 vp = pk_bf16(fmaxf(var[0] * (1.0f / SD), 0.f), fmaxf(var[1] * (1.0f / SD), 0.f));
    const u32 ep = pk_bf16(bflo(vp) + 1e-5f, bfhi(vp) + 1e-5f);
    const u32 sp = pk_bf16(__builtin_amdgcn_sqrtf(bflo(ep)), __builtin_amdgcn_sqrtf(bfhi(ep)));
    sd[e] = bflo(sp); sd[e + 1] = bfhi(sp);
  }
  float xf[2][8];
#pragma unroll
  for (int c = 0; c < 2; ++c)
#pragma unroll
    for (int j = 0; j < 4; ++j) { xf[c][2 * j] = bflo(xr[c][j]); xf[c][2 * j + 1] = bfhi(xr[c][j]); }
  TS(1)
  conv_body(smem, tid, xf, m, sd, dsk, (u16*)(ws + OFF_XD) + (size_t)d0 * XS KIDA(kid_) TSA);
  TS_FLUSH
}

constexpr int KG_ROW = 528;
constexpr int KG_CH = 64 * KG_ROW;
constexpr int KG_TAB = 2 * KG_CH;
constexpr int KG_TROW = 672 * 2;
constexpr int KG_MISC = KG_TAB + 2 * KG_TROW;
constexpr int KG_PACK = KG_MISC + 64;
constexpr int PK_IMG = 8192;
constexpr int FIRST_LDS = KG_PACK + 3 * PK_IMG;
static_assert(CONV_LDS <= KG_TAB, "alias");
#define XG_AUX 0

DEV cpx cpow2(float a2, float rh, float rl, float s) {
  const float mag = __builtin_amdgcn_exp2f(a2 * s);
  const float rev = fmaf(rl, s, __builtin_amdgcn_fractf(rh * s));
  return f32x2{__builtin_amdgcn_cosf(rev), __builtin_amdgcn_sinf(rev)} * f32x2{mag, mag};
}

__global__ __launch_bounds__(512, 2) void k_first(
    const u16* __restrict__ x, const u16* __restrict__ logA, const u16* __restrict__ Aim, const u16* __restrict__ Cre,
    const u16* __restrict__ Cim, const u16* __restrict__ logdt, const u16* __restrict__ dskip,
    const u16* __restrict__ wg, const u16* __restrict__ wu, const u16* __restrict__ wd, char* ws KIDP) {
  extern __shared__ __attribute__((aligned(16))) char smem[];
  TS_DECL
  const int tid = threadIdx.x, lane = tid & 63, w = __builtin_amdgcn_readfirstlane(tid >> 6), b = blockIdx.x;
  const auto rsw = __builtin_amdgcn_make_buffer_rsrc(ws, 0, (int)WS_USED, 0x00020000);
  if (w < 4) {
    const int d0 = 2 * chan_pair(b);
    const int ch = w >> 1, n = tid & 127, d = d0 + ch;
    const u16 l_dt = logdt[d], l_a = logA[d * SN + n], l_b = Aim[d * SN + n];
    const u16 l_cr = Cre[d * SN + n], l_ci = Cim[d * SN + n];
    const u16 l_cr2 = Cre[d * SN + (n ^ 64)], l_ci2 = Cim[d * SN + (n ^ 64)];
    const float dsk[2] = {bf2f(dskip[d0]), bf2f(dskip[d0 + 1])};
    const u32 ea = pk_bf16(exp_acc(bf2f(l_dt)), exp_acc(bf2f(l_a)));
    const float dt = bflo(ea);
    const float a2 = -(bfhi(ea) * dt) * 1.44269504088896340736f;
    const float bi = bf2f(l_b) * dt;
    const float rh = bi * 0.15915494309189533577f;
    const float rl = fmaf(bi, 6.420638e-9f, fmaf(bi, 0.15915494309189533577f, -rh));
    TS(1)
    u32 mx = max(max((u32)l_cr & 0x7fffu, (u32)l_ci & 0x7fffu), max((u32)l_cr2 & 0x7fffu, (u32)l_ci2 & 0x7fffu));
    mx = max(mx, (u32)__builtin_amdgcn_update_dpp(0, (int)mx, 0xB1, 0xf, 0xf, true));
    mx = max(mx, (u32)__builtin_amdgcn_update_dpp(0, (int)mx, 0x4E, 0xf, 0xf, true));
    mx = max(mx, (u32)__builtin_amdgcn_update_dpp(0, (int)mx, 0x141, 0xf, 0xf, true));
    mx = max(mx, (u32)__builtin_amdgcn_update_dpp(0, (int)mx, 0x140, 0xf, 0xf, true));
    mx = max(max((u32)__builtin_amdgcn_readlane((int)mx, 0), (u32)__builtin_amdgcn_readlane((int)mx, 16)),
             max((u32)__builtin_amdgcn_readlane((int)mx, 32), (u32)__builtin_amdgcn_readlane((int)mx, 48)));
    const u32 ex = min(max(mx >> 7, 2u), 252u) + 1u;
    const float fs = 2.0f * dt * __uint_as_float(ex << 23), isc = __uint_as_float((254u - ex) << 23);
    cpx z[12];
#pragma unroll
    for (int p = 0; p < 12; p += 3) {
      z[p] = cpow2(a2, rh, rl, (float)(1 << p));
      z[p + 1] = cmul(z[p], z[p]);
      z[p + 2] = cmul(z[p + 1], z[p + 1]);
    }
    char* base = smem + ch * KG_CH + n * 4;
    {
      cpx X[32];
      X[0] = cpx{1.0f, 0.0f};
#pragma unroll
      for (int q = 0; q < 5; ++q) {
        X[1 << q] = z[3 + q];
#pragma unroll
        for (int j = 1; j < (1 << q); ++j) X[(1 << q) + j] = cmul(X[j], z[3 + q]);
      }
#pragma unroll
      for (int i = 0; i < 32; ++i) *(u32*)(base + i * KG_ROW) = pk_f16(X[i][0], X[i][1]);
      cpx Y[21];
      Y[0] = cpx{bf2f(l_cr) * isc, bf2f(l_ci) * isc};
      Y[1] = cmul(Y[0], z[0]); Y[2] = cmul(Y[0], z[1]); Y[3] = cmul(Y[2], z[0]); Y[4] = cmul(Y[0], z[2]);
      Y[5] = cmul(Y[4], z[0]); Y[6] = cmul(Y[4], z[1]); Y[7] = cmul(Y[6], z[0]);
      Y[8] = cmul(Y[0], z[8]); Y[9] = cmul(Y[2], z[8]); Y[10] = cmul(Y[4], z[8]); Y[11] = cmul(Y[6], z[8]);
      Y[12] = cmul(Y[0], z[9]); Y[13] = cmul(Y[4], z[9]); Y[14] = cmul(Y[8], z[9]); Y[15] = cmul(Y[10], z[9]);
      Y[16] = cmul(Y[0], z[10]); Y[17] = cmul(Y[8], z[10]); Y[18] = cmul(Y[12], z[10]); Y[19] = cmul(Y[14], z[10]);
      Y[20] = cmul(Y[0], z[11]);
#pragma unroll
      for (int c = 0; c < 21; ++c) *(u32*)(base + (32 + c) * KG_ROW) = pk_f16(Y[c][0], -Y[c][1]);
      if (n < 8) *(u32*)(smem + KG_TAB + ch * KG_TROW + n * 4) = 0u;
    }
    TS(2)
    lds_bar();
    TS(3)
    {
      const int xt = w & 1, r = lane & 15, g = lane >> 4;
      const char* pa = smem + ch * KG_CH + (16 * xt + r) * KG_ROW + g * 16;
      const char* pb = smem + ch * KG_CH + (32 + r) * KG_ROW + g * 16;
      const char* pc = smem + ch * KG_CH + (48 + min(r, 4)) * KG_ROW + g * 16;
      f32x4 acc0 = {0, 0, 0, 0}, acc1 = {0, 0, 0, 0};
#pragma unroll
      for (int s = 0; s < 8; ++s) {
        const u32x4 a = *(const u32x4*)(pa + s * 64);
        acc0 = mfma_f16(a, *(const u32x4*)(pb + s * 64), acc0);
        acc1 = mfma_f16(a, *(const u32x4*)(pc + s * 64), acc1);
      }
      u16* tab = (u16*)(smem + KG_TAB + ch * KG_TROW) + 16;
      const int ts = r < 8 ? 8 : (r < 12 ? 4 : 2);
      const int tb = r < 8 ? r : (r < 12 ? 248 + r : 384 + 64 * ((r >> 1) & 1) + (r & 1));
      const int i0 = 16 * xt + 4 * g;
      acc0 *= fs; acc1 *= fs;
      const u32 v01 = pk_bf16(acc0[0], acc0[1]), v23 = pk_bf16(acc0[2], acc0[3]);
      u16* t0 = tab + tb + ts * i0;
      t0[0] = (u16)v01; t0[ts] = (u16)(v01 >> 16); t0[2 * ts] = (u16)v23; t0[3 * ts] = (u16)(v23 >> 16);
      if (r < 4 || i0 == 0)
        *(u32x2*)(tab + 512 + 32 * min(r, 4) + i0) = u32x2{pk_bf16(acc1[0], acc1[1]), pk_bf16(acc1[2], acc1[3])};
    }
    const u32 goff = (u32)OFF_GRAN + tid * 16, xoff = (u32)OFF_XG + (d0 >> 1) * XG_ROW + tid * 64;
    u32x4 gr[4], xg[4];
#pragma unroll
    for (int j = 0; j < 4; ++j) gr[j] = __builtin_amdgcn_raw_buffer_load_b128(rsw, goff, j * 4096, 16);
#pragma unroll
    for (int j = 0; j < 4; ++j) xg[j] = __builtin_amdgcn_raw_buffer_load_b128(rsw, xoff + j * 16, 0, 16);
    TS(4)
    lds_bar();
    TS(5)
    {
      const int c = tid;
      const float f0 = (float)(8 * c);
      int ia[5], ib[5];
#pragma unroll
      for (int j = 0; j < 5; ++j) {
        const u32 bits = pk_bf16(f0 + (float)(2 * j - 2), f0 + (float)(2 * j - 1));
        const int lin = (8 * c + 2 * j - 2 + 16) * 2;
        ia[j] = c < 24 ? lin : (int)(bits & 0xffffu) * 2 - (0x4300 - 144) * 2;
        ib[j] = c < 24 ? lin + 2 : (int)(bits >> 16) * 2 - (0x4300 - 144) * 2;
      }
#pragma unroll
      for (int cc = 0; cc < 2; ++cc) {
        const char* tab = smem + KG_TAB + cc * KG_TROW;
        u32 kv[5];
#pragma unroll
        for (int j = 0; j < 5; ++j) kv[j] = (u32)*(const u16*)(tab + ia[j]) | ((u32)*(const u16*)(tab + ib[j]) << 16);
        const u32x4 c0 = {kv[1], kv[2], kv[3], kv[4]}, c1 = {kv[0], kv[1], kv[2], kv[3]};
        const int o0 = cc * KIMG_CH + 32 + c * 16;
        *(u32x4*)(smem + o0) = c0;
        *(u32x4*)(smem + o0 + KIMG_C1) = c1;
      }
      if (c < 8) {
        const int oz = (c >> 2) * KIMG_CH + ((c >> 1) & 1) * KIMG_C1 + (c & 1) * 16;
        *(u32x4*)(smem + oz) = u32x4{0, 0, 0, 0};
      }
    }
    TS(6)
    const u32 tag = *(const u32*)(smem + KG_MISC);
    for (int sp = 0; sp < (1 << 18); ++sp) {
      u32 bad = 0;
#pragma unroll
      for (int j = 0; j < 4; ++j) bad |= ((gr[j][1] ^ tag) | (gr[j][3] ^ tag)) | ((xg[j][1] ^ tag) | (xg[j][3] ^ tag));
      if (__all(bad == 0u)) break;
      __builtin_amdgcn_s_sleep(2);
#pragma unroll
      for (int j = 0; j < 4; ++j) gr[j] = __builtin_amdgcn_raw_buffer_load_b128(rsw, goff, j * 4096, 16);
#pragma unroll
      for (int j = 0; j < 4; ++j) xg[j] = __builtin_amdgcn_raw_buffer_load_b128(rsw, xoff + j * 16, 0, 16);
    }
    TS(7)
    float m[8], sd[8], xf[2][8];
#pragma unroll
    for (int e = 0; e < 8; ++e) {
      const u32 gv = gr[e >> 1][(e & 1) * 2], xv = xg[e >> 1][(e & 1) * 2];
      m[e] = bflo(gv); sd[e] = bfhi(gv);
      xf[0][e] = bflo(xv); xf[1][e] = bfhi(xv);
    }
    conv_body(smem, tid, xf, m, sd, dsk, (u16*)(ws + OFF_XB) + (size_t)d0 * XS KIDA(kid_) TSA);
    if (b == 0 && tid == 0) __hip_atomic_store((gu32*)(ws + OFF_EPOCH), tag, __ATOMIC_RELAXED, __HIP_MEMORY_SCOPE_AGENT);
    TS_FLUSH
  } else {
    const u32 tag = __hip_atomic_load((gu32*)(ws + OFF_EPOCH), __ATOMIC_RELAXED, __HIP_MEMORY_SCOPE_AGENT) + 1u;
    const int kr = lane >> 3, no = lane & 7;
    u32x4 v[8];
    if (w <= 5) {
      const u16* src = w == 4 ? x + (size_t)(8 * b + kr) * SD + no * 8
                              : x + (size_t)(64 * (b >> 3) + kr) * SD + (b & 7) * 64 + no * 8;
      const int stp = w == 4 ? 64 : 8 * SD;
#pragma unroll
      for (int i = 0; i < 8; ++i) v[i] = *(const u32x4*)(src + i * stp);
    }
    const int id = (7 - w) * 256 + b;
    const bool gu = id < 352, pk = w >= 5 && id < 352 + 8 * NKF;
    const int ps = id & 15, ng = id >> 4, ct = (id - 352) & 7, kp = (id - 352) >> 3;
    const bool ok = 8 * ng + no < 172;
    u32x4 gv[4], uv[4];
    if (gu) {
      const size_t so = (size_t)(32 * ps + kr) * SF + 8 * (ok ? 8 * ng + no : 0);
#pragma unroll
      for (int i = 0; i < 4; ++i) {
        gv[i] = *(const u32x4*)(wg + so + (size_t)i * 8 * SF);
        uv[i] = *(const u32x4*)(wu + so + (size_t)i * 8 * SF);
      }
    } else if (pk) {
#pragma unroll
      for (int i = 0; i < 4; ++i)
        gv[i] = *(const u32x4*)(wd + (size_t)(8 * tile_of_oct(4 * kp + i) + kr) * SD + ct * 64 + no * 8);
    }
    char* img = smem + KG_PACK + (w - 5) * PK_IMG;
    if (w == 4) {
      float a[2] = {0.f, 0.f}, q[2] = {0.f, 0.f};
#pragma unroll
      for (int i = 0; i < 8; ++i)
#pragma unroll
        for (int j = 0; j < 4; ++j) {
          a[j & 1] = dot2bf(v[i][j], 0x3f803f80u, a[j & 1]);
          q[j & 1] = dot2bf(v[i][j], v[i][j], q[j & 1]);
        }
      float m, sd;
      ln_consts(sum8(a[0] + a[1]), sum8(q[0] + q[1]), m, sd);
      const u32 val = (__float_as_uint(m) >> 16) | (__float_as_uint(sd) & 0xffff0000u);
      for (int sp = 0; sp < (1 << 20) && *(volatile u32*)(smem + KG_MISC + 4) != tag; ++sp) __builtin_amdgcn_s_sleep(1);
      if (no == 0)
        __hip_atomic_store((gu64*)(ws + OFF_GRAN) + (lane >> 4) * 512 + 2 * b + (kr & 1),
                           ((unsigned long long)tag << 32) | val, __ATOMIC_RELAXED, __HIP_MEMORY_SCOPE_AGENT);
      if (lane == 0) *(u32*)(smem + KG_MISC) = tag;
      if (lane < 8) ((f64x2*)(ws + OFF_ST2))[b * 8 + lane] = f64x2{0.0, 0.0};
      if (b == 0) { ((u32*)(ws + OFF_CNT))[lane * 64] = 0u; ((u32*)(ws + OFF_CNT))[lane * 64 + 32] = 0u; }
    } else if (w == 5) {
      if (lane == 0) *(volatile u32*)(smem + KG_MISC + 4) = tag;
#pragma unroll
      for (int i = 0; i < 8; ++i)
        *(u32x4*)(img + (8 * i + kr) * 128 + ((no ^ ((4 * i + (kr >> 1)) & 7)) * 16)) = v[i];
      wait_lgkm0();
      asm volatile("" ::: "memory");
      const int pq = kr & 3, hi = lane >> 5;
      const char* rp = img + (no + 8 * hi) * 256 + pq * 4;
      const u32 go = (u32)OFF_XG + ((b & 7) * 32 + pq) * XG_ROW + (b >> 3) * 512 + (no + 8 * hi) * 16;
#pragma unroll
      for (int i = 0; i < 16; ++i) {
        const char* a = rp + (((i & 7) ^ no) * 16) + (i >> 3) * 4096;
        const u32 r0 = *(const u32*)a, r1 = *(const u32*)(a + 128);
        __builtin_amdgcn_raw_buffer_store_b128(u32x4{r0, tag, r1, tag}, rsw, go + (i & 7) * 4 * XG_ROW + (i >> 3) * 256, 0, 16);
      }
    }
    TS(1)
    lds_bar();
    bar();
    bar();
    TS(2)
    if (w == 4) {
      const u32 go = (u32)OFF_KIMG + 2 * chan_pair(b) * KIMG_CH + lane * 16;
#pragma unroll
      for (int i = 0; i < 17; ++i)
        if (i < 16 || lane < 32)
          __builtin_amdgcn_raw_buffer_store_b128(*(const u32x4*)(smem + i * 1024 + lane * 16), rsw, go + i * 1024, 0, PACK_AUX);
    } else {
      const int col = lane & 15, g = lane >> 4, q = (lane & 15) >> 2, p = lane & 3;
      const u32 ra = (u32)(uintptr_t)img + (8 * g + q) * 256 + 8 * p;
      const int rkey = q | ((g & 1) << 2);
      if (gu) {
#pragma unroll
        for (int i = 0; i < 4; ++i) {
          if (!ok) { gv[i] = u32x4{0, 0, 0, 0}; uv[i] = u32x4{0, 0, 0, 0}; }
          char* dst = img + (8 * i + kr) * 256 + ((no ^ ((kr & 3) | ((i & 1) << 2))) * 32);
          *(u32x4*)dst = u32x4{gv[i][0], uv[i][0], gv[i][1], uv[i][1]};
          *(u32x4*)(dst + 16) = u32x4{gv[i][2], uv[i][2], gv[i][3], uv[i][3]};
        }
        wait_lgkm0();
        asm volatile("" ::: "memory");
        f32x4 cs = {0, 0, 0, 0};
#pragma unroll
        for (int t8 = 0; t8 < 8; ++t8) {
          const u32x2 lo = lds_tr32(ra + ((t8 ^ rkey) * 32)), hi = lds_tr32(ra + ((t8 ^ rkey) * 32) + 1024);
          const u32x4 fr = {lo[0], lo[1], hi[0], hi[1]};
          const int Tt = 8 * ng + t8;
          __builtin_amdgcn_raw_buffer_store_b128(fr, rsw, lane * 16 + (u32)OFF_WGU + wgu_off(Tt / 11, ps, Tt % 11), 0, PACK_AUX);
          const u32 sel = col == t8 ? 0x3f803f80u : 0u;
          cs = mfma_bf16(fr, u32x4{sel, sel, sel, sel}, cs);
        }
        if (col < 8) *(f32x4*)(ws + OFF_CSP + ((ps * 176 + 8 * ng + col) * 16 + g * 4) * 4) = cs;
      } else if (pk) {
#pragma unroll
        for (int i = 0; i < 4; ++i)
          *(u32x4*)(img + (8 * i + kr) * 256 + (((no >> 1) ^ ((kr & 3) | ((i & 1) << 2))) * 32) + (no & 1) * 16) = gv[i];
        wait_lgkm0();
        asm volatile("" ::: "memory");
#pragma unroll
        for (int nt = 0; nt < 4; ++nt) {
          const u32x2 lo = lds_tr32(ra + ((nt ^ rkey) * 32)), hi = lds_tr32(ra + ((nt ^ rkey) * 32) + 1024);
          __builtin_amdgcn_raw_buffer_store_b128(u32x4{lo[0], lo[1], hi[0], hi[1]}, rsw,
                                                 lane * 16 + (u32)OFF_WD + ((ct * NKF + kp) * 4 + nt) * 1024, 0, PACK_AUX);
        }
      }
    }
    TS(3)
    TS_FLUSH
  }
}

constexpr int F1_SLOT = 28672;
constexpr int F_E = 5 * F1_SLOT;
constexpr int F_CS = F_E + 16384;
constexpr int F_LDS = F_CS + 176 * 4;
constexpr int f_tile(int t) { return t == 6 ? F_E : t == 10 ? 0 : 3 * F1_SLOT + (t - 7) * 16384; }
constexpr int f_wd(int u) {
  return u == 0 ? 16384 : u == 1 ? 3 * F1_SLOT + 49152 : u < 9 ? 24576 + (u - 2) * 8192
       : u < 11 ? F_E + (u - 9) * 8192 : u < 17 ? 3 * F1_SLOT + (u - 11) * 8192
       : u < 19 ? (u - 17) * 8192 : f_wd(u - 19);
}
constexpr int F_RES = f_wd(4);

DEV void gload16_sc1(u32x4& dst, u32 voff, const char* base) {
  asm volatile("global_load_dwordx4 %0, %1, %2 sc1" : "=v"(dst) : "v"(voff), "s"(base) : "memory");
}
DEV void dma1(u32 voff, const char* base, u32 lds) {
  asm volatile("s_mov_b32 m0, %2\n\ts_nop 0\n\tglobal_load_lds_dwordx4 %0, %1" ::"v"(voff), "s"(base), "s"(lds) : "memory");
}
DEV void dma2(u32 voff, const char* base, u32 lds) {
  asm volatile("s_mov_b32 m0, %2\n\ts_nop 0\n\tglobal_load_lds_dwordx4 %0, %1\n\tglobal_load_lds_dwordx4 %0, %1 offset:1024"
               ::"v"(voff), "s"(base), "s"(lds) : "memory");
}
DEV float s_fma(float a, float b, float c) { float d; asm("v_fma_f32 %0, %1, %2, %3" : "=v"(d) : "v"(a), "v"(b), "v"(c)); return d; }
DEV float s_nfma(float a, float b, float c) { float d; asm("v_fma_f32 %0, -%1, %2, %3" : "=v"(d) : "v"(a), "v"(b), "v"(c)); return d; }
DEV float s_mul(float a, float b) { float d; asm("v_mul_f32 %0, %1, %2" : "=v"(d) : "v"(a), "v"(b)); return d; }
DEV u32 glu_tile(f32x4 acc, f32x4 cs, float m, float s, float c1) {
  const f32x2 nm = {-m, -m};
  const f32x2 gg = __builtin_elementwise_fma(nm, f32x2{cs[0], cs[1]}, f32x2{acc[0], acc[1]}) * f32x2{c1, c1};
  const f32x2 uu = __builtin_elementwise_fma(nm, f32x2{cs[2], cs[3]}, f32x2{acc[2], acc[3]});
  const f32x2 ex = {__builtin_amdgcn_exp2f(gg[0]), __builtin_amdgcn_exp2f(gg[1])};
  const f32x2 dn = __builtin_elementwise_fma(ex, f32x2{s, s}, f32x2{s, s});
  const f32x2 hh = uu * f32x2{__builtin_amdgcn_rcpf(dn[0]), __builtin_amdgcn_rcpf(dn[1])};
  return pk_bf16(hh[0], hh[1]);
}
DEV u32x4 tr4(u32 r0, u32 r1, u32 r2, u32 r3) {
  auto a = __builtin_amdgcn_permlane32_swap(r0, r2, false, false);
  auto b = __builtin_amdgcn_permlane32_swap(r1, r3, false, false);
  auto c = __builtin_amdgcn_permlane16_swap(a[0], b[0], false, false);
  auto d = __builtin_amdgcn_permlane16_swap(a[1], b[1], false, false);
  return u32x4{c[0], c[1], d[0], d[1]};
}

#define F_DEPTH 2
#define F_WIN 16
#define F_WIN0 8
#define F_UB 16

template <bool FINAL>
DEV void ffn_compute(char* smem, char* ws, int lane, int mq, int rb, int c, int rb64, int ct, u32 xin, u32 hoff,
                     u16* xout, double* st KIDP) {
  TS_DECL
  const int col = lane & 15, g = lane >> 4, q = (lane & 15) >> 2, p = lane & 3;
  const u32 lds0 = (u32)(uintptr_t)smem;
  const int key = q | ((g & 1) << 2);
  u32 xa[3][2], wa[3];
#pragma unroll
  for (int r = 0; r < 3; ++r) {
#pragma unroll
    for (int j = 0; j < 2; ++j) {
      xa[r][j] = lds0 + r * 2 * F1_SLOT + (8 * g + q) * 256 + (((2 * mq + j) ^ key) * 32) + 8 * p;
      asm volatile("" : "+v"(xa[r][j]));
    }
    wa[r] = lds0 + r * 2 * F1_SLOT + lane * 16;
    asm volatile("" : "+v"(wa[r]));
  }
  f32x4 acc[11][2];
#pragma unroll
  for (int t = 0; t < 11; ++t)
#pragma unroll
    for (int j = 0; j < 2; ++j) acc[t][j] = f32x4{0, 0, 0, 0};
  f32x4 s1[2] = {{0, 0, 0, 0}, {0, 0, 0, 0}}, s2[2] = {{0, 0, 0, 0}, {0, 0, 0, 0}};
  const u32x4 ones = {0x3f803f80u, 0x3f803f80u, 0x3f803f80u, 0x3f803f80u};
  u32x4 xf[16][2], wf[2][6];
  auto rd = [&](auto K) {
    constexpr int k = decltype(K)::value, sl = (k >> 1) % 5, r = sl >> 1, o = (sl & 1) * F1_SLOT;
#pragma unroll
    for (int j = 0; j < 2; ++j) {
      const u32x2 lo = lds_tr32(xa[r][j] + o + (k & 1) * 8192), hi = lds_tr32(xa[r][j] + o + (k & 1) * 8192 + 1024);
      xf[k][j] = u32x4{lo[0], lo[1], hi[0], hi[1]};
    }
#pragma unroll
    for (int t = 0; t < 6; ++t) wf[k & 1][t] = lds_rd128(wa[r] + o + 16384 + (k & 1) * 6144 + t * 1024);
  };
  auto mm = [&](auto K) {
    constexpr int k = decltype(K)::value;
#pragma unroll
    for (int j = 0; j < 2; ++j) {
      s2[j] = mfma_bf16(xf[k][j], xf[k][j], s2[j]);
      s1[j] = mfma_bf16(ones, xf[k][j], s1[j]);
    }
#pragma unroll
    for (int t = 0; t < 6; ++t)
#pragma unroll
      for (int j = 0; j < 2; ++j) acc[t][j] = mfma_bf16(wf[k & 1][t], xf[k][j], acc[t][j]);
  };
  auto weave = [&]() {
#pragma unroll
    for (int k = 0; k < 10; ++k) {
      __builtin_amdgcn_sched_group_barrier(0x008, 1, 0);
      __builtin_amdgcn_sched_group_barrier(0x100, 1, 0);
    }
    __builtin_amdgcn_sched_group_barrier(0x008, 6, 0);
    __builtin_amdgcn_sched_barrier(0);
  };
  static_for<0, 8>([&](auto I) {
    constexpr int i = decltype(I)::value;
    wait_lgkm0();
    __builtin_amdgcn_sched_barrier(0);
    bar();
    if constexpr (i == 0) { TS(1) }
    rd(std::integral_constant<int, 2 * i>{});
    if constexpr (i > 0) mm(std::integral_constant<int, 2 * i - 1>{});
    weave();
    rd(std::integral_constant<int, 2 * i + 1>{});
    mm(std::integral_constant<int, 2 * i>{});
    weave();
  });
  wait_lgkm0();
  __builtin_amdgcn_sched_barrier(0);
  bar();
  TS(2)
  mm(std::integral_constant<int, 15>{});
  __builtin_amdgcn_sched_barrier(0);
  float lm[2], ls[2], lc[2];
  auto consts = [&](int j) {
    const float d01 = (p & 1) ? s2[j][1] : s2[j][0], d23 = (p & 1) ? s2[j][3] : s2[j][2];
    u32 dv = __float_as_uint((g == q) ? ((p & 2) ? d23 : d01) : 0.f);
    auto x32 = __builtin_amdgcn_permlane32_swap(dv, dv, false, false);
    dv = x32[0] | x32[1];
    auto x16 = __builtin_amdgcn_permlane16_swap(dv, dv, false, false);
    dv = x16[0] | x16[1];
    ln_consts(s1[j][0], __uint_as_float(dv), lm[j], ls[j]);
    lc[j] = -1.44269504088896340736f * __builtin_amdgcn_rcpf(ls[j]);
  };
  const auto rs = __builtin_amdgcn_make_buffer_rsrc(ws, 0, (int)WS_USED, 0x00020000);
  const int lt = rb * 8 + 2 * mq + (g & 1);
  const u32 hb = hoff + (((lt >> 2) * NKF * 4 + (lt & 3)) << 10) + col * 16;
  const u32 csa = lds0 + F_CS + g * 16;
  u32 hv[12][2];
  hv[11][0] = 0; hv[11][1] = 0;
  auto epi = [&](int t) {
    const f32x4 cs = __builtin_bit_cast(f32x4, lds_rd128(csa + t * 64));
#pragma unroll
    for (int j = 0; j < 2; ++j) hv[t][j] = glu_tile(acc[t][j], cs, lm[j], ls[j], lc[j]);
  };
  auto hstore = [&](int t) {
    const int oct = (t < 6 ? c * 6 + t : 96 + c * 5 + (t - 6)) + (g >> 1);
    const bool ok = t < 6 || (oct < 172 && (t < 10 || g < 2));
    const u32 off = ok ? hb + (oct >> 2) * 4096 + (oct & 3) * 256 : 0x7ffffff0u;
    __builtin_amdgcn_raw_buffer_store_b128(tr4(hv[t][0], hv[t][1], hv[t + 1][0], hv[t + 1][1]), rs, off, 0, 16);
  };
  __builtin_amdgcn_sched_barrier(0);
  TS(3)
  static_for<6, 11>([&](auto T) {
    constexpr int t = decltype(T)::value, base = f_tile(t), r = base / (2 * F1_SLOT), o = base % (2 * F1_SLOT);
    constexpr int e0 = t == 6 ? 0 : t == 7 ? 3 : t == 8 ? 6 : t - 1, ne = t < 8 ? 3 : t == 8 ? 2 : 1;
    u32x4 wv[16];
    f32x4 cs[3];
    f32x2 ga[6], ua[6];
#pragma unroll
    for (int k = 0; k < 4; ++k) wv[k] = lds_rd128(wa[r] + o + k * 1024);
#pragma unroll
    for (int e = 0; e < ne; ++e) cs[e] = __builtin_bit_cast(f32x4, lds_rd128(csa + (e0 + e) * 64));
    __builtin_amdgcn_sched_barrier(0);
    static_for<0, 32>([&](auto S) {
      constexpr int sg = decltype(S)::value, k = sg >> 1, j = sg & 1;
      if constexpr ((sg & 7) == 0) { wait_lgkm0(); __builtin_amdgcn_sched_barrier(0); }
      acc[t][j] = mfma_bf16(wv[k], xf[k][j], acc[t][j]);
      if constexpr ((sg & 7) < 4 && (sg >> 3) < 3) wv[4 + (sg >> 3) * 4 + (sg & 3)] = lds_rd128(wa[r] + o + (4 + (sg >> 3) * 4 + (sg & 3)) * 1024);
      constexpr int it = sg - (t == 6 ? 4 : 0), pr = it / 8, st = it % 8;
      if constexpr (it >= 0 && pr < ne && st < 6) {
        const f32x4 cv = cs[pr];
#pragma unroll
        for (int jj = 0; jj < 2; ++jj) {
          constexpr int a0 = 2 * pr;
          const int a = a0 + jj;
          const f32x4 av = acc[e0 + pr][jj];
          if constexpr (st == 0) {
            const f32x2 nm = {-lm[jj], -lm[jj]};
            ga[a] = __builtin_elementwise_fma(nm, f32x2{cv[0], cv[1]}, f32x2{av[0], av[1]});
            ua[a] = __builtin_elementwise_fma(nm, f32x2{cv[2], cv[3]}, f32x2{av[2], av[3]});
          } else if constexpr (st == 1) {
            ga[a] = ga[a] * f32x2{lc[jj], lc[jj]};
          } else if constexpr (st == 2) {
            ga[a] = f32x2{__builtin_amdgcn_exp2f(ga[a][0]), __builtin_amdgcn_exp2f(ga[a][1])};
          } else if constexpr (st == 3) {
            ga[a] = __builtin_elementwise_fma(ga[a], f32x2{ls[jj], ls[jj]}, f32x2{ls[jj], ls[jj]});
          } else if constexpr (st == 4) {
            ga[a] = f32x2{__builtin_amdgcn_rcpf(ga[a][0]), __builtin_amdgcn_rcpf(ga[a][1])};
          } else {
            ua[a] = ua[a] * ga[a];
          }
        }
      }
      if constexpr (it >= 0 && pr < ne && st == 6) {
#pragma unroll
        for (int jj = 0; jj < 2; ++jj) hv[e0 + pr][jj] = pk_bf16(ua[2 * pr + jj][0], ua[2 * pr + jj][1]);
      }
      if constexpr (t == 6 && sg == 0) consts(0);
      if constexpr (t == 6 && sg == 2) consts(1);
      if constexpr (sg == 28) {
        if constexpr (t == 6) hstore(0);
        if constexpr (t == 7) hstore(2);
        if constexpr (t == 8) hstore(6);
        if constexpr (t == 10) hstore(8);
      }
      if constexpr (sg == 30 && t == 7) hstore(4);
      __builtin_amdgcn_sched_barrier(0);
    });
    if constexpr (t == 6) { TS(11) }
    if constexpr (t == 7) { TS(13) }
    wait_lgkm0();
    if constexpr (t == 8) wait_vm<1>();
    bar();
    __builtin_amdgcn_sched_barrier(0);
    if constexpr (t == 6) { TS(12) }
    if constexpr (t == 7) { TS(4) }
  });
  TS(5)
  constexpr int WIN = F_WIN;
  const u32 ho = hoff + (rb64 * NKF * 4 + mq) * 1024 + lane * 16;
  u32x4 hf[NKF];
  epi(10);
  hstore(10);
  __builtin_amdgcn_sched_barrier(0);
#pragma unroll
  for (int u = 0; u < F_WIN0; ++u) gload16_sc1(hf[u], ho, ws + u * 4096);
  TS(6)
  const int l0 = rb64 * 64 + mq * 16, d0 = ct * 64;
  f32x4 ac2[4] = {{0, 0, 0, 0}, {0, 0, 0, 0}, {0, 0, 0, 0}, {0, 0, 0, 0}};
  u32x4 wf2[2][4];
  auto rd2 = [&](auto U) {
    constexpr int u = decltype(U)::value, base = f_wd(u >> 1) + (u & 1) * 4096, r = base / (2 * F1_SLOT), o = base % (2 * F1_SLOT);
#pragma unroll
    for (int i = 0; i < 4; ++i) wf2[u & 1][i] = lds_rd128(wa[r] + o + i * 1024);
  };
  rd2(std::integral_constant<int, 0>{});
  __builtin_amdgcn_sched_barrier(0);
  u32x2 rv[4];
  u16 rf[4][4];
  constexpr auto issued = [](int u) {
    int n = u == 0 ? F_WIN0 : u + WIN;
    if (u <= F_UB && n > 24) n = 24;
    return n > NKF ? NKF : n;
  };
  static_for<0, NKF>([&](auto U) {
    constexpr int u = decltype(U)::value;
    asm volatile("s_waitcnt vmcnt(%1)" : "+v"(hf[u]) : "n"(issued(u) - 1 - u) : "memory");
    if constexpr (u == 0) { bar(); TS(7) }
    if constexpr (u == F_UB) { wait_lgkm0(); bar(); TS(8) }
    if constexpr (u == 37) bar();
    __builtin_amdgcn_sched_barrier(0);
#pragma unroll
    for (int v = issued(u); v < issued(u + 1); ++v) gload16_sc1(hf[v], ho, ws + v * 4096);
    if constexpr (u + 1 < NKF) rd2(std::integral_constant<int, u + 1>{});
#pragma unroll
    for (int i = 0; i < 4; ++i)
      ac2[i] = FINAL ? mfma_bf16(wf2[u & 1][i], hf[u], ac2[i]) : mfma_bf16(hf[u], wf2[u & 1][i], ac2[i]);
#pragma unroll
    for (int k = 0; k < 4; ++k) {
      __builtin_amdgcn_sched_group_barrier(0x008, 1, 0);
      __builtin_amdgcn_sched_group_barrier(0x100, 1, 0);
    }
    __builtin_amdgcn_sched_barrier(0);
  });
  TS(9)
  const char* rp = smem + F_RES;
  if (!FINAL) {
    f32x4 sa = {0, 0, 0, 0}, sq = {0, 0, 0, 0};
#pragma unroll
    for (int i = 0; i < 4; ++i) {
      rv[i] = *(const u32x2*)(rp + (i * 16 + col) * 144 + mq * 32 + g * 8);
      const u32 o0 = pk_bf16(ac2[i][0], ac2[i][1]), o1 = pk_bf16(ac2[i][2], ac2[i][3]);
      const u32 p0 = pk_bf16(bflo(rv[i][0]) + bflo(o0), bfhi(rv[i][0]) + bfhi(o0));
      const u32 p1 = pk_bf16(bflo(rv[i][1]) + bflo(o1), bfhi(rv[i][1]) + bfhi(o1));
      *(u32x2*)(xout + (size_t)(d0 + i * 16 + col) * XS + l0 + 4 * g) = u32x2{p0, p1};
      const f32x4 v = {bflo(p0), bfhi(p0), bflo(p1), bfhi(p1)};
      sa += v; sq += v * v;
    }
    float r8[8];
#pragma unroll
    for (int e = 0; e < 4; ++e) { r8[e] = sum16(sa[e]); r8[4 + e] = sum16(sq[e]); }
    float v = r8[0];
#pragma unroll
    for (int e = 1; e < 8; ++e) v = (col == e) ? r8[e] : v;
    if (col < 8) {
      const int l = l0 + 4 * g + (col & 3);
      unsafeAtomicAdd(st + ((l & 7) * 256 + (l >> 3)) * 2 + (col >> 2), (double)v);
    }
  } else {
#pragma unroll
    for (int i = 0; i < 4; ++i) {
#pragma unroll
      for (int e = 0; e < 4; ++e) rf[i][e] = *(const u16*)(rp + (i * 16 + 4 * g + e) * 144 + mq * 32 + col * 2);
      const u32 o0 = pk_bf16(ac2[i][0], ac2[i][1]), o1 = pk_bf16(ac2[i][2], ac2[i][3]);
      const u32 p0 = pk_bf16(bf2f(rf[i][0]) + bflo(o0), bf2f(rf[i][1]) + bfhi(o0));
      const u32 p1 = pk_bf16(bf2f(rf[i][2]) + bflo(o1), bf2f(rf[i][3]) + bfhi(o1));
      *(u32x2*)(xout + (size_t)(l0 + col) * SD + d0 + i * 16 + 4 * g) = u32x2{p0, p1};
    }
  }
  TS(10) TS_FLUSH
}

DEV void poll_ge(gu32* cp, u32 want) {
  for (int sp = 0; sp < (1 << 20); ++sp) {
    if (__hip_atomic_load(cp, __ATOMIC_RELAXED, __HIP_MEMORY_SCOPE_AGENT) >= want) break;
    __builtin_amdgcn_s_sleep(1);
  }
}

DEV void ffn_loader(char* smem, char* ws, int lane, int lw, int rb, int c, int rb64, int ct, u32 xin, u32 cnt KIDP) {
  TS_DECL
  const u32 lds0 = (u32)(uintptr_t)smem;
  const u32 wbase = (u32)OFF_WGU + c * (176 * 1024) + lane * 16;
  u32 vo[2];
#pragma unroll
  for (int o = 0; o < 2; ++o) {
    const int seg = ((lane & 15) >> 1) ^ ((lane >> 4) | (o << 2));
    vo[o] = xin + rb * 256 + (lw * 16 + o * 8 + (lane >> 4)) * (XS * 2) + seg * 32 + (lane & 1) * 16;
  }
  const u32 vw = wbase + lw * 3072;
  auto stage = [&](int s) {
    const u32 slot = lds0 + (s % 5) * F1_SLOT;
    const char* sb = ws + (size_t)s * (64 * XS * 2);
    asm volatile(
        "s_mov_b32 m0, %4\n\ts_nop 0\n\tglobal_load_lds_dwordx4 %0, %2\n\tglobal_load_lds_dwordx4 %0, %3 offset:1024\n\t"
        "s_add_u32 m0, m0, 0x800\n\ts_nop 0\n\tglobal_load_lds_dwordx4 %1, %2\n\tglobal_load_lds_dwordx4 %1, %3 offset:1024\n\t"
        "s_mov_b32 m0, %7\n\ts_nop 0\n\tglobal_load_lds_dwordx4 %5, %6\n\tglobal_load_lds_dwordx4 %5, %6 offset:1024\n\t"
        "global_load_lds_dwordx4 %5, %6 offset:2048"
        ::"v"(vo[0]), "v"(vo[1]), "s"(sb), "s"(sb + (4 * XS * 2 - 1024)), "s"(slot + lw * 4096), "v"(vw),
        "s"(ws + s * 12288), "s"(slot + 16384 + lw * 3072) : "memory", "scc");
  };
#pragma unroll
  for (int s = 0; s < F_DEPTH; ++s) stage(s);
  TS(1)
  u32x4 cq[4];
  const int f4a = 11 * lw + (lane >> 3), f4b = min(f4a + 8, 43);
  {
    const u32 co = (u32)OFF_CSP + ((lane & 7) * 176 + c * 11) * 64;
    asm volatile("global_load_dwordx4 %0, %4, %6\n\tglobal_load_dwordx4 %1, %4, %7\n\t"
                 "global_load_dwordx4 %2, %5, %6\n\tglobal_load_dwordx4 %3, %5, %7"
                 : "=&v"(cq[0]), "=&v"(cq[1]), "=&v"(cq[2]), "=&v"(cq[3])
                 : "v"(co + f4a * 16), "v"(co + f4b * 16), "s"(ws), "s"(ws + 8 * 176 * 64) : "memory");
  }
  auto tile = [&](int t) {
    asm volatile("s_mov_b32 m0, %2\n\ts_nop 0\n\tglobal_load_lds_dwordx4 %0, %1\n\tglobal_load_lds_dwordx4 %0, %1 offset:1024\n\t"
                 "global_load_lds_dwordx4 %0, %1 offset:2048\n\tglobal_load_lds_dwordx4 %0, %1 offset:3072"
                 ::"v"(wbase + lw * 4096), "s"(ws + (96 + (t - 6) * 16) * 1024), "s"(lds0 + f_tile(t) + lw * 4096) : "memory");
  };
  const u32 wdo = (u32)OFF_WD + (ct * NKF * 4 + lw * 2) * 1024 + lane * 16;
  auto wdp = [&](int u) { dma2(wdo, ws + u * 8192, lds0 + f_wd(u) + lw * 2048); };
  constexpr auto grp = [](int i) { return i + F_DEPTH < 8 ? 7 : 4; };
  static_for<0, 8>([&](auto I) {
    constexpr int i = decltype(I)::value;
    constexpr int younger = [&]() { int n = i < F_DEPTH ? 4 : 0; for (int k = i + 1; k < F_DEPTH; ++k) n += 7;
                                    for (int k = (i >= F_DEPTH ? i - F_DEPTH + 1 : 0); k < i; ++k) n += grp(k); return n; }();
    wait_vm<younger>();
    bar();
    if constexpr (i == 0) { TS(2) }
    if constexpr (i + F_DEPTH < 8) stage(i + F_DEPTH);
    else tile(6 + i + F_DEPTH - 8);
    if constexpr (i == 6) {
      asm volatile("" : "+v"(cq[0]), "+v"(cq[1]), "+v"(cq[2]), "+v"(cq[3]));
#pragma unroll
      for (int r = 0; r < 2; ++r) {
        const f32x4 cv = __builtin_bit_cast(f32x4, cq[2 * r]) + __builtin_bit_cast(f32x4, cq[2 * r + 1]);
        const f32x4 v = {sum8(cv[0]), sum8(cv[1]), sum8(cv[2]), sum8(cv[3])};
        if ((lane & 7) == 0) *(f32x4*)(smem + F_CS + (r ? f4b : f4a) * 16) = v;
      }
      wait_lgkm0();
    }
  });
  wait_vm<4 * (F_DEPTH - 1)>();
  TS(3)
  bar();
#pragma unroll
  for (int t = 6 + F_DEPTH; t < 11; ++t) tile(t);
  wdp(0); wdp(1);
  wait_vm<12 + 4>();
  bar();
#pragma unroll
  for (int u = 2; u < 6; ++u) wdp(u);
  wait_vm<8 + 12>();
  bar();
#pragma unroll
  for (int u = 6; u < 10; ++u) wdp(u);
  wait_vm<4 + 20>();
  bar();
  gu32* ca = (gu32*)(ws + cnt);
  if (lw == 0 && lane == 0) __hip_atomic_fetch_add(ca + rb * 64, 1u, __ATOMIC_RELAXED, __HIP_MEMORY_SCOPE_AGENT);
  asm volatile("" ::: "memory");
#pragma unroll
  for (int u = 10; u < 13; ++u) wdp(u);
  wait_vm<26>();
  bar();
  TS(4)
  if (lw == 0) {
    if (lane == 0) poll_ge(ca + (rb64 >> 1) * 64, 16u);
    wait_vm<0>();
  } else {
    const u32 po = (u32)OFF_WD + (ct * NKF * 4) * 1024 + (12 + lw) * 8192 + lane * 16;
#pragma unroll
    for (int i = 0; i < 4; ++i) dma2(po, ws + i * 2048, lds0 + f_wd(12) + lw * 8192 + i * 2048);
    wait_vm<8>();
  }
  TS(5)
  bar();
  bar();
  if (lw == 0 && lane == 0) __hip_atomic_fetch_add(ca + rb * 64 + 32, 1u, __ATOMIC_RELAXED, __HIP_MEMORY_SCOPE_AGENT);
  asm volatile("" ::: "memory");
#pragma unroll
  for (int u = 16; u < 19; ++u) wdp(u);
  if (lw == 0 && lane == 0) poll_ge(ca + (rb64 >> 1) * 64 + 32, 16u);
  wait_vm<0>();
  TS(6)
  bar();
  wdp(19); wdp(20);
  dma2(wdo - (lw >= 2 ? 4096 : 0), ws + 21 * 8192, lds0 + f_wd(21) + lw * 2048);
  {
    const u16* xres = (const u16*)(ws + xin) + (size_t)(ct * 64 + lw * 16 + (lane >> 3)) * XS + rb64 * 64 + (lane & 7) * 8;
    const u32x4 r0 = *(const u32x4*)xres, r1 = *(const u32x4*)(xres + 8 * XS);
    char* rp = smem + F_RES + (lw * 16 + (lane >> 3)) * 144 + (lane & 7) * 16;
    *(u32x4*)rp = r0;
    *(u32x4*)(rp + 8 * 144) = r1;
  }
  wait_vm<0>();
  wait_lgkm0();
  bar();
  TS(7) TS_FLUSH
}

template <bool FINAL>
__global__ __launch_bounds__(512, 2) void k_ffn(char* ws, u16* xout, double* st, u32 xin, u32 hoff, u32 cnt,
                                                u32 unused KIDP) {
  extern __shared__ __attribute__((aligned(16))) char smem[];
  const int tid = threadIdx.x, lane = tid & 63;
  const int w = __builtin_amdgcn_readfirstlane(tid >> 6);
  const int xcd = blockIdx.x & 7, idx = blockIdx.x >> 3;
  const int rb = (xcd & 1) * 8 + (idx & 7), c = (xcd >> 1) * 4 + (idx >> 3);
  const int rb64 = xcd * 4 + (idx & 3), ct = idx >> 2;
  if (__builtin_expect(w >= 4, 1)) ffn_loader(smem, ws, lane, w - 4, rb, c, rb64, ct, xin, cnt KIDA(kid_));
  else ffn_compute<FINAL>(smem, ws, lane, w, rb, c, rb64, ct, xin, hoff, xout, st KIDA(kid_));
}

extern "C" void kernel_launch(void* const* d_in, const int* in_sizes, int n_in, void* d_out, int out_size, void* d_ws,
                              size_t ws_size, hipStream_t stream) {
  const u16* x = (const u16*)d_in[0];
  const u16* logA = (const u16*)d_in[1];
  const u16* Aim = (const u16*)d_in[2];
  const u16* Cre = (const u16*)d_in[3];
  const u16* Cim = (const u16*)d_in[4];
  const u16* logdt = (const u16*)d_in[5];
  const u16* dskip = (const u16*)d_in[6];
  const u16* wg = (const u16*)d_in[8];
  const u16* wu = (const u16*)d_in[9];
  const u16* wd = (const u16*)d_in[10];
  char* ws = (char*)d_ws;
  (void)hipFuncSetAttribute((const void*)k_first, hipFuncAttributeMaxDynamicSharedMemorySize, FIRST_LDS);
  (void)hipFuncSetAttribute((const void*)k_conv, hipFuncAttributeMaxDynamicSharedMemorySize, CONV_LDS);
  (void)hipFuncSetAttribute((const void*)k_ffn<false>, hipFuncAttributeMaxDynamicSharedMemorySize, F_LDS);
  (void)hipFuncSetAttribute((const void*)k_ffn<true>, hipFuncAttributeMaxDynamicSharedMemorySize, F_LDS);

  k_first<<<256, 512, FIRST_LDS, stream>>>(x, logA, Aim, Cre, Cim, logdt, dskip, wg, wu, wd, ws KIDA(0));
  k_ffn<false><<<256, 512, F_LDS, stream>>>(ws, (u16*)(ws + OFF_XC), (double*)(ws + OFF_ST2), (u32)OFF_XB, (u32)OFF_H,
                                            (u32)OFF_CNT, 0u KIDA(1));
  k_conv<<<256, 320, CONV_LDS, stream>>>(ws, dskip KIDA(2));
  k_ffn<true><<<256, 512, F_LDS, stream>>>(ws, (u16*)d_out, (double*)(ws + OFF_ST2), (u32)OFF_XD, (u32)OFF_H2,
                                           (u32)OFF_CNT + 16 * 256, 0u KIDA(3));
}
```

```cpp
#include <hip/hip_runtime.h>
#include <hip/hip_bf16.h>
#include <stdint.h>
#include <type_traits>

typedef unsigned short u16;
typedef unsigned int u32;
typedef __attribute__((ext_vector_type(8))) __bf16 bf16x8;
typedef __attribute__((ext_vector_type(2))) __bf16 bf16x2;
typedef __attribute__((ext_vector_type(8))) _Float16 f16x8;
typedef __attribute__((ext_vector_type(2))) _Float16 f16x2;
typedef __attribute__((ext_vector_type(4))) float f32x4;
typedef __attribute__((ext_vector_type(2))) float f32x2;
typedef __attribute__((ext_vector_type(4))) u32 u32x4;
typedef __attribute__((ext_vector_type(2))) u32 u32x2;
typedef __attribute__((ext_vector_type(4))) short s16x4;
typedef __attribute__((ext_vector_type(2))) double f64x2;
#define LAS __attribute__((address_space(3)))
#define DEV __device__ __forceinline__

#define TSL(i)
#define TSL_FLUSH
#define KIDP
#define KIDA(n)
#define TSP
#define TSA
#define TS_DECL
#define TS(i)
#define TS_FLUSH

constexpr int SL = 2048, SD = 512, SN = 128, SF = 1376;
constexpr int NKF = 43;
constexpr int KIMG_CH = 8448;
constexpr int KIMG_C1 = 4224;
constexpr int XG_ROW = SL * 8 + 256;

constexpr u32 OFF_WGU = 0;
constexpr u32 OFF_WD = OFF_WGU + 16 * 176 * 1024;
constexpr u32 OFF_CSP = OFF_WD + 8 * NKF * 4096;
constexpr u32 OFF_X = OFF_CSP + 16 * 176 * 64;
constexpr u32 X_SZ = SL * SD * 2;
constexpr u32 OFF_H = OFF_X + 2 * X_SZ;
constexpr u32 H_SZ = 32 * NKF * 4096;
constexpr u32 OFF_GRAN = OFF_H + 2 * H_SZ;
constexpr u32 OFF_XG = OFF_GRAN + 2 * SL * 8;
constexpr u32 XG_SZ = 256 * XG_ROW;
constexpr u32 OFF_PART = OFF_XG + 2 * XG_SZ;
constexpr u32 OFF_CNT = OFF_PART + SL * 128;
constexpr u32 OFF_EPOCH = OFF_CNT + 256 * 128;
constexpr u32 WS_USED = OFF_EPOCH + 256;

DEV float bflo(u32 v) { return __uint_as_float(v << 16); }
DEV float bfhi(u32 v) { return __uint_as_float(v & 0xffff0000u); }
DEV float bf2f(u16 v) { return __uint_as_float((u32)v << 16); }
DEV u32 pk_bf16(float a, float b) {
  f32x2 v = {a, b};
  return __builtin_bit_cast(u32, __builtin_convertvector(v, bf16x2));
}
DEV u32 pk_f16(float a, float b) {
  f32x2 v = {a, b};
  return __builtin_bit_cast(u32, __builtin_convertvector(v, f16x2));
}
DEV float bfr(float a) { return bflo(pk_bf16(a, a)); }
DEV float dot2bf(u32 a, u32 b, float c) {
  return __builtin_amdgcn_fdot2_f32_bf16(__builtin_bit_cast(bf16x2, a), __builtin_bit_cast(bf16x2, b), c, false);
}
DEV f32x4 mfma_bf16(u32x4 a, u32x4 b, f32x4 c) {
  return __builtin_amdgcn_mfma_f32_16x16x32_bf16(__builtin_bit_cast(bf16x8, a), __builtin_bit_cast(bf16x8, b), c, 0, 0, 0);
}
DEV f32x4 mfma_f16(u32x4 a, u32x4 b, f32x4 c) {
  return __builtin_amdgcn_mfma_f32_16x16x32_f16(__builtin_bit_cast(f16x8, a), __builtin_bit_cast(f16x8, b), c, 0, 0, 0);
}
DEV u32x2 lds_tr(const char* p) {
  return __builtin_bit_cast(u32x2, __builtin_amdgcn_ds_read_tr16_b64_v4i16((LAS s16x4*)p));
}
DEV u32x2 lds_tr32(u32 a) {
  return __builtin_bit_cast(u32x2, __builtin_amdgcn_ds_read_tr16_b64_v4i16((LAS s16x4*)a));
}
DEV u32x4 lds_rd128(u32 a) { return *(const LAS u32x4*)a; }
template <int CTRL>
DEV float dpp_f(float v) {
  return __builtin_bit_cast(float, __builtin_amdgcn_update_dpp(0, __builtin_bit_cast(int, v), CTRL, 0xf, 0xf, true));
}
DEV float sum8(float v) {
  v += dpp_f<0xB1>(v);
  v += dpp_f<0x4E>(v);
  v += dpp_f<0x141>(v);
  return v;
}
DEV float sum16(float v) { v = sum8(v); return v + dpp_f<0x140>(v); }
DEV void ln_consts(float sum, float sumsq, float& m, float& s) {
  float mean = sum * (1.0f / SD);
  float var = fmaxf(sumsq * (1.0f / SD) - mean * mean, 0.0f);
  m = bfr(mean);
  s = bfr(__builtin_amdgcn_sqrtf(bfr(bfr(var) + 1e-5f)));
}

typedef f32x2 cpx;
DEV cpx cmul(cpx a, cpx b) {
  const f32x2 t = a * f32x2{b[0], b[0]};
  return __builtin_elementwise_fma(f32x2{a[1], a[0]}, f32x2{-b[1], b[1]}, t);
}
DEV cpx cmulc(cpx a, cpx b) {
  const f32x2 t = a * f32x2{b[0], b[0]};
  return __builtin_elementwise_fma(f32x2{a[1], a[0]}, f32x2{b[1], -b[1]}, t);
}
DEV float exp_acc(float x) {
  const float t = x * 1.44269504088896340736f;
  const float e = fmaf(x, 1.44269504088896340736f, -t) + x * 1.92596299e-8f;
  const float r = __builtin_amdgcn_exp2f(t);
  return fmaf(r, e * 0.69314718056f, r);
}

typedef __attribute__((address_space(1))) u32 gu32;
typedef __attribute__((address_space(1))) unsigned long long gu64;
template <int N>
DEV void wait_vm() { asm volatile("s_waitcnt vmcnt(%0)" ::"n"(N) : "memory"); }
DEV void wait_lgkm0() { __builtin_amdgcn_s_waitcnt(0xC07F); }
DEV void bar() {
  asm volatile("" ::: "memory");
  __builtin_amdgcn_s_barrier();
  asm volatile("" ::: "memory");
}
DEV void lds_bar() { wait_lgkm0(); bar(); }
template <int I, int N, class Fn>
DEV void static_for(Fn&& f) {
  if constexpr (I < N) { f(std::integral_constant<int, I>{}); static_for<I + 1, N>(f); }
}
DEV int wgu_off(int c, int s, int t) { return (c * 176 + (t < 6 ? s * 6 + t : 96 + (t - 6) * 16 + s)) << 10; }
DEV int tile_of_oct(int oct) { return oct < 96 ? (oct / 6) * 11 + oct % 6 : ((oct - 96) / 5) * 11 + 6 + (oct - 96) % 5; }

constexpr int CV_U = 2 * KIMG_CH;
constexpr int CV_UROW = (128 + 1024) * 2;
constexpr int CV_P = CV_U + 4 * CV_UROW;
constexpr int CV_PROW = 2048 * 4 + 64 * 16 + 16;
constexpr int L_MISC = CV_P + 4 * CV_PROW;
constexpr int L_CS = L_MISC + 64;
constexpr int KG_CH = 64 * 512;
constexpr int KG_TAB = 65536;
constexpr int KG_TROW = 672 * 2;
constexpr int KG_PACK = 68608;
constexpr int PK_IMG = 8192;
constexpr int F_W1 = 65536;
constexpr int LDS_BYTES = 163840;
constexpr int F_BASE = 57344;
static_assert(L_CS + 704 <= 65536 && KG_CH + 53 * 512 <= L_MISC, "lds map");
constexpr int f_tile(int t) { return F_W1 + (t - 6) * 16384; }
constexpr int f_wd(int u) {
  return u < 7 ? u * 8192 : u < 9 ? F_W1 + 81920 + (u - 7) * 8192 : u < 19 ? F_W1 + (u - 9) * 8192 : f_wd(u - 19);
}
constexpr int F_RES = f_wd(4);

DEV int chan_pair(int b) { return (b & 7) * 32 + (b >> 3); }
DEV u32 lds_ld(u32 a) { return *(volatile LAS u32*)a; }
DEV void lds_st(u32 a, u32 v) { *(volatile LAS u32*)a = v; }
DEV u32 lds_arrive(u32 word, int lane) {
  wait_lgkm0();
  asm volatile("" ::: "memory");
  u32 old = 0;
  if (lane == 0) old = __hip_atomic_fetch_add((LAS u32*)word, 1u, __ATOMIC_RELAXED, __HIP_MEMORY_SCOPE_WORKGROUP);
  return __builtin_amdgcn_readfirstlane(old);
}
DEV void lds_wait(u32 word, u32 want) {
  for (int sp = 0; sp < (1 << 24); ++sp)
    if (lds_ld(word) >= want) break;
  asm volatile("" ::: "memory");
}
DEV void lds_wait_slow(u32 word, u32 want) {
  for (int sp = 0; sp < (1 << 20); ++sp) {
    if (lds_ld(word) >= want) break;
    __builtin_amdgcn_s_sleep(2);
  }
  asm volatile("" ::: "memory");
}
#define GW_SLEEP 4
#define GR_SLEEP 6
#define GR_DELAY 16
DEV void grid_signal(char* ws, int k, int b, int lane) {
  if (lane < 8)
    __hip_atomic_fetch_add((gu32*)(ws + OFF_CNT) + (k * 64 + lane * 8 + (b & 7)) * 32, 1u, __ATOMIC_RELAXED,
                           __HIP_MEMORY_SCOPE_AGENT);
}
DEV void grid_wait(char* ws, int k, int b, int lane) {
  gu32* p = (gu32*)(ws + OFF_CNT) + (k * 64 + (b & 7) * 8 + (lane & 7)) * 32;
  for (int sp = 0; sp < (1 << 20); ++sp) {
    u32 v = 32u;
    if (lane < 8) v = __hip_atomic_load(p, __ATOMIC_RELAXED, __HIP_MEMORY_SCOPE_AGENT);
    if (__builtin_amdgcn_ballot_w64(v < 32u) == 0ull) break;
    __builtin_amdgcn_s_sleep(GW_SLEEP);
  }
  asm volatile("" ::: "memory");
}
DEV void poll_ge(gu32* cp, u32 want) {
  for (int sp = 0; sp < (1 << 20); ++sp) {
    if (__hip_atomic_load(cp, __ATOMIC_RELAXED, __HIP_MEMORY_SCOPE_AGENT) >= want) break;
    __builtin_amdgcn_s_sleep(1);
  }
}
DEV void gload16_sc1(u32x4& dst, u32 voff, const char* base) {
  asm volatile("global_load_dwordx4 %0, %1, %2 sc1" : "=v"(dst) : "v"(voff), "s"(base) : "memory");
}
DEV void dma2(u32 voff, const char* base, u32 lds) {
  asm volatile("s_mov_b32 m0, %2\n\ts_nop 0\n\tglobal_load_lds_dwordx4 %0, %1 sc1\n\t"
               "global_load_lds_dwordx4 %0, %1 offset:1024 sc1" ::"v"(voff), "s"(base), "s"(lds) : "memory");
}
DEV void dma4(u32 voff, const char* base, u32 lds) {
  asm volatile("s_mov_b32 m0, %2\n\ts_nop 0\n\tglobal_load_lds_dwordx4 %0, %1 sc1\n\t"
               "global_load_lds_dwordx4 %0, %1 offset:1024 sc1\n\tglobal_load_lds_dwordx4 %0, %1 offset:2048 sc1\n\t"
               "global_load_lds_dwordx4 %0, %1 offset:3072 sc1" ::"v"(voff), "s"(base), "s"(lds) : "memory");
}
DEV cpx cpow2(float a2, float rh, float rl, float s) {
  const float mag = __builtin_amdgcn_exp2f(a2 * s);
  const float rev = fmaf(rl, s, __builtin_amdgcn_fractf(rh * s));
  return f32x2{__builtin_amdgcn_cosf(rev), __builtin_amdgcn_sinf(rev)} * f32x2{mag, mag};
}
DEV u32 glu_tile(f32x4 acc, f32x4 cs, float m, float s, float c1) {
  const f32x2 nm = {-m, -m};
  const f32x2 gg = __builtin_elementwise_fma(nm, f32x2{cs[0], cs[1]}, f32x2{acc[0], acc[1]}) * f32x2{c1, c1};
  const f32x2 uu = __builtin_elementwise_fma(nm, f32x2{cs[2], cs[3]}, f32x2{acc[2], acc[3]});
  const f32x2 ex = {__builtin_amdgcn_exp2f(gg[0]), __builtin_amdgcn_exp2f(gg[1])};
  const f32x2 dn = __builtin_elementwise_fma(ex, f32x2{s, s}, f32x2{s, s});
  const f32x2 hh = uu * f32x2{__builtin_amdgcn_rcpf(dn[0]), __builtin_amdgcn_rcpf(dn[1])};
  return pk_bf16(hh[0], hh[1]);
}
DEV u32x4 tr4(u32 r0, u32 r1, u32 r2, u32 r3) {
  auto a = __builtin_amdgcn_permlane32_swap(r0, r2, false, false);
  auto b = __builtin_amdgcn_permlane32_swap(r1, r3, false, false);
  auto c = __builtin_amdgcn_permlane16_swap(a[0], b[0], false, false);
  auto d = __builtin_amdgcn_permlane16_swap(a[1], b[1], false, false);
  return u32x4{c[0], c[1], d[0], d[1]};
}
DEV float sum_groups(float v) {
  auto a = __builtin_amdgcn_permlane32_swap(__float_as_uint(v), __float_as_uint(v), false, false);
  v = __uint_as_float(a[0]) + __uint_as_float(a[1]);
  auto b = __builtin_amdgcn_permlane16_swap(__float_as_uint(v), __float_as_uint(v), false, false);
  return __uint_as_float(b[0]) + __uint_as_float(b[1]);
}

#define F_WIN 16
#define F_WIN0 8
#define F_UB 18

struct Args {
  const u16 *x, *logA, *Aim, *Cre, *Cim, *logdt, *dskip, *wg, *wu, *wd;
  u16* out;
  char* ws;
};

DEV void conv_mfma(u32 lds0, char* smem, int lane, int w) {
  const int np = lane & 15, h = lane >> 4, q = (lane & 15) >> 2, p = lane & 3;
  u32 ka = lds0 + (w >> 1) * KIMG_CH + ((q & 1) ? KIMG_C1 + 2 * 18 : 2 * 16) + 2 * (48 - 16 * h - 2 * q + 4 * p) - 16;
  u32 ua = lds0 + CV_U + w * CV_UROW + 256 + 2 * (8 * (np + h) - 24) - 192;
  asm volatile("" : "+v"(ka), "+v"(ua));
  f32x4 acc[8];
#pragma unroll
  for (int j = 0; j < 8; ++j) acc[j] = f32x4{0, 0, 0, 0};
  u32x4 kf[2][8], uf[2][8];
  auto rd = [&](auto D) {
    constexpr int dl = decltype(D)::value;
#pragma unroll
    for (int i = 0; i < 8; ++i) {
      const u32x2 lo = lds_tr32(ka + (4 * i + dl) * 128 + 16), hi = lds_tr32(ka + (4 * i + dl) * 128);
      kf[dl & 1][i] = u32x4{lo[0], lo[1], hi[0], hi[1]};
      uf[dl & 1][i] = lds_rd128(ua + i * 256 + (3 - dl) * 64);
    }
  };
  rd(std::integral_constant<int, 0>{});
  __builtin_amdgcn_sched_barrier(0);
  static_for<0, 4>([&](auto D) {
    constexpr int dl = decltype(D)::value;
    if constexpr (dl < 3) rd(std::integral_constant<int, dl + 1>{});
#pragma unroll
    for (int mx = 0; mx < 8; ++mx) {
#pragma unroll
      for (int o = 0; o <= mx; ++o)
        if (mx + o < 8) acc[mx + o] = mfma_bf16(kf[dl & 1][mx], uf[dl & 1][o], acc[mx + o]);
#pragma unroll
      for (int o = 0; o < mx; ++o)
        if (mx + o < 8) acc[mx + o] = mfma_bf16(kf[dl & 1][o], uf[dl & 1][mx], acc[mx + o]);
    }
    if constexpr (dl < 3) {
#pragma unroll
      for (int k = 0; k < 24; ++k) {
        __builtin_amdgcn_sched_group_barrier(0x008, 1, 0);
        __builtin_amdgcn_sched_group_barrier(0x100, 1, 0);
      }
      __builtin_amdgcn_sched_group_barrier(0x008, 12, 0);
    }
    __builtin_amdgcn_sched_barrier(0);
  });
  char* pp = smem + CV_P + w * CV_PROW + 16 + np * 64 + h * 16 + (np >> 1) * 16;
#pragma unroll
  for (int j = 0; j < 8; ++j) *(f32x4*)(pp + j * (1024 + 128)) = acc[j];
}

DEV void compute_waves(const Args& A, char* smem, int tid, int w, int b KIDP) {
  TS_DECL
  char* ws = A.ws;
  int lane = tid & 63;
  const u32 lds0 = (u32)(uintptr_t)smem;
  const auto rsw = __builtin_amdgcn_make_buffer_rsrc(ws, 0, (int)WS_USED, 0x00020000);
  const int d0 = 2 * chan_pair(b);
  const u32 l_dsk = *(const u32*)(A.dskip + d0);
  u32x4 gr[4], xg[4];
  {
    const int ch = w >> 1, n = tid & 127;
    const u32 pi = d0 * SN + tid;
    const u16 l_dt = A.logdt[d0 + ch], l_a = A.logA[pi], l_b = A.Aim[pi];
    const u16 l_cr = A.Cre[pi], l_ci = A.Cim[pi], l_cr2 = A.Cre[pi ^ 64], l_ci2 = A.Cim[pi ^ 64];
    if (tid < 8) *(u32*)(smem + L_MISC + tid * 4) = 0u;
    if (n < 8) *(u32*)(smem + KG_TAB + ch * KG_TROW + n * 4) = 0u;
    __builtin_amdgcn_sched_barrier(0);
    u32 wr[8];
#pragma unroll
    for (int r = 0; r < 8; ++r) { wr[r] = lds0 + ch * KG_CH + r * 512 + ((n * 4) ^ (r << 4)); asm volatile("" : "+v"(wr[r])); }
    const int xt = w & 1, r16 = lane & 15, g = lane >> 4;
    u32 fa[2], fb[2], fc[2];
#pragma unroll
    for (int o = 0; o < 2; ++o) {
      const int rc = min(r16, 4);
      fa[o] = lds0 + ch * KG_CH + (16 * xt + r16) * 512 + (((o * 4 + g) ^ (r16 & 7)) << 4);
      fb[o] = lds0 + ch * KG_CH + (32 + r16) * 512 + (((o * 4 + g) ^ (r16 & 7)) << 4);
      fc[o] = lds0 + ch * KG_CH + (48 + rc) * 512 + (((o * 4 + g) ^ rc) << 4);
      asm volatile("" : "+v"(fa[o]), "+v"(fb[o]), "+v"(fc[o]));
    }
    const int i0 = 16 * xt + 4 * g;
    const int ts2 = r16 < 8 ? 16 : (r16 < 12 ? 8 : 4);
    const int tb = r16 < 8 ? r16 : (r16 < 12 ? 248 + r16 : 384 + 64 * ((r16 >> 1) & 1) + (r16 & 1));
    u32 ta[4];
#pragma unroll
    for (int e = 0; e < 4; ++e) { ta[e] = lds0 + KG_TAB + ch * KG_TROW + 32 + tb * 2 + ts2 * (i0 + e); asm volatile("" : "+v"(ta[e])); }
    u32 tc = lds0 + KG_TAB + ch * KG_TROW + 32 + 2 * ((r16 < 4 || i0 == 0) ? 512 + 32 * min(r16, 4) + i0 : 648);
    asm volatile("" : "+v"(tc));
    u32 ia[10];
    {
      const float f0 = (float)(8 * tid);
#pragma unroll
      for (int j = 0; j < 5; ++j) {
        const u32 bits = pk_bf16(f0 + (float)(2 * j - 2), f0 + (float)(2 * j - 1));
        const u32 lin = lds0 + KG_TAB + (8 * tid + 2 * j - 2 + 16) * 2;
        ia[2 * j] = tid < 24 ? lin : (bits & 0xffffu) * 2 + (lds0 + KG_TAB - (0x4300 - 144) * 2);
        ia[2 * j + 1] = tid < 24 ? lin + 2 : (bits >> 16) * 2 + (lds0 + KG_TAB - (0x4300 - 144) * 2);
        asm volatile("" : "+v"(ia[2 * j]), "+v"(ia[2 * j + 1]));
      }
    }
    __builtin_amdgcn_sched_barrier(0);
    lds_bar();
    __builtin_amdgcn_sched_barrier(0);
    const u32 ea = pk_bf16(exp_acc(bf2f(l_dt)), exp_acc(bf2f(l_a)));
    const float dt = bflo(ea);
    const float a2 = -(bfhi(ea) * dt) * 1.44269504088896340736f;
    const float bi = bf2f(l_b) * dt;
    const float rh = bi * 0.15915494309189533577f;
    const float rl = fmaf(bi, 6.420638e-9f, fmaf(bi, 0.15915494309189533577f, -rh));
    u32 mx = max(max((u32)l_cr & 0x7fffu, (u32)l_ci & 0x7fffu), max((u32)l_cr2 & 0x7fffu, (u32)l_ci2 & 0x7fffu));
    mx = max(mx, (u32)__builtin_amdgcn_update_dpp(0, (int)mx, 0xB1, 0xf, 0xf, true));
    mx = max(mx, (u32)__builtin_amdgcn_update_dpp(0, (int)mx, 0x4E, 0xf, 0xf, true));
    mx = max(mx, (u32)__builtin_amdgcn_update_dpp(0, (int)mx, 0x141, 0xf, 0xf, true));
    mx = max(mx, (u32)__builtin_amdgcn_update_dpp(0, (int)mx, 0x140, 0xf, 0xf, true));
    mx = max(max((u32)__builtin_amdgcn_readlane((int)mx, 0), (u32)__builtin_amdgcn_readlane((int)mx, 16)),
             max((u32)__builtin_amdgcn_readlane((int)mx, 32), (u32)__builtin_amdgcn_readlane((int)mx, 48)));
    const u32 ex = min(max(mx >> 7, 2u), 252u) + 1u;
    const float fs = 2.0f * dt * __uint_as_float(ex << 23), isc = __uint_as_float((254u - ex) << 23);
    cpx z[12];
#pragma unroll
    for (int p = 0; p < 12; p += 3) {
      z[p] = cpow2(a2, rh, rl, (float)(1 << p));
      z[p + 1] = cmul(z[p], z[p]);
      z[p + 2] = cmul(z[p + 1], z[p + 1]);
    }
    {
      cpx V[56];
      auto put = [&](int i) { *(LAS u32*)(wr[i & 7] + (i >> 3) * 4096) = pk_f16(V[i][0], V[i][1]); };
      V[0] = cpx{1.0f, 0.0f};
      V[1] = z[3]; V[2] = z[4]; V[4] = z[5]; V[8] = z[6]; V[16] = z[7];
      V[32] = cpx{bf2f(l_cr) * isc, bf2f(l_ci) * -isc};
      constexpr int OP[47][3] = {
          {3, 1, 4},   {5, 1, 5},   {6, 2, 5},   {33, 32, 0}, {34, 32, 1}, {36, 32, 2}, {40, 32, 8}, {44, 32, 9},
          {7, 3, 5},   {9, 1, 6},   {10, 2, 6},  {11, 3, 6},  {35, 34, 0}, {37, 36, 0}, {38, 36, 1}, {41, 34, 8},
          {12, 4, 6},  {13, 5, 6},  {14, 6, 6},  {15, 7, 6},  {39, 38, 0}, {42, 36, 8}, {43, 38, 8}, {45, 36, 9},
          {17, 1, 7},  {18, 2, 7},  {19, 3, 7},  {20, 4, 7},  {46, 40, 9}, {47, 42, 9}, {48, 32, 10}, {49, 40, 10},
          {21, 5, 7},  {22, 6, 7},  {23, 7, 7},  {24, 8, 7},  {50, 44, 10}, {51, 46, 10}, {52, 32, 11}, {25, 9, 7},
          {26, 10, 7}, {27, 11, 7}, {28, 12, 7}, {29, 13, 7}, {30, 14, 7}, {31, 15, 7}, {0, 0, 0}};
      put(0); put(1); put(2); put(4); put(8); put(16); put(32);
      static_for<0, 13>([&](auto G) {
        constexpr int g4 = decltype(G)::value * 4;
        f32x2 t[4];
#pragma unroll
        for (int k = 0; k < 4; ++k)
          if (g4 + k < 46) t[k] = V[OP[g4 + k][1]] * f32x2{z[OP[g4 + k][2]][0], z[OP[g4 + k][2]][0]};
#pragma unroll
        for (int k = 0; k < 4; ++k)
          if (g4 >= 4 && g4 - 4 + k < 46) put(OP[g4 - 4 + k][0]);
#pragma unroll
        for (int k = 0; k < 4; ++k)
          if (g4 + k < 46) {
            const cpx a = V[OP[g4 + k][1]];
            const float zi = z[OP[g4 + k][2]][1];
            V[OP[g4 + k][0]] = __builtin_elementwise_fma(f32x2{a[1], a[0]}, OP[g4 + k][0] < 32 ? f32x2{-zi, zi} : f32x2{zi, -zi}, t[k]);
          }
        __builtin_amdgcn_sched_barrier(0);
      });
    }
    lds_arrive(lds0 + L_MISC + 8 + ch * 4, lane);
    lds_wait(lds0 + L_MISC + 8 + ch * 4, 2u);
    {
      f32x4 acc0 = {0, 0, 0, 0}, acc1 = {0, 0, 0, 0};
#pragma unroll
      for (int s = 0; s < 8; ++s) {
        const u32x4 a = lds_rd128(fa[s & 1] + (s >> 1) * 128);
        acc0 = mfma_f16(a, lds_rd128(fb[s & 1] + (s >> 1) * 128), acc0);
        acc1 = mfma_f16(a, lds_rd128(fc[s & 1] + (s >> 1) * 128), acc1);
      }
#pragma unroll
      for (int j = 0; j < 4; ++j)
        xg[j] = __builtin_amdgcn_raw_buffer_load_b128(rsw, OFF_XG + (d0 >> 1) * XG_ROW + tid * 64 + j * 16, 0, 16);
#pragma unroll
      for (int j = 0; j < 4; ++j) gr[j] = __builtin_amdgcn_raw_buffer_load_b128(rsw, OFF_GRAN + tid * 16 + j * 4096, 0, 16);
      acc0 *= fs; acc1 *= fs;
      const u32 v01 = pk_bf16(acc0[0], acc0[1]), v23 = pk_bf16(acc0[2], acc0[3]);
      *(LAS u16*)ta[0] = (u16)v01; *(LAS u16*)ta[1] = (u16)(v01 >> 16);
      *(LAS u16*)ta[2] = (u16)v23; *(LAS u16*)ta[3] = (u16)(v23 >> 16);
      *(LAS u32x2*)tc = u32x2{pk_bf16(acc1[0], acc1[1]), pk_bf16(acc1[2], acc1[3])};
    }
    lds_arrive(lds0 + L_MISC + 16, lane);
    lds_wait(lds0 + L_MISC + 16, 4u);
#pragma unroll
    for (int cc = 0; cc < 2; ++cc) {
      u32 kv[5];
#pragma unroll
      for (int j = 0; j < 5; ++j)
        kv[j] = (u32)*(const LAS u16*)(ia[2 * j] + cc * KG_TROW) | ((u32)*(const LAS u16*)(ia[2 * j + 1] + cc * KG_TROW) << 16);
      char* o0 = smem + cc * KIMG_CH + 32 + tid * 16;
      *(u32x4*)o0 = u32x4{kv[1], kv[2], kv[3], kv[4]};
      *(u32x4*)(o0 + KIMG_C1) = u32x4{kv[0], kv[1], kv[2], kv[3]};
    }
    if (tid < 8) *(u32x4*)(smem + (tid >> 2) * KIMG_CH + ((tid >> 1) & 1) * KIMG_C1 + (tid & 1) * 16) = u32x4{0, 0, 0, 0};
  }
  lds_wait(lds0 + L_MISC, 1u);
  const u32 tag = __builtin_amdgcn_readfirstlane(lds_ld(lds0 + L_MISC + 4));
  TS(1)
  const int xcd = b & 7, idx = b >> 3;
  const int rb = (xcd & 1) * 8 + (idx & 7), c = (xcd >> 1) * 4 + (idx >> 3);
  const int rb64 = xcd * 4 + (idx & 3), ct = idx >> 2;

#pragma unroll 1
  for (int layer = 0; layer < 2; ++layer) {
    {
      asm volatile("" : "+v"(tid), "+v"(lane));
      const u32 goff = OFF_GRAN + tid * 16, xoff = OFF_XG + (d0 >> 1) * XG_ROW + tid * 64;
      bool fresh = layer == 0;
      if (layer == 1) lds_wait_slow(lds0 + L_MISC + 8, 3u);
      for (int sp = 0; sp < (1 << 18); ++sp) {
        if (!fresh) {
#pragma unroll
          for (int j = 0; j < 4; ++j) xg[j] = __builtin_amdgcn_raw_buffer_load_b128(rsw, xoff + layer * XG_SZ + j * 16, 0, 16);
        }
        u32 bad = 0;
#pragma unroll
        for (int j = 0; j < 4; ++j) bad |= (xg[j][1] ^ tag) | (xg[j][3] ^ tag);
        if (__builtin_amdgcn_ballot_w64(bad != 0u) == 0ull) break;
        fresh = false;
        __builtin_amdgcn_s_sleep(GR_SLEEP);
      }
      fresh = layer == 0;
      for (int sp = 0; sp < (1 << 18); ++sp) {
        if (!fresh) {
#pragma unroll
          for (int j = 0; j < 4; ++j) gr[j] = __builtin_amdgcn_raw_buffer_load_b128(rsw, goff + layer * (SL * 8) + j * 4096, 0, 16);
        }
        u32 bad = 0;
#pragma unroll
        for (int j = 0; j < 4; ++j) bad |= (gr[j][1] ^ tag) | (gr[j][3] ^ tag);
        if (__builtin_amdgcn_ballot_w64(bad != 0u) == 0ull) break;
        fresh = false;
        __builtin_amdgcn_s_sleep(GR_SLEEP);
      }
      TSL(2)
      float base[2][8];
#pragma unroll
      for (int r = 0; r < 2; ++r) {
        u32 up[2][2];
#pragma unroll
        for (int j = 0; j < 2; ++j) {
          const u32 g0 = gr[2 * j][2 * r], g1 = gr[2 * j + 1][2 * r], x0 = xg[2 * j][2 * r], x1 = xg[2 * j + 1][2 * r];
          const f32x2 rs = {__builtin_amdgcn_rcpf(bfhi(g0)), __builtin_amdgcn_rcpf(bfhi(g1))};
          const f32x2 mm = {bflo(g0), bflo(g1)};
#pragma unroll
          for (int cc = 0; cc < 2; ++cc) {
            const f32x2 xx = cc ? f32x2{bfhi(x0), bfhi(x1)} : f32x2{bflo(x0), bflo(x1)};
            const float dk = cc ? bfhi(l_dsk) : bflo(l_dsk);
            const f32x2 u = (xx - mm) * rs;
            up[cc][j] = pk_bf16(u[0], u[1]);
            const f32x2 bs = __builtin_elementwise_fma(u, f32x2{dk, dk}, xx);
            base[cc][4 * j + r] = bs[0]; base[cc][4 * j + r + 2] = bs[1];
          }
        }
#pragma unroll
        for (int cc = 0; cc < 2; ++cc)
          *(u32x2*)(smem + CV_U + (cc * 2 + r) * CV_UROW + 256 + tid * 8) = u32x2{up[cc][0], up[cc][1]};
      }
      if (tid < 64) *(u32x4*)(smem + CV_U + (tid >> 4) * CV_UROW + (tid & 15) * 16) = u32x4{0, 0, 0, 0};
      if (tid < 4) *(float*)(smem + CV_P + tid * CV_PROW + 12) = 0.f;
      lds_bar();
      TSL(3)
      conv_mfma(lds0, smem, lane, w);
      lds_arrive(lds0 + L_MISC + 20, lane);
      lds_wait(lds0 + L_MISC + 20, 4u * (layer + 1));
      TSL(4)
      const int off = CV_P + 16 + tid * 32 + (tid >> 2) * 16;
      const int offm = off + CV_PROW - (((tid & 3) == 0 && tid > 0) ? 20 : 4);
      const u32 xo = OFF_X + layer * X_SZ + ((tid >> 4) * SD + d0) * 256 + (tid & 15) * 16;
#pragma unroll
      for (int cc = 0; cc < 2; ++cc) {
        const char* p0 = smem + off + (2 * cc) * CV_PROW;
        const f32x4 a0 = *(const f32x4*)p0, a1 = *(const f32x4*)(p0 + 16);
        const f32x4 b0 = *(const f32x4*)(p0 + CV_PROW), b1 = *(const f32x4*)(p0 + CV_PROW + 16);
        const float bm = *(const float*)(smem + offm + (2 * cc) * CV_PROW);
        const float o[8] = {base[cc][0] + a0[0] + bm,    base[cc][1] + a0[1] + b0[0], base[cc][2] + a0[2] + b0[1],
                            base[cc][3] + a0[3] + b0[2], base[cc][4] + a1[0] + b0[3], base[cc][5] + a1[1] + b1[0],
                            base[cc][6] + a1[2] + b1[1], base[cc][7] + a1[3] + b1[2]};
        const u32x4 ov = {pk_bf16(o[0], o[1]), pk_bf16(o[2], o[3]), pk_bf16(o[4], o[5]), pk_bf16(o[6], o[7])};
        __builtin_amdgcn_raw_buffer_store_b128(ov, rsw, xo + cc * 256, 0, 16);
      }
      wait_vm<0>();
      if (lds_arrive(lds0 + L_MISC + 24, lane) == 4u * layer + 3u) grid_signal(ws, 1 + layer, b, lane);
      TSL(5)
    }
    asm volatile("" : "+v"(lane));
    asm volatile("" : "+s"(ws));
    const int mq = w;
    const int col = lane & 15, g = lane >> 4, q = (lane & 15) >> 2, p = lane & 3;
    const u32 hoff = OFF_H + layer * H_SZ;
    const int key = q | ((g & 1) << 2);
    u32 xa[2], wa[3];
#pragma unroll
    for (int j = 0; j < 2; ++j) {
      xa[j] = lds0 + (8 * g + q) * 256 + (((2 * mq + j) ^ key) * 32) + 8 * p;
      asm volatile("" : "+v"(xa[j]));
    }
#pragma unroll
    for (int r = 0; r < 3; ++r) {
      wa[r] = lds0 + r * F_BASE + lane * 16;
      asm volatile("" : "+v"(wa[r]));
    }
    auto lw128 = [&](auto O) { constexpr int o = decltype(O)::value; return lds_rd128(wa[o / F_BASE] + o % F_BASE); };
    f32x4 acc[11][2];
#pragma unroll
    for (int t = 0; t < 11; ++t)
#pragma unroll
      for (int j = 0; j < 2; ++j) acc[t][j] = f32x4{0, 0, 0, 0};
    f32x4 s1[2] = {{0, 0, 0, 0}, {0, 0, 0, 0}}, s2[2] = {{0, 0, 0, 0}, {0, 0, 0, 0}};
    const u32x4 ones = {0x3f803f80u, 0x3f803f80u, 0x3f803f80u, 0x3f803f80u};
    u32x4 xf[16][2], wf[2][6];
    auto rd = [&](auto K) {
      constexpr int k = decltype(K)::value, o = ((k >> 1) % 3) * 16384 + (k & 1) * 8192;
#pragma unroll
      for (int j = 0; j < 2; ++j) {
        const u32x2 lo = lds_tr32(xa[j] + o), hi = lds_tr32(xa[j] + o + 1024);
        xf[k][j] = u32x4{lo[0], lo[1], hi[0], hi[1]};
      }
      static_for<0, 6>([&](auto T) {
        constexpr int t = decltype(T)::value;
        wf[k & 1][t] = lw128(std::integral_constant<int, F_W1 + (k * 6 + t) * 1024>{});
      });
    };
    auto mm = [&](auto K) {
      constexpr int k = decltype(K)::value;
#pragma unroll
      for (int j = 0; j < 2; ++j) {
        s2[j] = mfma_bf16(xf[k][j], xf[k][j], s2[j]);
        s1[j] = mfma_bf16(ones, xf[k][j], s1[j]);
      }
#pragma unroll
      for (int t = 0; t < 6; ++t)
#pragma unroll
        for (int j = 0; j < 2; ++j) acc[t][j] = mfma_bf16(wf[k & 1][t], xf[k][j], acc[t][j]);
    };
    auto weave = [&]() {
#pragma unroll
      for (int k = 0; k < 10; ++k) {
        __builtin_amdgcn_sched_group_barrier(0x008, 1, 0);
        __builtin_amdgcn_sched_group_barrier(0x100, 1, 0);
      }
      __builtin_amdgcn_sched_group_barrier(0x008, 6, 0);
      __builtin_amdgcn_sched_barrier(0);
    };
    static_for<0, 8>([&](auto I) {
      constexpr int i = decltype(I)::value;
      wait_lgkm0();
      __builtin_amdgcn_sched_barrier(0);
      bar();
      if constexpr (i == 0) { TSL(6) }
      rd(std::integral_constant<int, 2 * i>{});
      if constexpr (i > 0) mm(std::integral_constant<int, 2 * i - 1>{});
      weave();
      rd(std::integral_constant<int, 2 * i + 1>{});
      mm(std::integral_constant<int, 2 * i>{});
      weave();
    });
    wait_lgkm0();
    __builtin_amdgcn_sched_barrier(0);
    bar();
    TSL(7)
    mm(std::integral_constant<int, 15>{});
    __builtin_amdgcn_sched_barrier(0);
    float lm[2], ls[2], lc[2];
    auto consts = [&](int j) {
      const float d01 = (p & 1) ? s2[j][1] : s2[j][0], d23 = (p & 1) ? s2[j][3] : s2[j][2];
      const float dv = sum_groups((g == q) ? ((p & 2) ? d23 : d01) : 0.f);
      ln_consts(s1[j][0], dv, lm[j], ls[j]);
      lc[j] = -1.44269504088896340736f * __builtin_amdgcn_rcpf(ls[j]);
    };
    const int lt = rb * 8 + 2 * mq + (g & 1);
    const u32 hb = hoff + (((lt >> 2) * NKF * 4 + (lt & 3)) << 10) + col * 16;
    const u32 csa = lds0 + L_CS + g * 16;
    u32 hv[12][2];
    hv[11][0] = 0; hv[11][1] = 0;
    auto hstore = [&](int t) {
      const int oct = (t < 6 ? c * 6 + t : 96 + c * 5 + (t - 6)) + (g >> 1);
      const bool ok = t < 6 || (oct < 172 && (t < 10 || g < 2));
      const u32 off = ok ? hb + (oct >> 2) * 4096 + (oct & 3) * 256 : 0x7ffffff0u;
      __builtin_amdgcn_raw_buffer_store_b128(tr4(hv[t][0], hv[t][1], hv[t + 1][0], hv[t + 1][1]), rsw, off, 0, 16);
    };
    static_for<6, 11>([&](auto T) {
      constexpr int t = decltype(T)::value;
      constexpr int e0 = t == 6 ? 0 : t == 7 ? 3 : t == 8 ? 6 : t - 1, ne = t < 8 ? 3 : t == 8 ? 2 : 1;
      u32x4 wv[16];
      f32x4 cs[3];
      f32x2 ga[6], ua[6];
      static_for<0, 4>([&](auto K) { wv[decltype(K)::value] = lw128(std::integral_constant<int, f_tile(t) + decltype(K)::value * 1024>{}); });
#pragma unroll
      for (int e = 0; e < ne; ++e) cs[e] = __builtin_bit_cast(f32x4, lds_rd128(csa + (e0 + e) * 64));
      __builtin_amdgcn_sched_barrier(0);
      static_for<0, 32>([&](auto S) {
        constexpr int sg = decltype(S)::value, k = sg >> 1, j = sg & 1;
        if constexpr ((sg & 7) == 0) { wait_lgkm0(); __builtin_amdgcn_sched_barrier(0); }
        acc[t][j] = mfma_bf16(wv[k], xf[k][j], acc[t][j]);
        if constexpr ((sg & 7) < 4 && (sg >> 3) < 3) {
          constexpr int kk = 4 + (sg >> 3) * 4 + (sg & 3);
          wv[kk] = lw128(std::integral_constant<int, f_tile(t) + kk * 1024>{});
        }
        constexpr int it = sg - (t == 6 ? 4 : 0), pr = it / 8, st = it % 8;
        if constexpr (it >= 0 && pr < ne && st < 6) {
          const f32x4 cv = cs[pr];
#pragma unroll
          for (int jj = 0; jj < 2; ++jj) {
            const int a = 2 * pr + jj;
            const f32x4 av = acc[e0 + pr][jj];
            if constexpr (st == 0) {
              const f32x2 nm = {-lm[jj], -lm[jj]};
              ga[a] = __builtin_elementwise_fma(nm, f32x2{cv[0], cv[1]}, f32x2{av[0], av[1]});
              ua[a] = __builtin_elementwise_fma(nm, f32x2{cv[2], cv[3]}, f32x2{av[2], av[3]});
            } else if constexpr (st == 1) {
              ga[a] = ga[a] * f32x2{lc[jj], lc[jj]};
            } else if constexpr (st == 2) {
              ga[a] = f32x2{__builtin_amdgcn_exp2f(ga[a][0]), __builtin_amdgcn_exp2f(ga[a][1])};
            } else if constexpr (st == 3) {
              ga[a] = __builtin_elementwise_fma(ga[a], f32x2{ls[jj], ls[jj]}, f32x2{ls[jj], ls[jj]});
            } else if constexpr (st == 4) {
              ga[a] = f32x2{__builtin_amdgcn_rcpf(ga[a][0]), __builtin_amdgcn_rcpf(ga[a][1])};
            } else {
              ua[a] = ua[a] * ga[a];
            }
          }
        }
        if constexpr (it >= 0 && pr < ne && st == 6) {
#pragma unroll
          for (int jj = 0; jj < 2; ++jj) hv[e0 + pr][jj] = pk_bf16(ua[2 * pr + jj][0], ua[2 * pr + jj][1]);
        }
        if constexpr (t == 6 && sg == 0) consts(0);
        if constexpr (t == 6 && sg == 2) consts(1);
        if constexpr (sg == 28) {
          if constexpr (t == 6) hstore(0);
          if constexpr (t == 7) hstore(2);
          if constexpr (t == 8) hstore(6);
          if constexpr (t == 10) hstore(8);
        }
        if constexpr (sg == 30 && t == 7) hstore(4);
        __builtin_amdgcn_sched_barrier(0);
      });
      wait_lgkm0();
      if constexpr (t == 8) wait_vm<1>();
      bar();
      __builtin_amdgcn_sched_barrier(0);
    });
    TSL(8)
    constexpr int WIN = F_WIN;
    const u32 ho = hoff + (rb64 * NKF * 4 + mq) * 1024 + lane * 16;
    u32x4 hf[NKF];
    {
      const f32x4 cs = __builtin_bit_cast(f32x4, lds_rd128(csa + 10 * 64));
#pragma unroll
      for (int j = 0; j < 2; ++j) hv[10][j] = glu_tile(acc[10][j], cs, lm[j], ls[j], lc[j]);
      hstore(10);
    }
    __builtin_amdgcn_sched_barrier(0);
#pragma unroll
    for (int u = 0; u < F_WIN0; ++u) gload16_sc1(hf[u], ho, ws + u * 4096);
    f32x4 ac2[4] = {{0, 0, 0, 0}, {0, 0, 0, 0}, {0, 0, 0, 0}, {0, 0, 0, 0}};
    u32x4 wf2[2][4];
    auto rd2 = [&](auto U) {
      constexpr int u = decltype(U)::value;
      static_for<0, 4>([&](auto I) {
        constexpr int i = decltype(I)::value;
        wf2[u & 1][i] = lw128(std::integral_constant<int, f_wd(u >> 1) + (u & 1) * 4096 + i * 1024>{});
      });
    };
    rd2(std::integral_constant<int, 0>{});
    __builtin_amdgcn_sched_barrier(0);
    constexpr auto issued = [](int u) {
      int n = u == 0 ? F_WIN0 : u + WIN;
      if (u <= F_UB && n > 24) n = 24;
      return n > NKF ? NKF : n;
    };
    static_for<0, NKF>([&](auto U) {
      constexpr int u = decltype(U)::value;
      asm volatile("s_waitcnt vmcnt(%1)" : "+v"(hf[u]) : "n"(issued(u) - 1 - u) : "memory");
      if constexpr (u == 0) { bar(); TSL(9) }
      if constexpr (u == F_UB) { wait_lgkm0(); bar(); TSL(10) }
      if constexpr (u == 37) bar();
      __builtin_amdgcn_sched_barrier(0);
#pragma unroll
      for (int v = issued(u); v < issued(u + 1); ++v) gload16_sc1(hf[v], ho, ws + v * 4096);
      if constexpr (u + 1 < NKF) rd2(std::integral_constant<int, u + 1>{});
#pragma unroll
      for (int i = 0; i < 4; ++i) ac2[i] = mfma_bf16(wf2[u & 1][i], hf[u], ac2[i]);
#pragma unroll
      for (int k = 0; k < 4; ++k) {
        __builtin_amdgcn_sched_group_barrier(0x008, 1, 0);
        __builtin_amdgcn_sched_group_barrier(0x100, 1, 0);
      }
      __builtin_amdgcn_sched_barrier(0);
    });
    TSL(11)
    asm volatile("" : "+v"(lane));
    {
    const int col = lane & 15, g = lane >> 4, q = (lane & 15) >> 2, p = lane & 3;
    const int l = rb64 * 64 + mq * 16 + col;
    u32x2 ov[4];
#pragma unroll
    for (int i = 0; i < 4; ++i) {
      const u32x2 rv = lds_tr32(lds0 + F_RES + (i * 16 + 4 * g + q) * 144 + mq * 32 + p * 8);
      const u32 o0 = pk_bf16(ac2[i][0], ac2[i][1]), o1 = pk_bf16(ac2[i][2], ac2[i][3]);
      ov[i] = u32x2{pk_bf16(bflo(rv[0]) + bflo(o0), bfhi(rv[0]) + bfhi(o0)),
                    pk_bf16(bflo(rv[1]) + bflo(o1), bfhi(rv[1]) + bfhi(o1))};
    }
    if (layer == 0) {
      lds_bar();
      float sa = 0.f, sq = 0.f;
#pragma unroll
      for (int i = 0; i < 4; ++i)
#pragma unroll
        for (int e = 0; e < 2; ++e) { sa = dot2bf(ov[i][e], 0x3f803f80u, sa); sq = dot2bf(ov[i][e], ov[i][e], sq); }
      sa = sum_groups(sa); sq = sum_groups(sq);
      __builtin_amdgcn_raw_buffer_store_b128(u32x4{__float_as_uint(sa), tag, __float_as_uint(sq), tag}, rsw,
                                             g == 0 ? OFF_PART + (l * 8 + ct) * 16 : 0x7ffffff0u, 0, 16);
#pragma unroll
      for (int i = 0; i < 4; ++i) {
        const u32 give = (col & 1) ? ov[i][0] : ov[i][1];
        const u32 got = (u32)__builtin_amdgcn_update_dpp(0, (int)give, 0xB1, 0xf, 0xf, true);
        const u32x4 gv = {(col & 1) ? got : ov[i][0], tag, (col & 1) ? ov[i][1] : got, tag};
        const u32 go = OFF_XG + XG_SZ + (ct * 32 + i * 8 + g * 2 + (col & 1)) * XG_ROW + (l & ~1) * 8;
        __builtin_amdgcn_raw_buffer_store_b128(gv, rsw, go, 0, 16);
      }
    } else {
#pragma unroll
      for (int i = 0; i < 4; ++i) *(u32x2*)(A.out + (size_t)l * SD + ct * 64 + i * 16 + 4 * g) = ov[i];
    }
    }
#pragma unroll
    for (int j = 0; j < 4; ++j) { gr[j] = u32x4{0, 0, 0, 0}; xg[j] = u32x4{0, 0, 0, 0}; }
    TSL(12)
    TSL_FLUSH
  }
  if (b == 0 && tid == 0) __hip_atomic_store((gu32*)(ws + OFF_EPOCH), tag, __ATOMIC_RELAXED, __HIP_MEMORY_SCOPE_AGENT);
}

DEV void publish_ln(char* ws, u32 off, float sum, float sumsq, u32 tag, int b, int lane) {
  float m, sd;
  ln_consts(sum8(sum), sum8(sumsq), m, sd);
  const u32 val = (__float_as_uint(m) >> 16) | (__float_as_uint(sd) & 0xffff0000u);
  if ((lane & 7) == 0)
    __hip_atomic_store((gu64*)(ws + off) + (lane >> 4) * 512 + 2 * b + ((lane >> 3) & 1),
                       ((unsigned long long)tag << 32) | val, __ATOMIC_RELAXED, __HIP_MEMORY_SCOPE_AGENT);
}

DEV void helper_waves(const Args& A, char* smem, int tid, int lw, int b KIDP) {
  TS_DECL
  char* ws = A.ws;
  const int lane = tid & 63;
  const u32 lds0 = (u32)(uintptr_t)smem;
  const auto rsw = __builtin_amdgcn_make_buffer_rsrc(ws, 0, (int)WS_USED, 0x00020000);
  const int kr = lane >> 3, no = lane & 7;
  u32 tag;
  {
    char* img = smem + KG_PACK + (lw - 1) * PK_IMG;
    if (lw <= 1) {
      const u16* src = lw == 0 ? A.x + (size_t)(8 * b + kr) * SD + no * 8
                               : A.x + (size_t)(64 * (b >> 3) + kr) * SD + (b & 7) * 64 + no * 8;
      const int stp = lw == 0 ? 64 : 8 * SD;
      u32x4 v[8];
#pragma unroll
      for (int i = 0; i < 8; ++i) v[i] = *(const u32x4*)(src + i * stp);
      tag = __builtin_amdgcn_raw_buffer_load_b32(rsw, OFF_EPOCH, 0, 16);
      if (lw == 0 && b == 0) {
#pragma unroll
        for (int i = 0; i < 4; ++i) __builtin_amdgcn_raw_buffer_store_b32(0u, rsw, OFF_CNT + (i * 64 + lane) * 128, 0, 16);
      }
      __builtin_amdgcn_sched_barrier(0);
      bar();
      __builtin_amdgcn_sched_barrier(0);
      if (lw == 1) {
        tag += 1u;
        if (lane == 0) { lds_st(lds0 + L_MISC + 4, tag); lds_st(lds0 + L_MISC, 1u); }
#pragma unroll
        for (int i = 0; i < 8; ++i)
          *(u32x4*)(img + (8 * i + kr) * 128 + ((no ^ ((4 * i + (kr >> 1)) & 7)) * 16)) = v[i];
        wait_lgkm0();
        asm volatile("" ::: "memory");
        const int pq = kr & 3, hi = lane >> 5;
        const char* rp = img + (no + 8 * hi) * 256 + pq * 4;
        const u32 go = OFF_XG + ((b & 7) * 32 + pq) * XG_ROW + (b >> 3) * 512 + (no + 8 * hi) * 16;
#pragma unroll
        for (int i = 0; i < 16; ++i) {
          const char* a = rp + (((i & 7) ^ no) * 16) + (i >> 3) * 4096;
          const u32 r0 = *(const u32*)a, r1 = *(const u32*)(a + 128);
          __builtin_amdgcn_raw_buffer_store_b128(u32x4{r0, tag, r1, tag}, rsw, go + (i & 7) * 4 * XG_ROW + (i >> 3) * 256, 0, 16);
        }
        asm volatile("" ::: "memory");
      } else {
        float a[2] = {0.f, 0.f}, q[2] = {0.f, 0.f};
#pragma unroll
        for (int i = 0; i < 8; ++i)
#pragma unroll
          for (int j = 0; j < 4; ++j) {
            a[j & 1] = dot2bf(v[i][j], 0x3f803f80u, a[j & 1]);
            q[j & 1] = dot2bf(v[i][j], v[i][j], q[j & 1]);
          }
        lds_wait(lds0 + L_MISC, 1u);
        tag = lds_ld(lds0 + L_MISC + 4);
        wait_vm<0>();
        publish_ln(ws, OFF_GRAN, a[0] + a[1], q[0] + q[1], tag, b, lane);
      }
    } else {
      bar();
    }
    const int id = (3 - lw) * 256 + b;
    const int col = lane & 15, g = lane >> 4, q = (lane & 15) >> 2, p = lane & 3;
    const u32 ra = (u32)(uintptr_t)img + (8 * g + q) * 256 + 8 * p;
    const int rkey = q | ((g & 1) << 2);
    if (id < 352) {
      const int ps = id & 15, ng = id >> 4;
      const size_t so = (size_t)(32 * ps + kr) * SF + 8 * (8 * ng + no < 172 ? 8 * ng + no : 0);
      u32x4 gv[4], uv[4];
#pragma unroll
      for (int i = 0; i < 4; ++i) {
        gv[i] = *(const u32x4*)(A.wg + so + (size_t)i * 8 * SF);
        uv[i] = *(const u32x4*)(A.wu + so + (size_t)i * 8 * SF);
      }
#pragma unroll
      for (int i = 0; i < 4; ++i) {
        char* dst = img + (8 * i + kr) * 256 + ((no ^ ((kr & 3) | ((i & 1) << 2))) * 32);
        *(u32x4*)dst = u32x4{gv[i][0], uv[i][0], gv[i][1], uv[i][1]};
        *(u32x4*)(dst + 16) = u32x4{gv[i][2], uv[i][2], gv[i][3], uv[i][3]};
      }
      wait_lgkm0();
      asm volatile("" ::: "memory");
      f32x4 cs = {0, 0, 0, 0};
#pragma unroll
      for (int t8 = 0; t8 < 8; ++t8) {
        const u32x2 lo = lds_tr32(ra + ((t8 ^ rkey) * 32)), hi = lds_tr32(ra + ((t8 ^ rkey) * 32) + 1024);
        const u32x4 fr = {lo[0], lo[1], hi[0], hi[1]};
        const int Tt = 8 * ng + t8;
        __builtin_amdgcn_raw_buffer_store_b128(fr, rsw, lane * 16 + OFF_WGU + wgu_off(Tt / 11, ps, Tt % 11), 0, 16);
        const u32 sel = col == t8 ? 0x3f803f80u : 0u;
        cs = mfma_bf16(fr, u32x4{sel, sel, sel, sel}, cs);
      }
      __builtin_amdgcn_raw_buffer_store_b128(__builtin_bit_cast(u32x4, cs), rsw,
                                             col < 8 ? OFF_CSP + ((ps * 176 + 8 * ng + col) * 16 + g * 4) * 4 : 0x7ffffff0u, 0, 16);
    } else if (id < 352 + 8 * NKF) {
      const int ct = (id - 352) & 7, kp = (id - 352) >> 3;
      u32x4 gv[4];
#pragma unroll
      for (int i = 0; i < 4; ++i)
        gv[i] = *(const u32x4*)(A.wd + (size_t)(8 * tile_of_oct(4 * kp + i) + kr) * SD + ct * 64 + no * 8);
#pragma unroll
      for (int i = 0; i < 4; ++i)
        *(u32x4*)(img + (8 * i + kr) * 256 + (((no >> 1) ^ ((kr & 3) | ((i & 1) << 2))) * 32) + (no & 1) * 16) = gv[i];
      wait_lgkm0();
      asm volatile("" ::: "memory");
#pragma unroll
      for (int nt = 0; nt < 4; ++nt) {
        const u32x2 lo = lds_tr32(ra + ((nt ^ rkey) * 32)), hi = lds_tr32(ra + ((nt ^ rkey) * 32) + 1024);
        __builtin_amdgcn_raw_buffer_store_b128(u32x4{lo[0], lo[1], hi[0], hi[1]}, rsw,
                                               lane * 16 + OFF_WD + ((ct * NKF + kp) * 4 + nt) * 1024, 0, 16);
      }
    }
    wait_vm<0>();
  }
  TS(1)
  const int xcd = b & 7, idx = b >> 3;
  const int rb = (xcd & 1) * 8 + (idx & 7), c = (xcd >> 1) * 4 + (idx >> 3);
  const int rb64 = xcd * 4 + (idx & 3), ct = idx >> 2;
  const u32 wbase = OFF_WGU + c * (176 * 1024) + lane * 16;
  const u32 wdo = OFF_WD + (ct * NKF * 4 + lw * 2) * 1024 + lane * 16;
  u32x4 kimg[5];
  const u32 ko = lds0 + (lw * 64 + lane) * 16;

#pragma unroll 1
  for (int layer = 0; layer < 2; ++layer) {
    asm volatile("" : "+s"(ws));
    auto stage_w1 = [&]() {
#pragma unroll
      for (int i = 0; i < 6; ++i) dma4(wbase + lw * 24576, ws + i * 4096, lds0 + F_W1 + lw * 24576 + i * 4096);
    };
    if (layer == 1) {
#pragma unroll
      for (int i = 0; i < 5; ++i)
        if (i < 4 || tid < 256 + 32) *(LAS u32x4*)(ko + i * 4096) = kimg[i];
      if (lw == 0) {
        u32x4 pv;
        __builtin_amdgcn_s_sleep(GR_DELAY);
        for (int sp = 0; sp < (1 << 18); ++sp) {
          pv = __builtin_amdgcn_raw_buffer_load_b128(rsw, OFF_PART + b * 1024 + lane * 16, 0, 16);
          if (__builtin_amdgcn_ballot_w64(((pv[1] ^ tag) | (pv[3] ^ tag)) != 0u) == 0ull) break;
          __builtin_amdgcn_s_sleep(3);
        }
        publish_ln(ws, OFF_GRAN + SL * 8, __uint_as_float(pv[0]), __uint_as_float(pv[2]), tag, b, lane);
        if (lane == 0) lds_st(lds0 + L_MISC + 8, 3u);
        TSL(8)
      }
    }
    lds_bar();
    if (layer == 1) stage_w1();
    if (layer == 0) {
#pragma unroll
      for (int i = 0; i < 5; ++i)
        if (i < 4 || tid < 256 + 32) kimg[i] = lds_rd128(ko + i * 4096);
      if (lw == 0) {
        grid_signal(ws, 0, b, lane);
        grid_wait(ws, 0, b, lane);
        if (lane == 0) lds_st(lds0 + L_MISC + 28, 1u);
      } else {
        lds_wait_slow(lds0 + L_MISC + 28, 1u);
      }
      TS(2)
      stage_w1();
#pragma unroll
      for (int r = 0; r < 2; ++r) {
        const int f4 = min(11 * lw + r * 8 + kr, 43);
        const u32 co = OFF_CSP + (no * 176 + c * 11) * 64 + f4 * 16;
        const f32x4 cv = __builtin_bit_cast(f32x4, __builtin_amdgcn_raw_buffer_load_b128(rsw, co, 0, 16)) +
                         __builtin_bit_cast(f32x4, __builtin_amdgcn_raw_buffer_load_b128(rsw, co + 8 * 176 * 64, 0, 16));
        const f32x4 v = {sum8(cv[0]), sum8(cv[1]), sum8(cv[2]), sum8(cv[3])};
        if (no == 0) *(f32x4*)(smem + L_CS + f4 * 16) = v;
      }
      wait_lgkm0();
    }
    wait_vm<0>();
    TSL(3)
    if (lw == 0) {
      lds_wait_slow(lds0 + L_MISC + 24, 4u * (layer + 1));
      grid_wait(ws, 1 + layer, b, lane);
      if (lane == 0) lds_st(lds0 + L_MISC + 28, 2u + layer);
    } else {
      lds_wait_slow(lds0 + L_MISC + 28, 2u + layer);
    }
    TSL(4)
    const u32 xin = OFF_X + layer * X_SZ;
    u32 vo[2];
#pragma unroll
    for (int o = 0; o < 2; ++o) {
      const int seg = ((lane & 15) >> 1) ^ ((lane >> 4) | (o << 2));
      vo[o] = xin + rb * (SD * 256) + (lw * 16 + (lane >> 4)) * 256 + seg * 32 + (lane & 1) * 16;
    }
    auto stage = [&](int s) {
      asm volatile(
          "s_mov_b32 m0, %3\n\ts_nop 0\n\tglobal_load_lds_dwordx4 %0, %2 sc1\n\tglobal_load_lds_dwordx4 %0, %2 offset:1024 sc1\n\t"
          "global_load_lds_dwordx4 %1, %2 offset:2048 sc1\n\tglobal_load_lds_dwordx4 %1, %2 offset:3072 sc1"
          ::"v"(vo[0]), "v"(vo[1]), "s"(ws + s * 16384), "s"(lds0 + (s % 3) * 16384 + lw * 4096) : "memory");
    };
    auto htile = [&](int h) { dma2(wbase + lw * 2048, ws + (96 + h * 8) * 1024, lds0 + F_W1 + h * 8192 + lw * 2048); };
    auto wdp = [&](int u) { dma2(wdo, ws + u * 8192, lds0 + f_wd(u) + lw * 2048); };
    stage(0); stage(1);
    static_for<0, 8>([&](auto I) {
      constexpr int i = decltype(I)::value;
      wait_vm<(i < 3 ? 4 : i == 3 ? 6 : 8)>();
      bar();
      if constexpr (i < 6) stage(i + 2);
      if constexpr (i >= 2 && i < 6) htile(i - 2);
      if constexpr (i >= 6) { htile(3 * i - 14); htile(3 * i - 13); htile(3 * i - 12); }
    });
    wait_vm<12>();
    bar();
#pragma unroll
    for (int u = 0; u < 6; ++u) wdp(u);
    bar();
#pragma unroll
    for (int u = 6; u < 11; ++u) wdp(u);
    wait_vm<30>();
    bar();
    wdp(11); wdp(12);
    wait_vm<30>();
    bar();
    gu32* ca = (gu32*)(ws + OFF_CNT) + (192 + layer * 32) * 32;
    if (lw == 0 && lane == 0) __hip_atomic_fetch_add(ca + rb * 64, 1u, __ATOMIC_RELAXED, __HIP_MEMORY_SCOPE_AGENT);
    asm volatile("" ::: "memory");
    wdp(13); wdp(14);
    wait_vm<30>();
    bar();
    wdp(15); wdp(16);
    if (lw == 0 && lane == 0) poll_ge(ca + (rb64 >> 1) * 64, 16u);
    wait_vm<10>();
    TSL(5)
    bar();
    wdp(17); wdp(18);
    bar();
    if (lw == 0 && lane == 0) {
      __hip_atomic_fetch_add(ca + rb * 64 + 32, 1u, __ATOMIC_RELAXED, __HIP_MEMORY_SCOPE_AGENT);
      poll_ge(ca + (rb64 >> 1) * 64 + 32, 16u);
    }
    wait_vm<0>();
    TSL(6)
    bar();
    wdp(19); wdp(20);
    dma2(wdo - (lw >= 2 ? 4096 : 0), ws + 21 * 8192, lds0 + f_wd(21) + lw * 2048);
    {
      const u32 ro = xin + ((rb64 >> 1) * SD + ct * 64 + lw * 16 + kr) * 256 + (rb64 & 1) * 128 + no * 16;
      const u32x4 r0 = __builtin_amdgcn_raw_buffer_load_b128(rsw, ro, 0, 16);
      const u32x4 r1 = __builtin_amdgcn_raw_buffer_load_b128(rsw, ro + 2048, 0, 16);
      char* rp = smem + F_RES + (lw * 16 + kr) * 144 + no * 16;
      *(u32x4*)rp = r0;
      *(u32x4*)(rp + 8 * 144) = r1;
    }
    wait_vm<0>();
    lds_bar();
    TSL(7)
    TSL_FLUSH
    if (layer == 0) bar();
  }
}

__global__ __launch_bounds__(512, 2) void k_all(Args A KIDP) {
  extern __shared__ __attribute__((aligned(16))) char smem[];
  const int tid = threadIdx.x, w = __builtin_amdgcn_readfirstlane(tid >> 6), b = blockIdx.x;
  if (w < 4) compute_waves(A, smem, tid, w, b KIDA(kid_));
  else helper_waves(A, smem, tid, w - 4, b KIDA(kid_));
}

extern "C" void kernel_launch(void* const* d_in, const int* in_sizes, int n_in, void* d_out, int out_size, void* d_ws,
                              size_t ws_size, hipStream_t stream) {
  Args A;
  A.x = (const u16*)d_in[0];
  A.logA = (const u16*)d_in[1];
  A.Aim = (const u16*)d_in[2];
  A.Cre = (const u16*)d_in[3];
  A.Cim = (const u16*)d_in[4];
  A.logdt = (const u16*)d_in[5];
  A.dskip = (const u16*)d_in[6];
  A.wg = (const u16*)d_in[8];
  A.wu = (const u16*)d_in[9];
  A.wd = (const u16*)d_in[10];
  A.out = (u16*)d_out;
  A.ws = (char*)d_ws;
  (void)hipFuncSetAttribute((const void*)k_all, hipFuncAttributeMaxDynamicSharedMemorySize, LDS_BYTES);
  k_all<<<256, 512, LDS_BYTES, stream>>>(A KIDA(0));
}
```

```cpp
#include <hip/hip_runtime.h>
#include <hip/hip_bf16.h>
#include <stdint.h>
#include <type_traits>

typedef unsigned short u16;
typedef unsigned int u32;
typedef __attribute__((ext_vector_type(8))) __bf16 bf16x8;
typedef __attribute__((ext_vector_type(2))) __bf16 bf16x2;
typedef __attribute__((ext_vector_type(8))) _Float16 f16x8;
typedef __attribute__((ext_vector_type(2))) _Float16 f16x2;
typedef __attribute__((ext_vector_type(4))) float f32x4;
typedef __attribute__((ext_vector_type(2))) float f32x2;
typedef __attribute__((ext_vector_type(4))) u32 u32x4;
typedef __attribute__((ext_vector_type(2))) u32 u32x2;
typedef __attribute__((ext_vector_type(4))) short s16x4;
typedef __attribute__((ext_vector_type(2))) double f64x2;
#define LAS __attribute__((address_space(3)))
#define DEV __device__ __forceinline__

#define TSL(i)
#define TSL_FLUSH
#define KIDP
#define KIDA(n)
#define TSP
#define TSA
#define TS_DECL
#define TS(i)
#define TS_FLUSH

constexpr int SL = 2048, SD = 512, SN = 128, SF = 1376;
constexpr int NKF = 43;
constexpr int KIMG_CH = 8448;
constexpr int KIMG_C1 = 4224;
constexpr int XG_ROW = SL * 8 + 256;

constexpr u32 OFF_WGU = 0;
constexpr u32 OFF_WD = OFF_WGU + 16 * 176 * 1024;
constexpr u32 OFF_CSP = OFF_WD + 8 * NKF * 4096;
constexpr u32 OFF_X = OFF_CSP + 16 * 176 * 64;
constexpr u32 X_SZ = SL * SD * 2;
constexpr u32 OFF_H = OFF_X + 2 * X_SZ;
constexpr u32 H_SZ = 32 * NKF * 4096;
constexpr u32 OFF_GRAN = OFF_H + 2 * H_SZ;
constexpr u32 GR_SZ = SL * 8 + 256;
constexpr u32 OFF_XG = OFF_GRAN + 16 * GR_SZ;
constexpr u32 XG_SZ = 256 * XG_ROW;
constexpr u32 OFF_PART = OFF_XG + 2 * XG_SZ;
constexpr u32 OFF_CNT = OFF_PART + SL * 128;
constexpr u32 OFF_EPOCH = OFF_CNT + 640 * 128;
constexpr u32 WS_USED = OFF_EPOCH + 256;

DEV float bflo(u32 v) { return __uint_as_float(v << 16); }
DEV float bfhi(u32 v) { return __uint_as_float(v & 0xffff0000u); }
DEV float bf2f(u16 v) { return __uint_as_float((u32)v << 16); }
DEV u32 pk_bf16(float a, float b) {
  f32x2 v = {a, b};
  return __builtin_bit_cast(u32, __builtin_convertvector(v, bf16x2));
}
DEV u32 pk_f16(float a, float b) {
  f32x2 v = {a, b};
  return __builtin_bit_cast(u32, __builtin_convertvector(v, f16x2));
}
DEV float bfr(float a) { return bflo(pk_bf16(a, a)); }
DEV float dot2bf(u32 a, u32 b, float c) {
  return __builtin_amdgcn_fdot2_f32_bf16(__builtin_bit_cast(bf16x2, a), __builtin_bit_cast(bf16x2, b), c, false);
}
DEV f32x4 mfma_bf16(u32x4 a, u32x4 b, f32x4 c) {
  return __builtin_amdgcn_mfma_f32_16x16x32_bf16(__builtin_bit_cast(bf16x8, a), __builtin_bit_cast(bf16x8, b), c, 0, 0, 0);
}
DEV f32x4 mfma_f16(u32x4 a, u32x4 b, f32x4 c) {
  return __builtin_amdgcn_mfma_f32_16x16x32_f16(__builtin_bit_cast(f16x8, a), __builtin_bit_cast(f16x8, b), c, 0, 0, 0);
}
DEV u32x2 lds_tr(const char* p) {
  return __builtin_bit_cast(u32x2, __builtin_amdgcn_ds_read_tr16_b64_v4i16((LAS s16x4*)p));
}
DEV u32x2 lds_tr32(u32 a) {
  return __builtin_bit_cast(u32x2, __builtin_amdgcn_ds_read_tr16_b64_v4i16((LAS s16x4*)a));
}
DEV u32x4 lds_rd128(u32 a) { return *(const LAS u32x4*)a; }
template <int CTRL>
DEV float dpp_f(float v) {
  return __builtin_bit_cast(float, __builtin_amdgcn_update_dpp(0, __builtin_bit_cast(int, v), CTRL, 0xf, 0xf, true));
}
DEV float sum8(float v) {
  v += dpp_f<0xB1>(v);
  v += dpp_f<0x4E>(v);
  v += dpp_f<0x141>(v);
  return v;
}
DEV float sum16(float v) { v = sum8(v); return v + dpp_f<0x140>(v); }
DEV void ln_consts(float sum, float sumsq, float& m, float& s) {
  float mean = sum * (1.0f / SD);
  float var = fmaxf(sumsq * (1.0f / SD) - mean * mean, 0.0f);
  m = bfr(mean);
  s = bfr(__builtin_amdgcn_sqrtf(bfr(bfr(var) + 1e-5f)));
}

typedef f32x2 cpx;
DEV cpx cmul(cpx a, cpx b) {
  const f32x2 t = a * f32x2{b[0], b[0]};
  return __builtin_elementwise_fma(f32x2{a[1], a[0]}, f32x2{-b[1], b[1]}, t);
}
DEV cpx cmulc(cpx a, cpx b) {
  const f32x2 t = a * f32x2{b[0], b[0]};
  return __builtin_elementwise_fma(f32x2{a[1], a[0]}, f32x2{b[1], -b[1]}, t);
}
DEV float exp_acc(float x) {
  const float t = x * 1.44269504088896340736f;
  const float e = fmaf(x, 1.44269504088896340736f, -t) + x * 1.92596299e-8f;
  const float r = __builtin_amdgcn_exp2f(t);
  return fmaf(r, e * 0.69314718056f, r);
}

typedef __attribute__((address_space(1))) u32 gu32;
typedef __attribute__((address_space(1))) unsigned long long gu64;
template <int N>
DEV void wait_vm() { asm volatile("s_waitcnt vmcnt(%0)" ::"n"(N) : "memory"); }
DEV void wait_lgkm0() { __builtin_amdgcn_s_waitcnt(0xC07F); }
DEV void bar() {
  asm volatile("" ::: "memory");
  __builtin_amdgcn_s_barrier();
  asm volatile("" ::: "memory");
}
template <int N>
DEV void wait_lgkm() { __builtin_amdgcn_s_waitcnt(0xC07F | (N << 8)); }
DEV void lds_bar() { wait_lgkm0(); bar(); }
template <int I, int N, class Fn>
DEV void static_for(Fn&& f) {
  if constexpr (I < N) { f(std::integral_constant<int, I>{}); static_for<I + 1, N>(f); }
}
DEV int wgu_off(int c, int s, int t) { return (c * 176 + (t < 6 ? s * 6 + t : 96 + (t - 6) * 16 + s)) << 10; }
DEV int tile_of_oct(int oct) {
  return oct < 96 ? (oct / 6) * 11 + oct % 6
       : oct < 127 ? ((oct - 96) >> 1) * 11 + 6 + ((oct - 96) & 1) : ((oct - 127) / 3) * 11 + 8 + (oct - 127) % 3;
}
DEV float s_fma(float a, float b, float c) { float d; asm("v_fma_f32 %0, %1, %2, %3" : "=v"(d) : "v"(a), "v"(b), "v"(c)); return d; }
DEV float s_nfma(float a, float b, float c) { float d; asm("v_fma_f32 %0, -%1, %2, %3" : "=v"(d) : "v"(a), "v"(b), "v"(c)); return d; }
DEV float s_mul(float a, float b) { float d; asm("v_mul_f32 %0, %1, %2" : "=v"(d) : "v"(a), "v"(b)); return d; }

constexpr int CV_U = 2 * KIMG_CH;
constexpr int CV_UROW = (128 + 1024) * 2;
constexpr int CV_P = CV_U + 4 * CV_UROW;
constexpr int CV_PROW = 2048 * 4 + 64 * 16 + 16;
constexpr int L_MISC = CV_P + 4 * CV_PROW;
constexpr int L_CS = L_MISC + 64;
constexpr int KG_CH = 64 * 512;
constexpr int KG_TAB = KG_CH + 53 * 512;
constexpr int KG_TROW = 672 * 2;
constexpr int KG_PACK = 65536;
constexpr int PK_IMG = 8192;
constexpr int F_W1 = 65536;
constexpr int LDS_BYTES = 163840;
constexpr int F_BASE = 57344;
static_assert(L_CS + 704 <= 65536 && KG_TAB + 2 * KG_TROW <= L_MISC, "lds map");
constexpr int f_tile(int t) { return F_W1 + (t - 6) * 16384; }
constexpr int f_wd(int u) {
  return u < 7 ? u * 8192 : u < 9 ? F_W1 + 81920 + (u - 7) * 8192 : u < 19 ? F_W1 + (u - 9) * 8192 : f_wd(u - 19);
}
constexpr int F_RES = f_wd(4);

DEV int fresh_lane() {
  int l;
  asm volatile("v_mbcnt_lo_u32_b32 %0, -1, 0\n\tv_mbcnt_hi_u32_b32 %0, -1, %0" : "=v"(l));
  return l;
}
DEV u32 opq(u32 v) { asm volatile("" : "+v"(v)); return v; }
DEV int chan_pair(int b) { return (b & 7) * 32 + (b >> 3); }
DEV u32 lds_ld(u32 a) { return *(volatile LAS u32*)a; }
DEV void lds_st(u32 a, u32 v) { *(volatile LAS u32*)a = v; }
DEV u32 lds_arrive(u32 word, int lane) {
  wait_lgkm0();
  asm volatile("" ::: "memory");
  u32 old = 0;
  if (lane == 0) old = __hip_atomic_fetch_add((LAS u32*)word, 1u, __ATOMIC_RELAXED, __HIP_MEMORY_SCOPE_WORKGROUP);
  return __builtin_amdgcn_readfirstlane(old);
}
DEV void lds_post(u32 word, int lane) {
  wait_lgkm0();
  asm volatile("" ::: "memory");
  if (lane == 0) (void)__hip_atomic_fetch_add((LAS u32*)word, 1u, __ATOMIC_RELAXED, __HIP_MEMORY_SCOPE_WORKGROUP);
}
DEV void lds_wait(u32 word, u32 want) {
  for (int sp = 0; sp < (1 << 24); ++sp)
    if (lds_ld(word) >= want) break;
  asm volatile("" ::: "memory");
}
DEV void lds_wait_slow(u32 word, u32 want) {
  for (int sp = 0; sp < (1 << 20); ++sp) {
    if (lds_ld(word) >= want) break;
    __builtin_amdgcn_s_sleep(2);
  }
  asm volatile("" ::: "memory");
}
#define XG_AUX 16
#define GR_AUX 16
#define TLB_WARM 1
#define TLB_STRIDE 65536
#define TLB_N 16
#define FL_DELAY 10
#define G_REP 8
#define G_SH 8
#define GW_SLEEP 0
#define GR_DELAY2 6
#define X_DELAY 16
#define HPRIO 3
#define PF_DELAY 16
#define PK_DELAY 0
#define GR_SLEEP 8
#define GR_DELAY 8
DEV void grid_signal(char* ws, int k, int b, int lane) {
  if (lane < G_REP)
    __hip_atomic_fetch_add((gu32*)(ws + OFF_CNT) + (128 + k * 256 + lane * G_SH + (b % G_SH)) * 32, 1u, __ATOMIC_RELAXED,
                           __HIP_MEMORY_SCOPE_AGENT);
}
DEV void grid_wait(char* ws, int k, int b, int lane) {
  gu32* p = (gu32*)(ws + OFF_CNT) + (128 + k * 256 + (b % G_REP) * G_SH + (lane % G_SH)) * 32;
  for (int sp = 0; sp < (1 << 20); ++sp) {
    u32 v = 256u / G_SH;
    if (lane < G_SH) v = __hip_atomic_load(p, __ATOMIC_RELAXED, __HIP_MEMORY_SCOPE_AGENT);
    if (__builtin_amdgcn_ballot_w64(v < 256u / G_SH) == 0ull) break;
    __builtin_amdgcn_s_sleep(GW_SLEEP);
  }
  asm volatile("" ::: "memory");
}
DEV void poll_ge(gu32* cp, u32 want) {
  for (int sp = 0; sp < (1 << 20); ++sp) {
    if (__hip_atomic_load(cp, __ATOMIC_RELAXED, __HIP_MEMORY_SCOPE_AGENT) >= want) break;
  }
}
DEV void gload16_sc1(u32x4& dst, u32 voff, const char* base) {
  asm volatile("global_load_dwordx4 %0, %1, %2 sc1" : "=v"(dst) : "v"(voff), "s"(base) : "memory");
}
DEV void dma2(u32 voff, const char* base, u32 lds) {
  asm volatile("s_mov_b32 m0, %2\n\ts_nop 0\n\tglobal_load_lds_dwordx4 %0, %1 sc1\n\t"
               "global_load_lds_dwordx4 %0, %1 offset:1024 sc1" ::"v"(voff), "s"(base), "s"(lds) : "memory");
}
DEV void dma4(u32 voff, const char* base, u32 lds) {
  asm volatile("s_mov_b32 m0, %2\n\ts_nop 0\n\tglobal_load_lds_dwordx4 %0, %1 sc1\n\t"
               "global_load_lds_dwordx4 %0, %1 offset:1024 sc1\n\tglobal_load_lds_dwordx4 %0, %1 offset:2048 sc1\n\t"
               "global_load_lds_dwordx4 %0, %1 offset:3072 sc1" ::"v"(voff), "s"(base), "s"(lds) : "memory");
}
DEV cpx cpow2(float a2, float rh, float rl, float s) {
  const float mag = __builtin_amdgcn_exp2f(a2 * s);
  const float rev = fmaf(rl, s, __builtin_amdgcn_fractf(rh * s));
  return f32x2{__builtin_amdgcn_cosf(rev), __builtin_amdgcn_sinf(rev)} * f32x2{mag, mag};
}
DEV u32 glu_tile(f32x4 acc, f32x4 cs, float m, float s, float c1) {
  const f32x2 nm = {-m, -m};
  const f32x2 gg = __builtin_elementwise_fma(nm, f32x2{cs[0], cs[1]}, f32x2{acc[0], acc[1]}) * f32x2{c1, c1};
  const f32x2 uu = __builtin_elementwise_fma(nm, f32x2{cs[2], cs[3]}, f32x2{acc[2], acc[3]});
  const f32x2 ex = {__builtin_amdgcn_exp2f(gg[0]), __builtin_amdgcn_exp2f(gg[1])};
  const f32x2 dn = __builtin_elementwise_fma(ex, f32x2{s, s}, f32x2{s, s});
  const f32x2 hh = uu * f32x2{__builtin_amdgcn_rcpf(dn[0]), __builtin_amdgcn_rcpf(dn[1])};
  return pk_bf16(hh[0], hh[1]);
}
DEV u32x4 tr4(u32 r0, u32 r1, u32 r2, u32 r3) {
  auto a = __builtin_amdgcn_permlane32_swap(r0, r2, false, false);
  auto b = __builtin_amdgcn_permlane32_swap(r1, r3, false, false);
  auto c = __builtin_amdgcn_permlane16_swap(a[0], b[0], false, false);
  auto d = __builtin_amdgcn_permlane16_swap(a[1], b[1], false, false);
  return u32x4{c[0], c[1], d[0], d[1]};
}
DEV float sum_groups(float v) {
  auto a = __builtin_amdgcn_permlane32_swap(__float_as_uint(v), __float_as_uint(v), false, false);
  v = __uint_as_float(a[0]) + __uint_as_float(a[1]);
  auto b = __builtin_amdgcn_permlane16_swap(__float_as_uint(v), __float_as_uint(v), false, false);
  return __uint_as_float(b[0]) + __uint_as_float(b[1]);
}

#define F_WIN 18
#define F_PRE 10
#define F_RATE 2
#define F_U1 8
#define F_U2 12
#define F_U3 24
#define AB_DELAY 8

struct Args {
  const u16 *x, *logA, *Aim, *Cre, *Cim, *logdt, *dskip, *wg, *wu, *wd;
  u16* out;
  char* ws;
};

DEV void conv_mfma(u32 lds0, char* smem, int lane, int w) {
  const int np = lane & 15, h = lane >> 4, q = (lane & 15) >> 2, p = lane & 3;
  u32 ka = lds0 + (w >> 1) * KIMG_CH + ((q & 1) ? KIMG_C1 + 2 * 18 : 2 * 16) + 2 * (48 - 16 * h - 2 * q + 4 * p) - 16;
  u32 ua = lds0 + CV_U + w * CV_UROW + 256 + 2 * (8 * (np + h) - 24) - 192;
  u32 pp = lds0 + CV_P + w * CV_PROW + 16 + np * 64 + h * 16 + (np >> 1) * 16;
  asm volatile("" : "+v"(ka), "+v"(ua), "+v"(pp));
  f32x4 acc[8];
#pragma unroll
  for (int j = 0; j < 8; ++j) acc[j] = f32x4{0, 0, 0, 0};
  u32x4 kf[2][8], uf[2][8];
  constexpr auto pair_i = [](int n) { int i = 0; while (n >= 8 - i) { n -= 8 - i; ++i; } return i; };
  constexpr auto pair_j = [](int n) { int i = 0; while (n >= 8 - i) { n -= 8 - i; ++i; } return i + n; };
  auto rd1 = [&](auto D, auto N) {
    constexpr int dl = decltype(D)::value, n = decltype(N)::value;
    if constexpr (n >= 2 && n < 10) uf[dl & 1][n - 2] = lds_rd128(ua + (n - 2) * 256 + (3 - dl) * 64);
    else {
      constexpr int m = n < 2 ? n : n - 8, i = m >> 1;
      const u32x2 v = lds_tr32(ka + (4 * i + dl) * 128 + ((m & 1) ? 0 : 16));
      kf[dl & 1][i][2 * (m & 1)] = v[0]; kf[dl & 1][i][2 * (m & 1) + 1] = v[1];
    }
  };
  static_for<0, 24>([&](auto N) { rd1(std::integral_constant<int, 0>{}, N); });
  __builtin_amdgcn_sched_barrier(0);
  static_for<0, 4>([&](auto D) {
    constexpr int dl = decltype(D)::value;
    if constexpr (dl == 3) {
      if (lane == 0) __hip_atomic_fetch_add((LAS u32*)(lds0 + L_MISC + 48), 1u, __ATOMIC_RELAXED, __HIP_MEMORY_SCOPE_WORKGROUP);
    }
    if constexpr (dl > 0) wait_lgkm0();
    __builtin_amdgcn_sched_barrier(0);
    static_for<0, 36>([&](auto N) {
      constexpr int n = decltype(N)::value, i = dl < 3 ? pair_i(n) : pair_j(n) - pair_i(n), j = dl < 3 ? pair_j(n) : 7 - pair_i(n);
      acc[j] = mfma_bf16(kf[dl & 1][i], uf[dl & 1][j - i], acc[j]);
      constexpr int r0 = dl == 0 ? 8 : 0;
      if constexpr (dl < 3 && n >= r0 && n < r0 + 24)
        rd1(std::integral_constant<int, dl + 1>{}, std::integral_constant<int, n - r0>{});
      if constexpr (dl == 3 && j < 7 && i == (j < 2 ? j : 2)) *(LAS f32x4*)(pp + (j + 1) * (1024 + 128)) = acc[j + 1];
      __builtin_amdgcn_sched_barrier(0);
    });
  });
  *(LAS f32x4*)pp = acc[0];
}

DEV void compute_waves(const Args& A, char* smem, int tid, int w, int b KIDP) {
  TS_DECL
  char* ws = A.ws;
  int lane = tid & 63;
  const u32 lds0 = (u32)(uintptr_t)smem;
  const auto rsw = __builtin_amdgcn_make_buffer_rsrc(ws, 0, (int)WS_USED, 0x00020000);
  const int d0 = 2 * chan_pair(b);
  const u32 v_dsk = *(const u32*)(A.dskip + d0);
  u32x4 gr[4], xg[4];
  bool early;
  {
    const int ch = w >> 1, n = tid & 127;
    const u32 pi = d0 * SN + tid;
    const u16 l_dt = A.logdt[d0 + ch], l_a = A.logA[pi], l_b = A.Aim[pi];
    const u16 l_cr = A.Cre[pi], l_ci = A.Cim[pi], l_cr2 = A.Cre[pi ^ 64], l_ci2 = A.Cim[pi ^ 64];
    if (tid < 16) *(u32*)(smem + L_MISC + tid * 4) = 0u;
    if (n < 8) *(u32*)(smem + KG_TAB + ch * KG_TROW + n * 4) = 0u;
    __builtin_amdgcn_sched_barrier(0);
    u32 wr[8];
#pragma unroll
    for (int r = 0; r < 8; ++r) { wr[r] = lds0 + ch * KG_CH + r * 512 + ((n * 4) ^ (r << 4)); asm volatile("" : "+v"(wr[r])); }
    const int xt = w & 1, r16 = lane & 15, g = lane >> 4;
    u32 fa[2], fb[2], fc[2];
#pragma unroll
    for (int o = 0; o < 2; ++o) {
      const int rc = min(r16, 4);
      fa[o] = lds0 + ch * KG_CH + (16 * xt + r16) * 512 + (((o * 4 + g) ^ (r16 & 7)) << 4);
      fb[o] = lds0 + ch * KG_CH + (32 + r16) * 512 + (((o * 4 + g) ^ (r16 & 7)) << 4);
      fc[o] = lds0 + ch * KG_CH + (48 + rc) * 512 + (((o * 4 + g) ^ rc) << 4);
      asm volatile("" : "+v"(fa[o]), "+v"(fb[o]), "+v"(fc[o]));
    }
    const int i0 = 16 * xt + 4 * g;
    const int ts2 = r16 < 8 ? 16 : (r16 < 12 ? 8 : 4);
    const int tb = r16 < 8 ? r16 : (r16 < 12 ? 248 + r16 : 384 + 64 * ((r16 >> 1) & 1) + (r16 & 1));
    u32 ta[4];
#pragma unroll
    for (int e = 0; e < 4; ++e) { ta[e] = lds0 + KG_TAB + ch * KG_TROW + 32 + tb * 2 + ts2 * (i0 + e); asm volatile("" : "+v"(ta[e])); }
    u32 tc = lds0 + KG_TAB + ch * KG_TROW + 32 + 2 * ((r16 < 4 || i0 == 0) ? 512 + 32 * min(r16, 4) + i0 : 648);
    asm volatile("" : "+v"(tc));
    u32 ia[10];
    {
      const float f0 = (float)(8 * tid);
#pragma unroll
      for (int j = 0; j < 5; ++j) {
        const u32 bits = pk_bf16(f0 + (float)(2 * j - 2), f0 + (float)(2 * j - 1));
        const u32 lin = lds0 + KG_TAB + (8 * tid + 2 * j - 2 + 16) * 2;
        ia[2 * j] = tid < 24 ? lin : (bits & 0xffffu) * 2 + (lds0 + KG_TAB - (0x4300 - 144) * 2);
        ia[2 * j + 1] = tid < 24 ? lin + 2 : (bits >> 16) * 2 + (lds0 + KG_TAB - (0x4300 - 144) * 2);
        asm volatile("" : "+v"(ia[2 * j]), "+v"(ia[2 * j + 1]));
      }
    }
    __builtin_amdgcn_sched_barrier(0);
    lds_bar();
    __builtin_amdgcn_sched_barrier(0);
    const u32 ea = pk_bf16(exp_acc(bf2f(l_dt)), exp_acc(bf2f(l_a)));
    const float dt = bflo(ea);
    const float a2 = -(bfhi(ea) * dt) * 1.44269504088896340736f;
    const float bi = bf2f(l_b) * dt;
    const float rh = bi * 0.15915494309189533577f;
    const float rl = fmaf(bi, 6.420638e-9f, fmaf(bi, 0.15915494309189533577f, -rh));
    u32 mx = max(max((u32)l_cr & 0x7fffu, (u32)l_ci & 0x7fffu), max((u32)l_cr2 & 0x7fffu, (u32)l_ci2 & 0x7fffu));
    mx = max(mx, (u32)__builtin_amdgcn_update_dpp(0, (int)mx, 0xB1, 0xf, 0xf, true));
    mx = max(mx, (u32)__builtin_amdgcn_update_dpp(0, (int)mx, 0x4E, 0xf, 0xf, true));
    mx = max(mx, (u32)__builtin_amdgcn_update_dpp(0, (int)mx, 0x141, 0xf, 0xf, true));
    mx = max(mx, (u32)__builtin_amdgcn_update_dpp(0, (int)mx, 0x140, 0xf, 0xf, true));
    mx = max(max((u32)__builtin_amdgcn_readlane((int)mx, 0), (u32)__builtin_amdgcn_readlane((int)mx, 16)),
             max((u32)__builtin_amdgcn_readlane((int)mx, 32), (u32)__builtin_amdgcn_readlane((int)mx, 48)));
    const u32 ex = min(max(mx >> 7, 2u), 252u) + 1u;
    const float fs = 2.0f * dt * __uint_as_float(ex << 23), isc = __uint_as_float((254u - ex) << 23);
    cpx z[12];
#pragma unroll
    for (int p = 0; p < 12; p += 3) {
      z[p] = cpow2(a2, rh, rl, (float)(1 << p));
      z[p + 1] = cmul(z[p], z[p]);
      z[p + 2] = cmul(z[p + 1], z[p + 1]);
    }
    {
      cpx V[56];
      auto put = [&](int i) { *(LAS u32*)(wr[i & 7] + (i >> 3) * 4096) = pk_f16(V[i][0], V[i][1]); };
      V[0] = cpx{1.0f, 0.0f};
      V[1] = z[3]; V[2] = z[4]; V[4] = z[5]; V[8] = z[6]; V[16] = z[7];
      V[32] = cpx{bf2f(l_cr) * isc, bf2f(l_ci) * -isc};
      constexpr int OP[47][3] = {
          {3, 1, 4},   {5, 1, 5},   {6, 2, 5},   {33, 32, 0}, {34, 32, 1}, {36, 32, 2}, {40, 32, 8}, {44, 32, 9},
          {7, 3, 5},   {9, 1, 6},   {10, 2, 6},  {11, 3, 6},  {35, 34, 0}, {37, 36, 0}, {38, 36, 1}, {41, 34, 8},
          {12, 4, 6},  {13, 5, 6},  {14, 6, 6},  {15, 7, 6},  {39, 38, 0}, {42, 36, 8}, {43, 38, 8}, {45, 36, 9},
          {17, 1, 7},  {18, 2, 7},  {19, 3, 7},  {20, 4, 7},  {46, 40, 9}, {47, 42, 9}, {48, 32, 10}, {49, 40, 10},
          {21, 5, 7},  {22, 6, 7},  {23, 7, 7},  {24, 8, 7},  {50, 44, 10}, {51, 46, 10}, {52, 32, 11}, {25, 9, 7},
          {26, 10, 7}, {27, 11, 7}, {28, 12, 7}, {29, 13, 7}, {30, 14, 7}, {31, 15, 7}, {0, 0, 0}};
      put(0); put(1); put(2); put(4); put(8); put(16); put(32);
      static_for<0, 13>([&](auto G) {
        constexpr int g4 = decltype(G)::value * 4;
        f32x2 t[4];
#pragma unroll
        for (int k = 0; k < 4; ++k)
          if (g4 + k < 46) t[k] = V[OP[g4 + k][1]] * f32x2{z[OP[g4 + k][2]][0], z[OP[g4 + k][2]][0]};
#pragma unroll
        for (int k = 0; k < 4; ++k)
          if (g4 >= 4 && g4 - 4 + k < 46) put(OP[g4 - 4 + k][0]);
#pragma unroll
        for (int k = 0; k < 4; ++k)
          if (g4 + k < 46) {
            const cpx a = V[OP[g4 + k][1]];
            const float zi = z[OP[g4 + k][2]][1];
            V[OP[g4 + k][0]] = __builtin_elementwise_fma(f32x2{a[1], a[0]}, OP[g4 + k][0] < 32 ? f32x2{-zi, zi} : f32x2{zi, -zi}, t[k]);
          }
        __builtin_amdgcn_sched_barrier(0);
      });
    }
    lds_post(lds0 + L_MISC + 8 + ch * 4, lane);
    lds_wait(lds0 + L_MISC + 8 + ch * 4, 2u);
    {
      f32x4 acc0 = {0, 0, 0, 0}, acc1 = {0, 0, 0, 0};
#pragma unroll
      for (int s = 0; s < 8; ++s) {
        const u32x4 a = lds_rd128(fa[s & 1] + (s >> 1) * 128);
        acc0 = mfma_f16(a, lds_rd128(fb[s & 1] + (s >> 1) * 128), acc0);
        acc1 = mfma_f16(a, lds_rd128(fc[s & 1] + (s >> 1) * 128), acc1);
      }
      early = __builtin_amdgcn_readfirstlane(lds_ld(lds0 + L_MISC + 44)) != 0u;
      if (early) {
#pragma unroll
        for (int j = 0; j < 4; ++j)
          xg[j] = __builtin_amdgcn_raw_buffer_load_b128(rsw, OFF_XG + (d0 >> 1) * XG_ROW + tid * 64 + j * 16, 0, XG_AUX);
#pragma unroll
        for (int j = 0; j < 4; ++j)
          gr[j] = __builtin_amdgcn_raw_buffer_load_b128(rsw, OFF_GRAN + (b & 7) * GR_SZ + tid * 16 + j * 4096, 0, GR_AUX);
      }
      acc0 *= fs; acc1 *= fs;
      const u32 v01 = pk_bf16(acc0[0], acc0[1]), v23 = pk_bf16(acc0[2], acc0[3]);
      *(LAS u16*)ta[0] = (u16)v01; *(LAS u16*)ta[1] = (u16)(v01 >> 16);
      *(LAS u16*)ta[2] = (u16)v23; *(LAS u16*)ta[3] = (u16)(v23 >> 16);
      *(LAS u32x2*)tc = u32x2{pk_bf16(acc1[0], acc1[1]), pk_bf16(acc1[2], acc1[3])};
    }
    lds_post(lds0 + L_MISC + 16, lane);
    lds_wait(lds0 + L_MISC + 16, 4u);
#pragma unroll
    for (int cc = 0; cc < 2; ++cc) {
      u32 kv[5];
#pragma unroll
      for (int j = 0; j < 5; ++j)
        kv[j] = (u32)*(const LAS u16*)(ia[2 * j] + cc * KG_TROW) | ((u32)*(const LAS u16*)(ia[2 * j + 1] + cc * KG_TROW) << 16);
      char* o0 = smem + cc * KIMG_CH + 32 + tid * 16;
      *(u32x4*)o0 = u32x4{kv[1], kv[2], kv[3], kv[4]};
      *(u32x4*)(o0 + KIMG_C1) = u32x4{kv[0], kv[1], kv[2], kv[3]};
    }
    if (tid < 8) *(u32x4*)(smem + (tid >> 2) * KIMG_CH + ((tid >> 1) & 1) * KIMG_C1 + (tid & 1) * 16) = u32x4{0, 0, 0, 0};
  }
  const u32 l_dsk = __builtin_amdgcn_readfirstlane(v_dsk);
  lds_wait(lds0 + L_MISC, 1u);
  const u32 tag = __builtin_amdgcn_readfirstlane(lds_ld(lds0 + L_MISC + 4));
  TS(1)
  const int xcd = b & 7, idx = b >> 3;
  const int rb = (xcd & 1) * 8 + (idx & 7), c = (xcd >> 1) * 4 + (idx >> 3);
  const int rb64 = xcd * 4 + (idx & 3), ct = idx >> 2;

#pragma unroll 1
  for (int layer = 0; layer < 2; ++layer) {
    {
      lane = fresh_lane();
      tid = w * 64 + lane;
      const u32 goff = OFF_GRAN + (b & 7) * GR_SZ + tid * 16, xoff = OFF_XG + (d0 >> 1) * XG_ROW + tid * 64;
      bool needx = !early, needg = !early;
      early = false;
      lds_wait_slow(lds0 + L_MISC + 44, 1u + layer);
      for (int sp = 0; sp < (1 << 18); ++sp) {
        if (needx) {
#pragma unroll
          for (int j = 0; j < 4; ++j) xg[j] = __builtin_amdgcn_raw_buffer_load_b128(rsw, xoff + layer * XG_SZ + j * 16, 0, XG_AUX);
        }
        if (needg) {
          __builtin_amdgcn_s_sleep(GR_DELAY2);
#pragma unroll
          for (int j = 0; j < 4; ++j) gr[j] = __builtin_amdgcn_raw_buffer_load_b128(rsw, goff + layer * (8 * GR_SZ) + j * 4096, 0, GR_AUX);
        }
        u32 bx = 0, bg = 0;
#pragma unroll
        for (int j = 0; j < 4; ++j) { bx |= (xg[j][1] ^ tag) | (xg[j][3] ^ tag); bg |= (gr[j][1] ^ tag) | (gr[j][3] ^ tag); }
        needx = __builtin_amdgcn_ballot_w64(bx != 0u) != 0ull;
        needg = __builtin_amdgcn_ballot_w64(bg != 0u) != 0ull;
        if (!needx && !needg) break;
        __builtin_amdgcn_s_sleep(GR_SLEEP);
      }
      if (lane == 0) __hip_atomic_fetch_add((LAS u32*)(lds0 + L_MISC + 36), 1u, __ATOMIC_RELAXED, __HIP_MEMORY_SCOPE_WORKGROUP);
      TSL(2)
      float base[2][8];
#pragma unroll
      for (int r = 0; r < 2; ++r) {
        u32 up[2][2];
#pragma unroll
        for (int j = 0; j < 2; ++j) {
          const u32 g0 = gr[2 * j][2 * r], g1 = gr[2 * j + 1][2 * r], x0 = xg[2 * j][2 * r], x1 = xg[2 * j + 1][2 * r];
          const f32x2 rs = {__builtin_amdgcn_rcpf(bfhi(g0)), __builtin_amdgcn_rcpf(bfhi(g1))};
          const f32x2 mm = {bflo(g0), bflo(g1)};
#pragma unroll
          for (int cc = 0; cc < 2; ++cc) {
            const f32x2 xx = cc ? f32x2{bfhi(x0), bfhi(x1)} : f32x2{bflo(x0), bflo(x1)};
            const float dk = cc ? bfhi(l_dsk) : bflo(l_dsk);
            const f32x2 u = (xx - mm) * rs;
            up[cc][j] = pk_bf16(u[0], u[1]);
            const f32x2 bs = __builtin_elementwise_fma(u, f32x2{dk, dk}, xx);
            base[cc][4 * j + r] = bs[0]; base[cc][4 * j + r + 2] = bs[1];
          }
        }
#pragma unroll
        for (int cc = 0; cc < 2; ++cc)
          *(u32x2*)(smem + CV_U + (cc * 2 + r) * CV_UROW + 256 + tid * 8) = u32x2{up[cc][0], up[cc][1]};
      }
      {
        const u32 zz = opq(0u);
        *(u32*)(smem + CV_U + w * CV_UROW + lane * 4) = zz;
        *(u32*)(smem + CV_P + w * CV_PROW + 12) = zz;
      }
      lds_post(lds0 + L_MISC + 32, lane);
      lds_wait(lds0 + L_MISC + 32, 4u + 5u * layer);
      TSL(3)
      conv_mfma(lds0, smem, lane, w);
      lds_post(lds0 + L_MISC + 20, lane);
      lds_wait(lds0 + L_MISC + 20, 4u * (layer + 1));
      TSL(4)
      const int off = CV_P + 16 + tid * 32 + (tid >> 2) * 16;
      const int offm = off + CV_PROW - (((tid & 3) == 0 && tid > 0) ? 20 : 4);
      const u32 xo = OFF_X + layer * X_SZ + ((tid >> 4) * SD + d0) * 256 + (tid & 15) * 16;
#pragma unroll
      for (int cc = 0; cc < 2; ++cc) {
        const char* p0 = smem + off + (2 * cc) * CV_PROW;
        const f32x4 a0 = *(const f32x4*)p0, a1 = *(const f32x4*)(p0 + 16);
        const f32x4 b0 = *(const f32x4*)(p0 + CV_PROW), b1 = *(const f32x4*)(p0 + CV_PROW + 16);
        const float bm = *(const float*)(smem + offm + (2 * cc) * CV_PROW);
        const float o[8] = {base[cc][0] + a0[0] + bm,    base[cc][1] + a0[1] + b0[0], base[cc][2] + a0[2] + b0[1],
                            base[cc][3] + a0[3] + b0[2], base[cc][4] + a1[0] + b0[3], base[cc][5] + a1[1] + b1[0],
                            base[cc][6] + a1[2] + b1[1], base[cc][7] + a1[3] + b1[2]};
        const u32x4 ov = {pk_bf16(o[0], o[1]), pk_bf16(o[2], o[3]), pk_bf16(o[4], o[5]), pk_bf16(o[6], o[7])};
        __builtin_amdgcn_raw_buffer_store_b128(ov, rsw, xo + cc * 256, 0, 16);
      }
      wait_vm<0>();
      if (lds_arrive(lds0 + L_MISC + 24, lane) == 4u * layer + 3u) grid_signal(ws, layer, b, lane);
      TSL(5)
    }
    lane = fresh_lane();
    asm volatile("" : "+s"(ws));
    const int mq = w;
    int col = lane & 15, g = lane >> 4, q = (lane & 15) >> 2, p = lane & 3;
    const u32 hoff = OFF_H + layer * H_SZ;
    const int key = q | ((g & 1) << 2);
    u32 xa[2], wa[3];
#pragma unroll
    for (int j = 0; j < 2; ++j) {
      xa[j] = lds0 + (8 * g + q) * 256 + (((2 * mq + j) ^ key) * 32) + 8 * p;
      asm volatile("" : "+v"(xa[j]));
    }
#pragma unroll
    for (int r = 0; r < 3; ++r) {
      wa[r] = lds0 + r * F_BASE + lane * 16;
      asm volatile("" : "+v"(wa[r]));
    }
    auto lw128 = [&](auto O) { constexpr int o = decltype(O)::value; return lds_rd128(wa[o / F_BASE] + o % F_BASE); };
    f32x4 acc[11][2];
#pragma unroll
    for (int t = 0; t < 11; ++t)
#pragma unroll
      for (int j = 0; j < 2; ++j) acc[t][j] = f32x4{0, 0, 0, 0};
    f32x4 s1[2] = {{0, 0, 0, 0}, {0, 0, 0, 0}}, s2[2] = {{0, 0, 0, 0}, {0, 0, 0, 0}};
    const u32x4 ones = {0x3f803f80u, 0x3f803f80u, 0x3f803f80u, 0x3f803f80u};
    u32x4 xf[16][2], wf[2][6];
    auto rd = [&](auto K) {
      constexpr int k = decltype(K)::value, o = ((k >> 1) % 3) * 16384 + (k & 1) * 8192;
#pragma unroll
      for (int j = 0; j < 2; ++j) {
        const u32x2 lo = lds_tr32(xa[j] + o), hi = lds_tr32(xa[j] + o + 1024);
        xf[k][j] = u32x4{lo[0], lo[1], hi[0], hi[1]};
      }
      static_for<0, 6>([&](auto T) {
        constexpr int t = decltype(T)::value;
        wf[k & 1][t] = lw128(std::integral_constant<int, F_W1 + (k * 6 + t) * 1024>{});
      });
    };
    auto mm = [&](auto K) {
      constexpr int k = decltype(K)::value;
#pragma unroll
      for (int j = 0; j < 2; ++j) {
        s2[j] = mfma_bf16(xf[k][j], xf[k][j], s2[j]);
        s1[j] = mfma_bf16(ones, xf[k][j], s1[j]);
      }
#pragma unroll
      for (int t = 0; t < 6; ++t)
#pragma unroll
        for (int j = 0; j < 2; ++j) acc[t][j] = mfma_bf16(wf[k & 1][t], xf[k][j], acc[t][j]);
    };
    auto weave = [&]() {
#pragma unroll
      for (int k = 0; k < 10; ++k) {
        __builtin_amdgcn_sched_group_barrier(0x008, 1, 0);
        __builtin_amdgcn_sched_group_barrier(0x100, 1, 0);
      }
      __builtin_amdgcn_sched_group_barrier(0x008, 6, 0);
      __builtin_amdgcn_sched_barrier(0);
    };
    static_for<0, 8>([&](auto I) {
      constexpr int i = decltype(I)::value;
      wait_lgkm0();
      __builtin_amdgcn_sched_barrier(0);
      bar();
      if constexpr (i == 0) { TSL(6) }
      rd(std::integral_constant<int, 2 * i>{});
      if constexpr (i > 0) mm(std::integral_constant<int, 2 * i - 1>{});
      weave();
      rd(std::integral_constant<int, 2 * i + 1>{});
      mm(std::integral_constant<int, 2 * i>{});
      weave();
    });
    wait_lgkm0();
    __builtin_amdgcn_sched_barrier(0);
    bar();
    TSL(7)
    mm(std::integral_constant<int, 15>{});
    lane = fresh_lane();
    col = lane & 15; g = lane >> 4; q = (lane & 15) >> 2; p = lane & 3;
    u32 so[6];
    {
      const int lt = rb * 8 + 2 * mq + (g & 1);
      const u32 hb = hoff + (((lt >> 2) * NKF * 4 + (lt & 3)) << 10) + col * 16;
#pragma unroll
      for (int i = 0; i < 6; ++i) {
        const int o0 = i < 3 ? c * 6 + 2 * i : i == 3 ? 96 + c * 2 : 127 + c * 3 + 2 * (i - 4), o1 = o0 + 1;
        const u32 f0 = (i < 4 || c < 15) ? (o0 >> 2) * 4096 + (o0 & 3) * 256 : 0x70000000u;
        const u32 f1 = (i < 3 || (i < 5 && c < 15)) ? (o1 >> 2) * 4096 + (o1 & 3) * 256 : 0x70000000u;
        so[i] = hb + (g >= 2 ? f1 : f0);
      }
    }
    const u32 csa = lds0 + L_CS + g * 16;
    u32x4 wv[2][16];
    static_for<0, 4>([&](auto K) { wv[0][decltype(K)::value] = lw128(std::integral_constant<int, f_tile(6) + decltype(K)::value * 1024>{}); });
    __builtin_amdgcn_sched_barrier(0);
    float lm[2], ls[2], lc[2];
#pragma unroll
    for (int j = 0; j < 2; ++j) {
      const float d01 = (p & 1) ? s2[j][1] : s2[j][0], d23 = (p & 1) ? s2[j][3] : s2[j][2];
      const float dv = sum_groups((g == q) ? ((p & 2) ? d23 : d01) : 0.f);
      ln_consts(s1[j][0], dv, lm[j], ls[j]);
      lc[j] = -1.44269504088896340736f * __builtin_amdgcn_rcpf(ls[j]);
    }
    u32 hv[12][2];
    hv[11][0] = 0; hv[11][1] = 0;
    auto hstore = [&](int t) {
      __builtin_amdgcn_raw_buffer_store_b128(tr4(hv[t][0], hv[t][1], hv[t + 1][0], hv[t + 1][1]), rsw, so[t >> 1], 0, 16);
    };
    auto epi = [&](int t) {
      const f32x4 cs = __builtin_bit_cast(f32x4, lds_rd128(csa + t * 64));
#pragma unroll
      for (int j = 0; j < 2; ++j) hv[t][j] = glu_tile(acc[t][j], cs, lm[j], ls[j], lc[j]);
    };
#pragma unroll
    for (int t = 0; t < 6; t += 2) { epi(t); epi(t + 1); hstore(t); }
    __builtin_amdgcn_sched_barrier(0);
    TSL(13)
    constexpr int PRE = F_PRE, WIN = F_WIN;
    const u32 ho = hoff + (rb64 * NKF * 4 + mq) * 1024 + lane * 16;
    u32x4 hf[NKF];
    static_for<6, 11>([&](auto T) {
      constexpr int t = decltype(T)::value, e = t - 1, wb = t & 1;
      f32x4 cv;
      float ga[4], ua[4];
      if constexpr (t > 6) cv = __builtin_bit_cast(f32x4, lds_rd128(csa + e * 64));
      static_for<0, 32>([&](auto S) {
        constexpr int sg = decltype(S)::value, k = sg >> 1, j = sg & 1;
        if constexpr ((sg & 7) == 0) { wait_lgkm0(); __builtin_amdgcn_sched_barrier(0); }
        acc[t][j] = mfma_bf16(wv[wb][k], xf[k][j], acc[t][j]);
        if constexpr ((sg & 7) < 4) {
          constexpr int kk = 4 + (sg >> 3) * 4 + (sg & 3);
          if constexpr (kk < 16) wv[wb][kk] = lw128(std::integral_constant<int, f_tile(t) + kk * 1024>{});
          else if constexpr (t < 10) wv[wb ^ 1][kk - 16] = lw128(std::integral_constant<int, f_tile(t + 1) + (kk - 16) * 1024>{});
        }
        if constexpr (t > 6 && sg >= 2 && sg < 26) {
          constexpr int st = (sg - 2) >> 2, a = (sg - 2) & 3, jj = a >> 1, ee = a & 1;
          if constexpr (st == 0) { ga[a] = s_nfma(lm[jj], cv[ee], acc[e][jj][ee]); ua[a] = s_nfma(lm[jj], cv[2 + ee], acc[e][jj][2 + ee]); }
          if constexpr (st == 1) ga[a] = s_mul(ga[a], lc[jj]);
          if constexpr (st == 2) ga[a] = __builtin_amdgcn_exp2f(ga[a]);
          if constexpr (st == 3) ga[a] = s_fma(ga[a], ls[jj], ls[jj]);
          if constexpr (st == 4) ga[a] = __builtin_amdgcn_rcpf(ga[a]);
          if constexpr (st == 5) ua[a] = s_mul(ua[a], ga[a]);
        }
        if constexpr (t > 6 && sg == 27) { hv[e][0] = pk_bf16(ua[0], ua[1]); hv[e][1] = pk_bf16(ua[2], ua[3]); }
        if constexpr (sg == 29 && (t == 8 || t == 10)) hstore(t - 2);
        if constexpr (t == 10 && sg >= 4 && sg < 4 + 2 * PRE && (sg & 1) == 0)
          gload16_sc1(hf[(sg - 4) >> 1], ho, ws + ((sg - 4) >> 1) * 4096);
        __builtin_amdgcn_sched_barrier(0);
      });
      if constexpr (t == 10) { epi(10); hstore(10); __builtin_amdgcn_sched_barrier(0); }
      else wait_lgkm<4>();
      if constexpr (t == 6 || t == 9) wait_vm<0>();
      if constexpr (t == 10) wait_lgkm0();
      bar();
      __builtin_amdgcn_sched_barrier(0);
    });
    TSL(8)
    f32x4 ac2[4] = {{0, 0, 0, 0}, {0, 0, 0, 0}, {0, 0, 0, 0}, {0, 0, 0, 0}};
    u32x4 wf2[2][4];
    auto rd2 = [&](auto U) {
      constexpr int u = decltype(U)::value;
      static_for<0, 4>([&](auto I) {
        constexpr int i = decltype(I)::value;
        wf2[u & 1][i] = lw128(std::integral_constant<int, f_wd(u >> 1) + (u & 1) * 4096 + i * 1024>{});
      });
    };
    rd2(std::integral_constant<int, 0>{});
    __builtin_amdgcn_sched_barrier(0);
    constexpr auto issued = [](int u) {
      int n = PRE;
      for (int v = 1; v <= u; ++v) {
        int lim = v + WIN;
        if (v <= F_U2 && lim > 24) lim = 24;
        if (v <= F_U3 && lim > 31) lim = 31;
        if (lim > NKF) lim = NKF;
        n = n + F_RATE < lim ? n + F_RATE : (lim > n ? lim : n);
      }
      return n;
    };
    static_for<0, NKF>([&](auto U) {
      constexpr int u = decltype(U)::value;
      static_assert(issued(u) > u, "h window");
      asm volatile("s_waitcnt vmcnt(%1)" : "+v"(hf[u]) : "n"(issued(u) - 1 - u + (u < PRE ? 2 : 0)) : "memory");
      if constexpr (u == F_U1) { wait_vm<issued(u) - PRE>(); bar(); TSL(9) }
      if constexpr (u == F_U2) bar();
      if constexpr (u == F_U3) { wait_lgkm0(); bar(); TSL(10) }
      if constexpr (u == 37) bar();
      __builtin_amdgcn_sched_barrier(0);
#pragma unroll
      for (int v = issued(u); v < issued(u + 1); ++v) gload16_sc1(hf[v], ho, ws + v * 4096);
      if constexpr (u + 1 < NKF) rd2(std::integral_constant<int, u + 1>{});
#pragma unroll
      for (int i = 0; i < 4; ++i) ac2[i] = mfma_bf16(wf2[u & 1][i], hf[u], ac2[i]);
#pragma unroll
      for (int k = 0; k < 4; ++k) {
        __builtin_amdgcn_sched_group_barrier(0x008, 1, 0);
        __builtin_amdgcn_sched_group_barrier(0x100, 1, 0);
      }
      __builtin_amdgcn_sched_barrier(0);
    });
    TSL(11)
    lane = fresh_lane();
    {
    const int col = lane & 15, g = lane >> 4, q = (lane & 15) >> 2, p = lane & 3;
    const int l = rb64 * 64 + mq * 16 + col;
    u32x2 ov[4];
#pragma unroll
    for (int i = 0; i < 4; ++i) {
      const u32x2 rv = lds_tr32(lds0 + F_RES + (i * 16 + 4 * g + q) * 144 + mq * 32 + p * 8);
      const u32 o0 = pk_bf16(ac2[i][0], ac2[i][1]), o1 = pk_bf16(ac2[i][2], ac2[i][3]);
      ov[i] = u32x2{pk_bf16(bflo(rv[0]) + bflo(o0), bfhi(rv[0]) + bfhi(o0)),
                    pk_bf16(bflo(rv[1]) + bflo(o1), bfhi(rv[1]) + bfhi(o1))};
    }
    if (layer == 0) {
      lds_bar();
      float sa = 0.f, sq = 0.f;
#pragma unroll
      for (int i = 0; i < 4; ++i)
#pragma unroll
        for (int e = 0; e < 2; ++e) { sa = dot2bf(ov[i][e], 0x3f803f80u, sa); sq = dot2bf(ov[i][e], ov[i][e], sq); }
      sa = sum_groups(sa); sq = sum_groups(sq);
      __builtin_amdgcn_raw_buffer_store_b128(u32x4{__float_as_uint(sa), tag, __float_as_uint(sq), tag}, rsw,
                                             g == 0 ? OFF_PART + (l * 8 + ct) * 16 : 0x7ffffff0u, 0, 16);
#pragma unroll
      for (int i = 0; i < 4; ++i) {
        const u32 give = (col & 1) ? ov[i][0] : ov[i][1];
        const u32 got = (u32)__builtin_amdgcn_update_dpp(0, (int)give, 0xB1, 0xf, 0xf, true);
        const u32x4 gv = {(col & 1) ? got : ov[i][0], tag, (col & 1) ? ov[i][1] : got, tag};
        const u32 go = OFF_XG + XG_SZ + (ct * 32 + i * 8 + g * 2 + (col & 1)) * XG_ROW + (l & ~1) * 8;
        __builtin_amdgcn_raw_buffer_store_b128(gv, rsw, go, 0, 16);
      }
    } else {
#pragma unroll
      for (int i = 0; i < 4; ++i) *(u32x2*)(A.out + (size_t)l * SD + ct * 64 + i * 16 + 4 * g) = ov[i];
    }
    }
#pragma unroll
    for (int j = 0; j < 4; ++j) asm volatile("" : "=v"(gr[j]), "=v"(xg[j]));
    TSL(12)
    TSL_FLUSH
  }
  if (b == 0 && w == 0 && fresh_lane() == 0) __hip_atomic_store((gu32*)(ws + OFF_EPOCH), tag, __ATOMIC_RELAXED, __HIP_MEMORY_SCOPE_AGENT);
}

DEV void publish_ln(char* ws, u32 off, float sum, float sumsq, u32 tag, int b, int lane) {
  float m, sd;
  ln_consts(sum8(sum), sum8(sumsq), m, sd);
  const u32 val = (__float_as_uint(m) >> 16) | (__float_as_uint(sd) & 0xffff0000u);
  __hip_atomic_store((gu64*)(ws + off + (lane & 7) * GR_SZ) + (lane >> 4) * 512 + 2 * b + ((lane >> 3) & 1),
                     ((unsigned long long)tag << 32) | val, __ATOMIC_RELAXED, __HIP_MEMORY_SCOPE_AGENT);
}

DEV void helper_waves(const Args& A, char* smem, int tid, int lw, int b KIDP) {
  TS_DECL
  char* ws = A.ws;
  const int lane = tid & 63;
  const u32 lds0 = (u32)(uintptr_t)smem;
  const auto rsw = __builtin_amdgcn_make_buffer_rsrc(ws, 0, (int)WS_USED, 0x00020000);
  const int kr = lane >> 3, no = lane & 7;
  u32 tag;
  {
    char* img = smem + KG_PACK + (lw - 1) * PK_IMG;
    if (lw <= 1) {
      const u16* src = lw == 0 ? A.x + (size_t)(8 * b + kr) * SD + no * 8
                               : A.x + (size_t)(64 * (b >> 3) + kr) * SD + (b & 7) * 64 + no * 8;
      const int stp = lw == 0 ? 64 : 8 * SD;
      u32x4 v[8];
#pragma unroll
      for (int i = 0; i < 8; ++i) v[i] = *(const u32x4*)(src + i * stp);
      tag = __builtin_amdgcn_raw_buffer_load_b32(rsw, OFF_EPOCH, 0, 16);
      if (lw == 0 && b < 10) __builtin_amdgcn_raw_buffer_store_b32(0u, rsw, OFF_CNT + (b * 64 + lane) * 128, 0, 16);
      __builtin_amdgcn_sched_barrier(0);
      bar();
      __builtin_amdgcn_sched_barrier(0);
      if (lw == 1) {
        tag += 1u;
        if (lane == 0) { lds_st(lds0 + L_MISC + 4, tag); lds_st(lds0 + L_MISC, 1u); }
#pragma unroll
        for (int i = 0; i < 8; ++i)
          *(u32x4*)(img + (8 * i + kr) * 128 + ((no ^ ((4 * i + (kr >> 1)) & 7)) * 16)) = v[i];
        wait_lgkm0();
        asm volatile("" ::: "memory");
        const int pq = kr & 3, hi = lane >> 5;
        const char* rp = img + (no + 8 * hi) * 256 + pq * 4;
        const u32 go = OFF_XG + ((b & 7) * 32 + pq) * XG_ROW + (b >> 3) * 512 + (no + 8 * hi) * 16;
#pragma unroll
        for (int i = 0; i < 16; ++i) {
          const char* a = rp + (((i & 7) ^ no) * 16) + (i >> 3) * 4096;
          const u32 r0 = *(const u32*)a, r1 = *(const u32*)(a + 128);
          __builtin_amdgcn_raw_buffer_store_b128(u32x4{r0, tag, r1, tag}, rsw, go + (i & 7) * 4 * XG_ROW + (i >> 3) * 256, 0, 16);
        }
        wait_vm<0>();
        if (lane == 0) lds_st(lds0 + L_MISC + 44, 1u);
      } else {
        float a[2] = {0.f, 0.f}, q[2] = {0.f, 0.f};
#pragma unroll
        for (int i = 0; i < 8; ++i)
#pragma unroll
          for (int j = 0; j < 4; ++j) {
            a[j & 1] = dot2bf(v[i][j], 0x3f803f80u, a[j & 1]);
            q[j & 1] = dot2bf(v[i][j], v[i][j], q[j & 1]);
          }
        lds_wait(lds0 + L_MISC, 1u);
        tag = lds_ld(lds0 + L_MISC + 4);
        wait_vm<0>();
        publish_ln(ws, OFF_GRAN, a[0] + a[1], q[0] + q[1], tag, b, lane);
      }
    } else {
      if (lw == 2) {
        const u32 t0 = __builtin_amdgcn_raw_buffer_load_b32(rsw, ((b >> 3) * TLB_N + (lane % TLB_N)) * TLB_STRIDE + TLB_STRIDE - 256, 0, 16);
        const u16 t1 = A.out[((b >> 3) * 32768) + 16384];
        __builtin_amdgcn_sched_barrier(0);
        bar();
        asm volatile("" ::"v"(t0), "v"((u32)t1));
      } else
      bar();
      __builtin_amdgcn_s_sleep(PK_DELAY);
    }
    TS(12)
    const int id = (3 - lw) * 256 + b;
    const int col = lane & 15, g = lane >> 4, q = (lane & 15) >> 2, p = lane & 3;
    const u32 ra = (u32)(uintptr_t)img + (8 * g + q) * 256 + 8 * p;
    const int rkey = q | ((g & 1) << 2);
    u32x4 fr[9];
    u32 so9[9];
#pragma unroll
    for (int i = 0; i < 9; ++i) so9[i] = 0x7ffffff0u;
    if (id < 352) {
      const int ps = id & 15, ng = id >> 4;
      const size_t so = (size_t)(32 * ps + kr) * SF + 8 * (8 * ng + no < 172 ? 8 * ng + no : 0);
      u32x4 gv[4], uv[4];
#pragma unroll
      for (int i = 0; i < 4; ++i) {
        gv[i] = *(const u32x4*)(A.wg + so + (size_t)i * 8 * SF);
        uv[i] = *(const u32x4*)(A.wu + so + (size_t)i * 8 * SF);
      }
#pragma unroll
      for (int i = 0; i < 4; ++i) {
        char* dst = img + (8 * i + kr) * 256 + ((no ^ ((kr & 3) | ((i & 1) << 2))) * 32);
        *(u32x4*)dst = u32x4{gv[i][0], uv[i][0], gv[i][1], uv[i][1]};
        *(u32x4*)(dst + 16) = u32x4{gv[i][2], uv[i][2], gv[i][3], uv[i][3]};
      }
      wait_lgkm0();
      asm volatile("" ::: "memory");
      f32x4 cs = {0, 0, 0, 0};
#pragma unroll
      for (int t8 = 0; t8 < 8; ++t8) {
        const u32x2 lo = lds_tr32(ra + ((t8 ^ rkey) * 32)), hi = lds_tr32(ra + ((t8 ^ rkey) * 32) + 1024);
        fr[t8] = u32x4{lo[0], lo[1], hi[0], hi[1]};
        const int Tt = 8 * ng + t8;
        so9[t8] = lane * 16 + OFF_WGU + wgu_off(Tt / 11, ps, Tt % 11);
        const u32 sel = col == t8 ? 0x3f803f80u : 0u;
        cs = mfma_bf16(fr[t8], u32x4{sel, sel, sel, sel}, cs);
      }
      fr[8] = __builtin_bit_cast(u32x4, cs);
      if (col < 8) so9[8] = OFF_CSP + ((ps * 176 + 8 * ng + col) * 16 + g * 4) * 4;
    } else if (id < 352 + 8 * NKF) {
      const int ct = (id - 352) & 7, kp = (id - 352) >> 3;
      u32x4 gv[4];
#pragma unroll
      for (int i = 0; i < 4; ++i)
        gv[i] = *(const u32x4*)(A.wd + (size_t)(8 * tile_of_oct(4 * kp + i) + kr) * SD + ct * 64 + no * 8);
#pragma unroll
      for (int i = 0; i < 4; ++i)
        *(u32x4*)(img + (8 * i + kr) * 256 + (((no >> 1) ^ ((kr & 3) | ((i & 1) << 2))) * 32) + (no & 1) * 16) = gv[i];
      wait_lgkm0();
      asm volatile("" ::: "memory");
#pragma unroll
      for (int nt = 0; nt < 4; ++nt) {
        const u32x2 lo = lds_tr32(ra + ((nt ^ rkey) * 32)), hi = lds_tr32(ra + ((nt ^ rkey) * 32) + 1024);
        fr[nt] = u32x4{lo[0], lo[1], hi[0], hi[1]};
        so9[nt] = lane * 16 + OFF_WD + ((ct * NKF + kp) * 4 + nt) * 1024;
      }
    }
    if (id < 352) {
#pragma unroll
      for (int i = 0; i < 8; ++i)
        if ((8 * (id >> 4) + i) % 11 < 6) __builtin_amdgcn_raw_buffer_store_b128(fr[i], rsw, so9[i], 0, 16);
      __builtin_amdgcn_raw_buffer_store_b128(fr[8], rsw, so9[8], 0, 16);
    }
    TS(13)
    lds_post(lds0 + L_MISC + 28, lane);
    lds_wait_slow(lds0 + L_MISC + 36, 4u);
    tag = lds_ld(lds0 + L_MISC + 4);
    wait_vm<0>();
    if (id < 352) {
      if (lane == 0)
        __hip_atomic_fetch_add((gu32*)(ws + OFF_CNT) + (96 + (id >> 4)) * 32, 1u, __ATOMIC_RELAXED, __HIP_MEMORY_SCOPE_AGENT);
#pragma unroll
      for (int i = 0; i < 8; ++i)
        if ((8 * (id >> 4) + i) % 11 >= 6) __builtin_amdgcn_raw_buffer_store_b128(fr[i], rsw, so9[i], 0, 16);
    } else if (id < 352 + 8 * NKF) {
#pragma unroll
      for (int i = 0; i < 4; ++i) __builtin_amdgcn_raw_buffer_store_b128(fr[i], rsw, so9[i], 0, 16);
    }
    wait_vm<0>();
    if (lds_arrive(lds0 + L_MISC + 40, lane) == 3u && lane == 0)
      __hip_atomic_fetch_add((gu32*)(ws + OFF_CNT) + (120 + (b & 7)) * 32, 1u, __ATOMIC_RELAXED, __HIP_MEMORY_SCOPE_AGENT);
  }
  TS(1)
  const int xcd = b & 7, idx = b >> 3;
  const int rb = (xcd & 1) * 8 + (idx & 7), c = (xcd >> 1) * 4 + (idx >> 3);
  const int rb64 = xcd * 4 + (idx & 3), ct = idx >> 2;
  const u32 wbase = OFF_WGU + c * (176 * 1024) + lane * 16;
  const u32 wdo = OFF_WD + (ct * NKF * 4 + lw * 2) * 1024 + lane * 16;
  u32x4 kimg[17];
  const u32 ko = lds0 + lane * 16;

#pragma unroll 1
  for (int layer = 0; layer < 2; ++layer) {
    asm volatile("" : "+s"(ws));
    const u32 xin = OFF_X + layer * X_SZ;
    auto wdp = [&](int u) { dma2(wdo, ws + u * 8192, lds0 + f_wd(u) + lw * 2048); };
    if (lw == 0) {
      if (layer == 1) {
#pragma unroll
        for (int i = 0; i < 17; ++i)
          if (i < 16 || lane < 32) *(LAS u32x4*)(ko + i * 1024) = kimg[i];
        lds_post(lds0 + L_MISC + 32, lane);
        u32x4 pv;
        __builtin_amdgcn_s_sleep(GR_DELAY);
        for (int sp = 0; sp < (1 << 18); ++sp) {
          pv = __builtin_amdgcn_raw_buffer_load_b128(rsw, OFF_PART + b * 1024 + lane * 16, 0, XG_AUX);
          if (__builtin_amdgcn_ballot_w64(((pv[1] ^ tag) | (pv[3] ^ tag)) != 0u) == 0ull) break;
          __builtin_amdgcn_s_sleep(3);
        }
        publish_ln(ws, OFF_GRAN + 8 * GR_SZ, __uint_as_float(pv[0]), __uint_as_float(pv[2]), tag, b, lane);
        if (lane == 0) lds_st(lds0 + L_MISC + 44, 2u);
        TSL(8)
      } else {
        lds_wait_slow(lds0 + L_MISC + 32, 4u);
#pragma unroll
        for (int i = 0; i < 17; ++i)
          if (i < 16 || lane < 32) kimg[i] = lds_rd128(ko + i * 1024);
        gu32* fp = (gu32*)(ws + OFF_CNT) + (96 + min((c * 11 >> 3) + lane, (c * 11 + 10) >> 3)) * 32;
        lds_wait_slow(lds0 + L_MISC + 28, 4u);
        __builtin_amdgcn_s_sleep(FL_DELAY);
        for (int sp = 0; sp < (1 << 20); ++sp) {
          u32 v = 16u;
          if (lane < 3) v = __hip_atomic_load(fp, __ATOMIC_RELAXED, __HIP_MEMORY_SCOPE_AGENT);
          if (__builtin_amdgcn_ballot_w64(v < 16u) == 0ull) break;
          __builtin_amdgcn_s_sleep(2);
        }
        asm volatile("" ::: "memory");
        if (lane == 0) lds_st(lds0 + L_MISC + 28, 5u);
        TS(2)
        f32x4 cv[6];
#pragma unroll
        for (int r = 0; r < 6; ++r) {
          const u32 co = OFF_CSP + (no * 176 + c * 11) * 64 + min(r * 8 + kr, 43) * 16;
          cv[r] = __builtin_bit_cast(f32x4, __builtin_amdgcn_raw_buffer_load_b128(rsw, co, 0, 16)) +
                  __builtin_bit_cast(f32x4, __builtin_amdgcn_raw_buffer_load_b128(rsw, co + 8 * 176 * 64, 0, 16));
        }
#pragma unroll
        for (int r = 0; r < 6; ++r) {
          const f32x4 v = {sum8(cv[r][0]), sum8(cv[r][1]), sum8(cv[r][2]), sum8(cv[r][3])};
          if (no == 0) *(f32x4*)(smem + L_CS + min(r * 8 + kr, 43) * 16) = v;
        }
        wait_lgkm0();
        for (int sp = 0; sp < (1 << 20); ++sp) {
          u32 v = 32u;
          if (lane < 8) v = __hip_atomic_load((gu32*)(ws + OFF_CNT) + (120 + lane) * 32, __ATOMIC_RELAXED, __HIP_MEMORY_SCOPE_AGENT);
          if (__builtin_amdgcn_ballot_w64(v < 32u) == 0ull) break;
          __builtin_amdgcn_s_sleep(8);
        }
        asm volatile("" ::: "memory");
      }
      TSL(3)
      if (lds_ld(lds0 + L_MISC + 24) < 4u * (layer + 1)) {
        lds_wait_slow(lds0 + L_MISC + 24, 4u * (layer + 1));
        __builtin_amdgcn_s_sleep(X_DELAY);
      }
      grid_wait(ws, layer, b, lane);
      TSL(4)
      u32 vo[2];
#pragma unroll
      for (int o = 0; o < 2; ++o) {
        const int seg = ((lane & 15) >> 1) ^ ((lane >> 4) | (o << 2));
        vo[o] = xin + rb * (SD * 256) + (lane >> 4) * 256 + seg * 32 + (lane & 1) * 16;
      }
      auto stage = [&](int s) {
#pragma unroll
        for (int i = 0; i < 4; ++i)
          asm volatile(
              "s_mov_b32 m0, %3\n\ts_nop 0\n\tglobal_load_lds_dwordx4 %0, %2 sc1\n\tglobal_load_lds_dwordx4 %0, %2 offset:1024 sc1\n\t"
              "global_load_lds_dwordx4 %1, %2 offset:2048 sc1\n\tglobal_load_lds_dwordx4 %1, %2 offset:3072 sc1"
              ::"v"(vo[0]), "v"(vo[1]), "s"(ws + s * 16384 + i * 4096), "s"(lds0 + (s % 3) * 16384 + i * 4096) : "memory");
      };
      stage(0); stage(1);
      static_for<0, 8>([&](auto I) {
        constexpr int i = decltype(I)::value;
        wait_vm<(i < 7 ? 16 : 0)>();
        bar();
        if constexpr (i < 6) stage(i + 2);
      });
    } else {
      lds_wait_slow(layer == 0 ? lds0 + L_MISC + 28 : lds0 + L_MISC + 36, layer == 0 ? 5u : 8u);
      TS(2)
      const u32 wo = wbase + (lw - 1) * 4096;
#pragma unroll
      for (int i = 0; i < 8; ++i) {
        if (lds_ld(lds0 + L_MISC + 48) > 4u * layer) lds_wait_slow(lds0 + L_MISC + 24, 4u * (layer + 1));
        dma4(wo, ws + i * 12288, lds0 + F_W1 + i * 12288 + (lw - 1) * 4096);
      }
      TSL(3)
      static_for<0, 8>([&](auto I) {
        constexpr int i = decltype(I)::value;
        wait_vm<(i == 0 ? 28 : 24)>();
        bar();
        if constexpr (i >= 1)
          dma4(wo, ws + (96 + (i - 1) * 12) * 1024,
               (i == 7 && lw == 3) ? lds0 + F_BASE : lds0 + F_W1 + (i - 1) * 12288 + (lw - 1) * 4096);
      });
    }
    wait_vm<16>();
    bar();
#pragma unroll
    for (int u = 0; u < 8; ++u) wdp(u);
    wait_vm<12 + 16>();
    bar();
    gu32* cs_ = (gu32*)(ws + OFF_CNT) + (layer * 48 + rb * 3) * 32;
    gu32* cw_ = (gu32*)(ws + OFF_CNT) + (layer * 48 + (rb64 >> 1) * 3) * 32;
    const bool lead = lw == 0 && lane == 0;
    if (lead) __hip_atomic_fetch_add(cs_, 1u, __ATOMIC_RELAXED, __HIP_MEMORY_SCOPE_AGENT);
    asm volatile("" ::: "memory");
    wdp(8); wdp(9); wdp(10);
    wait_vm<4 + 22>();
    bar();
    wdp(11); wdp(12);
    wait_vm<26>();
    bar();
    wdp(13); wdp(14);
    if (lead) poll_ge(cw_, 16u);
    TSL(5)
    bar();
    if (lead) __hip_atomic_fetch_add(cs_ + 32, 1u, __ATOMIC_RELAXED, __HIP_MEMORY_SCOPE_AGENT);
    asm volatile("" ::: "memory");
    wdp(15); wdp(16);
    wait_vm<10>();
    bar();
    wdp(17); wdp(18);
    bar();
    if (lead) {
      __hip_atomic_fetch_add(cs_ + 64, 1u, __ATOMIC_RELAXED, __HIP_MEMORY_SCOPE_AGENT);
      poll_ge(cw_ + 32, 16u);
    }
    wait_vm<0>();
    bar();
    if (lw == 0) __builtin_amdgcn_s_sleep(AB_DELAY);
    if (lead) poll_ge(cw_ + 64, 16u);
    TSL(6)
    bar();
    wdp(19); wdp(20);
    dma2(wdo - (lw >= 2 ? 4096 : 0), ws + 21 * 8192, lds0 + f_wd(21) + lw * 2048);
    {
      const u32 ro = xin + ((rb64 >> 1) * SD + ct * 64 + lw * 16 + kr) * 256 + (rb64 & 1) * 128 + no * 16;
      const u32x4 r0 = __builtin_amdgcn_raw_buffer_load_b128(rsw, ro, 0, 16);
      const u32x4 r1 = __builtin_amdgcn_raw_buffer_load_b128(rsw, ro + 2048, 0, 16);
      char* rp = smem + F_RES + (lw * 16 + kr) * 144 + no * 16;
      *(u32x4*)rp = r0;
      *(u32x4*)(rp + 8 * 144) = r1;
    }
    wait_vm<0>();
    lds_bar();
    TSL(7)
    TSL_FLUSH
    if (layer == 0) bar();
  }
}

__global__ __launch_bounds__(512, 2) void k_all(Args A KIDP) {
  extern __shared__ __attribute__((aligned(16))) char smem[];
  const int tid = threadIdx.x, w = __builtin_amdgcn_readfirstlane(tid >> 6), b = blockIdx.x;
  if (w < 4) compute_waves(A, smem, tid, w, b KIDA(kid_));
  else helper_waves(A, smem, tid, w - 4, b KIDA(kid_));
}

extern "C" void kernel_launch(void* const* d_in, const int* in_sizes, int n_in, void* d_out, int out_size, void* d_ws,
                              size_t ws_size, hipStream_t stream) {
  Args A;
  A.x = (const u16*)d_in[0];
  A.logA = (const u16*)d_in[1];
  A.Aim = (const u16*)d_in[2];
  A.Cre = (const u16*)d_in[3];
  A.Cim = (const u16*)d_in[4];
  A.logdt = (const u16*)d_in[5];
  A.dskip = (const u16*)d_in[6];
  A.wg = (const u16*)d_in[8];
  A.wu = (const u16*)d_in[9];
  A.wd = (const u16*)d_in[10];
  A.out = (u16*)d_out;
  A.ws = (char*)d_ws;
  (void)hipFuncSetAttribute((const void*)k_all, hipFuncAttributeMaxDynamicSharedMemorySize, LDS_BYTES);
  k_all<<<256, 512, LDS_BYTES, stream>>>(A KIDA(0));
}
```

```cpp
#include <hip/hip_runtime.h>
#include <hip/hip_bf16.h>
#include <stdint.h>
#include <type_traits>

typedef unsigned short u16;
typedef unsigned int u32;
typedef __attribute__((ext_vector_type(8))) __bf16 bf16x8;
typedef __attribute__((ext_vector_type(2))) __bf16 bf16x2;
typedef __attribute__((ext_vector_type(8))) _Float16 f16x8;
typedef __attribute__((ext_vector_type(2))) _Float16 f16x2;
typedef __attribute__((ext_vector_type(4))) float f32x4;
typedef __attribute__((ext_vector_type(2))) float f32x2;
typedef __attribute__((ext_vector_type(4))) u32 u32x4;
typedef __attribute__((ext_vector_type(2))) u32 u32x2;
typedef __attribute__((ext_vector_type(4))) short s16x4;
typedef __attribute__((ext_vector_type(2))) double f64x2;
#define LAS __attribute__((address_space(3)))
#define DEV __device__ __forceinline__

#define TSL(i)
#define TSL_FLUSH
#define KIDP
#define KIDA(n)
#define TSP
#define TSA
#define TS_DECL
#define TS(i)
#define TS_FLUSH

constexpr int SL = 2048, SD = 512, SN = 128, SF = 1376;
constexpr int NKF = 43;
constexpr int KIMG_CH = 8448;
constexpr int KIMG_C1 = 4224;
constexpr int XG_ROW = SL * 8 + 256;

constexpr u32 OFF_WGU = 0;
constexpr u32 OFF_WD = OFF_WGU + 16 * 176 * 1024;
constexpr u32 OFF_CSP = OFF_WD + 8 * NKF * 4096;
constexpr u32 OFF_X = OFF_CSP + 16 * 176 * 64;
constexpr u32 X_SZ = SL * SD * 2;
constexpr u32 OFF_H = OFF_X + 2 * X_SZ;
constexpr u32 H_SZ = 32 * NKF * 4096;
constexpr u32 OFF_GRAN = OFF_H + 2 * H_SZ;
constexpr u32 GR_SZ = SL * 8 + 256;
constexpr u32 OFF_XG = OFF_GRAN + 16 * GR_SZ;
constexpr u32 XG_SZ = 256 * XG_ROW;
constexpr u32 OFF_PART = OFF_XG + 2 * XG_SZ;
constexpr u32 OFF_CNT = OFF_PART + SL * 128;
constexpr u32 OFF_EPOCH = OFF_CNT + 640 * 128;
constexpr u32 WS_USED = OFF_EPOCH + 256;

DEV float bflo(u32 v) { return __uint_as_float(v << 16); }
DEV float bfhi(u32 v) { return __uint_as_float(v & 0xffff0000u); }
DEV float bf2f(u16 v) { return __uint_as_float((u32)v << 16); }
DEV u32 pk_bf16(float a, float b) {
  f32x2 v = {a, b};
  return __builtin_bit_cast(u32, __builtin_convertvector(v, bf16x2));
}
DEV u32 pk_f16(float a, float b) {
  f32x2 v = {a, b};
  return __builtin_bit_cast(u32, __builtin_convertvector(v, f16x2));
}
DEV float bfr(float a) { return bflo(pk_bf16(a, a)); }
DEV float dot2bf(u32 a, u32 b, float c) {
  return __builtin_amdgcn_fdot2_f32_bf16(__builtin_bit_cast(bf16x2, a), __builtin_bit_cast(bf16x2, b), c, false);
}
DEV f32x4 mfma_bf16(u32x4 a, u32x4 b, f32x4 c) {
  return __builtin_amdgcn_mfma_f32_16x16x32_bf16(__builtin_bit_cast(bf16x8, a), __builtin_bit_cast(bf16x8, b), c, 0, 0, 0);
}
DEV f32x4 mfma_f16(u32x4 a, u32x4 b, f32x4 c) {
  return __builtin_amdgcn_mfma_f32_16x16x32_f16(__builtin_bit_cast(f16x8, a), __builtin_bit_cast(f16x8, b), c, 0, 0, 0);
}
DEV u32x2 lds_tr(const char* p) {
  return __builtin_bit_cast(u32x2, __builtin_amdgcn_ds_read_tr16_b64_v4i16((LAS s16x4*)p));
}
DEV u32x2 lds_tr32(u32 a) {
  return __builtin_bit_cast(u32x2, __builtin_amdgcn_ds_read_tr16_b64_v4i16((LAS s16x4*)a));
}
DEV u32x4 lds_rd128(u32 a) { return *(const LAS u32x4*)a; }
template <int CTRL>
DEV float dpp_f(float v) {
  return __builtin_bit_cast(float, __builtin_amdgcn_update_dpp(0, __builtin_bit_cast(int, v), CTRL, 0xf, 0xf, true));
}
DEV float sum8(float v) {
  v += dpp_f<0xB1>(v);
  v += dpp_f<0x4E>(v);
  v += dpp_f<0x141>(v);
  return v;
}
DEV float sum16(float v) { v = sum8(v); return v + dpp_f<0x140>(v); }
DEV void ln_consts(float sum, float sumsq, float& m, float& s) {
  float mean = sum * (1.0f / SD);
  float var = fmaxf(sumsq * (1.0f / SD) - mean * mean, 0.0f);
  m = bfr(mean);
  s = bfr(__builtin_amdgcn_sqrtf(bfr(bfr(var) + 1e-5f)));
}

typedef f32x2 cpx;
DEV cpx cmul(cpx a, cpx b) {
  const f32x2 t = a * f32x2{b[0], b[0]};
  return __builtin_elementwise_fma(f32x2{a[1], a[0]}, f32x2{-b[1], b[1]}, t);
}
DEV cpx cmulc(cpx a, cpx b) {
  const f32x2 t = a * f32x2{b[0], b[0]};
  return __builtin_elementwise_fma(f32x2{a[1], a[0]}, f32x2{b[1], -b[1]}, t);
}
DEV float exp_acc(float x) {
  const float t = x * 1.44269504088896340736f;
  const float e = fmaf(x, 1.44269504088896340736f, -t) + x * 1.92596299e-8f;
  const float r = __builtin_amdgcn_exp2f(t);
  return fmaf(r, e * 0.69314718056f, r);
}

typedef __attribute__((address_space(1))) u32 gu32;
typedef __attribute__((address_space(1))) unsigned long long gu64;
template <int N>
DEV void wait_vm() { asm volatile("s_waitcnt vmcnt(%0)" ::"n"(N) : "memory"); }
DEV void wait_lgkm0() { __builtin_amdgcn_s_waitcnt(0xC07F); }
DEV void bar() {
  asm volatile("" ::: "memory");
  __builtin_amdgcn_s_barrier();
  asm volatile("" ::: "memory");
}
template <int N>
DEV void wait_lgkm() { __builtin_amdgcn_s_waitcnt(0xC07F | (N << 8)); }
DEV void lds_bar() { wait_lgkm0(); bar(); }
template <int I, int N, class Fn>
DEV void static_for(Fn&& f) {
  if constexpr (I < N) { f(std::integral_constant<int, I>{}); static_for<I + 1, N>(f); }
}
DEV int wgu_off(int c, int s, int t) { return (c * 176 + (t < 6 ? s * 6 + t : 96 + (t - 6) * 16 + s)) << 10; }
DEV int tile_of_oct(int oct) {
  return oct < 96 ? (oct / 6) * 11 + oct % 6
       : oct < 127 ? ((oct - 96) >> 1) * 11 + 6 + ((oct - 96) & 1) : ((oct - 127) / 3) * 11 + 8 + (oct - 127) % 3;
}
DEV float s_fma(float a, float b, float c) { float d; asm("v_fma_f32 %0, %1, %2, %3" : "=v"(d) : "v"(a), "v"(b), "v"(c)); return d; }
DEV float s_nfma(float a, float b, float c) { float d; asm("v_fma_f32 %0, -%1, %2, %3" : "=v"(d) : "v"(a), "v"(b), "v"(c)); return d; }
DEV float s_mul(float a, float b) { float d; asm("v_mul_f32 %0, %1, %2" : "=v"(d) : "v"(a), "v"(b)); return d; }

constexpr int CV_U = 2 * KIMG_CH;
constexpr int CV_UROW = (128 + 1024) * 2;
constexpr int CV_P = CV_U + 4 * CV_UROW;
constexpr int CV_PROW = 2048 * 4 + 64 * 16 + 16;
constexpr int L_MISC = CV_P + 4 * CV_PROW;
constexpr int L_CS = L_MISC + 64;
constexpr int KG_CH = 64 * 512;
constexpr int KG_TAB = KG_CH + 53 * 512;
constexpr int KG_TROW = 672 * 2;
constexpr int KG_PACK = 65536;
constexpr int PK_IMG = 8192;
constexpr int F_W1 = 65536;
constexpr int LDS_BYTES = 163840;
constexpr int F_BASE = 57344;
static_assert(L_CS + 704 <= 65536 && KG_TAB + 2 * KG_TROW <= L_MISC, "lds map");
constexpr int f_tile(int t) { return F_W1 + (t - 6) * 16384; }
constexpr int f_wd(int u) {
  return u < 7 ? u * 8192 : u < 9 ? F_W1 + 81920 + (u - 7) * 8192 : u < 19 ? F_W1 + (u - 9) * 8192 : f_wd(u - 19);
}
constexpr int F_RES = f_wd(4);

DEV int fresh_lane() {
  int l;
  asm volatile("v_mbcnt_lo_u32_b32 %0, -1, 0\n\tv_mbcnt_hi_u32_b32 %0, -1, %0" : "=v"(l));
  return l;
}
DEV u32 opq(u32 v) { asm volatile("" : "+v"(v)); return v; }
DEV int chan_pair(int b) { return (b & 7) * 32 + (b >> 3); }
DEV u32 lds_ld(u32 a) { return *(volatile LAS u32*)a; }
DEV void lds_st(u32 a, u32 v) { *(volatile LAS u32*)a = v; }
DEV u32 lds_arrive(u32 word, int lane) {
  wait_lgkm0();
  asm volatile("" ::: "memory");
  u32 old = 0;
  if (lane == 0) old = __hip_atomic_fetch_add((LAS u32*)word, 1u, __ATOMIC_RELAXED, __HIP_MEMORY_SCOPE_WORKGROUP);
  return __builtin_amdgcn_readfirstlane(old);
}
DEV void lds_post(u32 word, int lane) {
  wait_lgkm0();
  asm volatile("" ::: "memory");
  if (lane == 0) (void)__hip_atomic_fetch_add((LAS u32*)word, 1u, __ATOMIC_RELAXED, __HIP_MEMORY_SCOPE_WORKGROUP);
}
DEV void lds_wait(u32 word, u32 want) {
#pragma unroll 1
  for (int sp = 0; sp < (1 << 24); ++sp)
    if (lds_ld(word) >= want) break;
  asm volatile("" ::: "memory");
}
DEV void lds_wait_slow(u32 word, u32 want) {
#pragma unroll 1
  for (int sp = 0; sp < (1 << 20); ++sp) {
    if (lds_ld(word) >= want) break;
    __builtin_amdgcn_s_sleep(2);
  }
  asm volatile("" ::: "memory");
}
#define XG_AUX 16
#define GR_AUX 16
#define TLB_WARM 1
#define TLB_STRIDE 65536
#define TLB_N 16
#define OUT_AUX 16
#define CODE_WARM 1
#define CODE_N 11
#define FL_DELAY 10
#define G_REP 8
#define G_SH 8
#define GW_SLEEP 0
#define GR_DELAY2 6
#define X_DELAY 16
#define HPRIO 3
#define PF_DELAY 16
#define PK_DELAY 0
#define GR_SLEEP 8
#define GR_DELAY 8
DEV void grid_signal(char* ws, int k, int b, int lane) {
  if (lane < G_REP)
    __hip_atomic_fetch_add((gu32*)(ws + OFF_CNT) + (128 + k * 256 + lane * G_SH + (b % G_SH)) * 32, 1u, __ATOMIC_RELAXED,
                           __HIP_MEMORY_SCOPE_AGENT);
}
DEV void grid_wait(char* ws, int k, int b, int lane) {
  gu32* p = (gu32*)(ws + OFF_CNT) + (128 + k * 256 + (b % G_REP) * G_SH + (lane % G_SH)) * 32;
#pragma unroll 1
  for (int sp = 0; sp < (1 << 20); ++sp) {
    u32 v = 256u / G_SH;
    if (lane < G_SH) v = __hip_atomic_load(p, __ATOMIC_RELAXED, __HIP_MEMORY_SCOPE_AGENT);
    if (__builtin_amdgcn_ballot_w64(v < 256u / G_SH) == 0ull) break;
    __builtin_amdgcn_s_sleep(GW_SLEEP);
  }
  asm volatile("" ::: "memory");
}
DEV void poll_ge(gu32* cp, u32 want) {
#pragma unroll 1
  for (int sp = 0; sp < (1 << 20); ++sp) {
    if (__hip_atomic_load(cp, __ATOMIC_RELAXED, __HIP_MEMORY_SCOPE_AGENT) >= want) break;
  }
}
DEV void gload16_sc1(u32x4& dst, u32 voff, const char* base) {
  asm volatile("global_load_dwordx4 %0, %1, %2 sc1" : "=v"(dst) : "v"(voff), "s"(base) : "memory");
}
DEV void dma2(u32 voff, const char* base, u32 lds) {
  asm volatile("s_mov_b32 m0, %2\n\ts_nop 0\n\tglobal_load_lds_dwordx4 %0, %1 sc1\n\t"
               "global_load_lds_dwordx4 %0, %1 offset:1024 sc1" ::"v"(voff), "s"(base), "s"(lds) : "memory");
}
DEV void dma4(u32 voff, const char* base, u32 lds) {
  asm volatile("s_mov_b32 m0, %2\n\ts_nop 0\n\tglobal_load_lds_dwordx4 %0, %1 sc1\n\t"
               "global_load_lds_dwordx4 %0, %1 offset:1024 sc1\n\tglobal_load_lds_dwordx4 %0, %1 offset:2048 sc1\n\t"
               "global_load_lds_dwordx4 %0, %1 offset:3072 sc1" ::"v"(voff), "s"(base), "s"(lds) : "memory");
}
DEV cpx cpow2(float a2, float rh, float rl, float s) {
  const float mag = __builtin_amdgcn_exp2f(a2 * s);
  const float rev = fmaf(rl, s, __builtin_amdgcn_fractf(rh * s));
  return f32x2{__builtin_amdgcn_cosf(rev), __builtin_amdgcn_sinf(rev)} * f32x2{mag, mag};
}
DEV u32 glu_tile(f32x4 acc, f32x4 cs, float m, float s, float c1) {
  const f32x2 nm = {-m, -m};
  const f32x2 gg = __builtin_elementwise_fma(nm, f32x2{cs[0], cs[1]}, f32x2{acc[0], acc[1]}) * f32x2{c1, c1};
  const f32x2 uu = __builtin_elementwise_fma(nm, f32x2{cs[2], cs[3]}, f32x2{acc[2], acc[3]});
  const f32x2 ex = {__builtin_amdgcn_exp2f(gg[0]), __builtin_amdgcn_exp2f(gg[1])};
  const f32x2 dn = __builtin_elementwise_fma(ex, f32x2{s, s}, f32x2{s, s});
  const f32x2 hh = uu * f32x2{__builtin_amdgcn_rcpf(dn[0]), __builtin_amdgcn_rcpf(dn[1])};
  return pk_bf16(hh[0], hh[1]);
}
DEV u32x4 tr4(u32 r0, u32 r1, u32 r2, u32 r3) {
  auto a = __builtin_amdgcn_permlane32_swap(r0, r2, false, false);
  auto b = __builtin_amdgcn_permlane32_swap(r1, r3, false, false);
  auto c = __builtin_amdgcn_permlane16_swap(a[0], b[0], false, false);
  auto d = __builtin_amdgcn_permlane16_swap(a[1], b[1], false, false);
  return u32x4{c[0], c[1], d[0], d[1]};
}
DEV float sum_groups(float v) {
  auto a = __builtin_amdgcn_permlane32_swap(__float_as_uint(v), __float_as_uint(v), false, false);
  v = __uint_as_float(a[0]) + __uint_as_float(a[1]);
  auto b = __builtin_amdgcn_permlane16_swap(__float_as_uint(v), __float_as_uint(v), false, false);
  return __uint_as_float(b[0]) + __uint_as_float(b[1]);
}

#define F_WIN 18
#define F_PRE 10
#define F_RATE 2
#define F_U1 6
#define F_U2 12
#define F_U3 26
#define AB_DELAY 0

struct Args {
  const u16 *x, *logA, *Aim, *Cre, *Cim, *logdt, *dskip, *wg, *wu, *wd;
  u16* out;
  char* ws;
};

DEV void conv_mfma(u32 lds0, char* smem, int lane, int w) {
  const int np = lane & 15, h = lane >> 4, q = (lane & 15) >> 2, p = lane & 3;
  u32 ka = lds0 + (w >> 1) * KIMG_CH + ((q & 1) ? KIMG_C1 + 2 * 18 : 2 * 16) + 2 * (48 - 16 * h - 2 * q + 4 * p) - 16;
  u32 ua = lds0 + CV_U + w * CV_UROW + 256 + 2 * (8 * (np + h) - 24) - 192;
  u32 pp = lds0 + CV_P + w * CV_PROW + 16 + np * 64 + h * 16 + (np >> 1) * 16;
  asm volatile("" : "+v"(ka), "+v"(ua), "+v"(pp));
  f32x4 acc[8];
#pragma unroll
  for (int j = 0; j < 8; ++j) acc[j] = f32x4{0, 0, 0, 0};
  u32x4 kf[2][8], uf[2][8];
  constexpr auto pair_i = [](int n) { int i = 0; while (n >= 8 - i) { n -= 8 - i; ++i; } return i; };
  constexpr auto pair_j = [](int n) { int i = 0; while (n >= 8 - i) { n -= 8 - i; ++i; } return i + n; };
  auto rd1 = [&](auto D, auto N) {
    constexpr int dl = decltype(D)::value, n = decltype(N)::value;
    if constexpr (n >= 2 && n < 10) uf[dl & 1][n - 2] = lds_rd128(ua + (n - 2) * 256 + (3 - dl) * 64);
    else {
      constexpr int m = n < 2 ? n : n - 8, i = m >> 1;
      const u32x2 v = lds_tr32(ka + (4 * i + dl) * 128 + ((m & 1) ? 0 : 16));
      kf[dl & 1][i][2 * (m & 1)] = v[0]; kf[dl & 1][i][2 * (m & 1) + 1] = v[1];
    }
  };
  static_for<0, 24>([&](auto N) { rd1(std::integral_constant<int, 0>{}, N); });
  __builtin_amdgcn_sched_barrier(0);
  static_for<0, 4>([&](auto D) {
    constexpr int dl = decltype(D)::value;
    if constexpr (dl == 3) {
      if (lane == 0) __hip_atomic_fetch_add((LAS u32*)(lds0 + L_MISC + 48), 1u, __ATOMIC_RELAXED, __HIP_MEMORY_SCOPE_WORKGROUP);
    }
    if constexpr (dl > 0) wait_lgkm0();
    __builtin_amdgcn_sched_barrier(0);
    static_for<0, 36>([&](auto N) {
      constexpr int n = decltype(N)::value, i = dl < 3 ? pair_i(n) : pair_j(n) - pair_i(n), j = dl < 3 ? pair_j(n) : 7 - pair_i(n);
      acc[j] = mfma_bf16(kf[dl & 1][i], uf[dl & 1][j - i], acc[j]);
      constexpr int r0 = dl == 0 ? 8 : 0;
      if constexpr (dl < 3 && n >= r0 && n < r0 + 24)
        rd1(std::integral_constant<int, dl + 1>{}, std::integral_constant<int, n - r0>{});
      if constexpr (dl == 3 && j < 7 && i == (j < 2 ? j : 2)) *(LAS f32x4*)(pp + (j + 1) * (1024 + 128)) = acc[j + 1];
      __builtin_amdgcn_sched_barrier(0);
    });
  });
  *(LAS f32x4*)pp = acc[0];
}

DEV void compute_waves(const Args& A, char* smem, int tid, int w, int b KIDP) {
  TS_DECL
  char* ws = A.ws;
  int lane = tid & 63;
  const u32 lds0 = (u32)(uintptr_t)smem;
  const auto rsw = __builtin_amdgcn_make_buffer_rsrc(ws, 0, (int)WS_USED, 0x00020000);
  const int d0 = 2 * chan_pair(b);
  const u32 v_dsk = *(const u32*)(A.dskip + d0);
  u32x4 gr[4], xg[4];
  bool early;
  {
    const int ch = w >> 1, n = tid & 127;
    const u32 pi = d0 * SN + tid;
    const u16 l_dt = A.logdt[d0 + ch], l_a = A.logA[pi], l_b = A.Aim[pi];
    const u16 l_cr = A.Cre[pi], l_ci = A.Cim[pi], l_cr2 = A.Cre[pi ^ 64], l_ci2 = A.Cim[pi ^ 64];
    if (tid < 16) *(u32*)(smem + L_MISC + tid * 4) = 0u;
    if (n < 8) *(u32*)(smem + KG_TAB + ch * KG_TROW + n * 4) = 0u;
    __builtin_amdgcn_sched_barrier(0);
    u32 wr[8];
#pragma unroll
    for (int r = 0; r < 8; ++r) { wr[r] = lds0 + ch * KG_CH + r * 512 + ((n * 4) ^ (r << 4)); asm volatile("" : "+v"(wr[r])); }
    const int xt = w & 1, r16 = lane & 15, g = lane >> 4;
    u32 fa[2], fb[2], fc[2];
#pragma unroll
    for (int o = 0; o < 2; ++o) {
      const int rc = min(r16, 4);
      fa[o] = lds0 + ch * KG_CH + (16 * xt + r16) * 512 + (((o * 4 + g) ^ (r16 & 7)) << 4);
      fb[o] = lds0 + ch * KG_CH + (32 + r16) * 512 + (((o * 4 + g) ^ (r16 & 7)) << 4);
      fc[o] = lds0 + ch * KG_CH + (48 + rc) * 512 + (((o * 4 + g) ^ rc) << 4);
      asm volatile("" : "+v"(fa[o]), "+v"(fb[o]), "+v"(fc[o]));
    }
    const int i0 = 16 * xt + 4 * g;
    const int ts2 = r16 < 8 ? 16 : (r16 < 12 ? 8 : 4);
    const int tb = r16 < 8 ? r16 : (r16 < 12 ? 248 + r16 : 384 + 64 * ((r16 >> 1) & 1) + (r16 & 1));
    u32 ta[4];
#pragma unroll
    for (int e = 0; e < 4; ++e) { ta[e] = lds0 + KG_TAB + ch * KG_TROW + 32 + tb * 2 + ts2 * (i0 + e); asm volatile("" : "+v"(ta[e])); }
    u32 tc = lds0 + KG_TAB + ch * KG_TROW + 32 + 2 * ((r16 < 4 || i0 == 0) ? 512 + 32 * min(r16, 4) + i0 : 648);
    asm volatile("" : "+v"(tc));
    u32 ia[10];
    {
      const float f0 = (float)(8 * tid);
#pragma unroll
      for (int j = 0; j < 5; ++j) {
        const u32 bits = pk_bf16(f0 + (float)(2 * j - 2), f0 + (float)(2 * j - 1));
        const u32 lin = lds0 + KG_TAB + (8 * tid + 2 * j - 2 + 16) * 2;
        ia[2 * j] = tid < 24 ? lin : (bits & 0xffffu) * 2 + (lds0 + KG_TAB - (0x4300 - 144) * 2);
        ia[2 * j + 1] = tid < 24 ? lin + 2 : (bits >> 16) * 2 + (lds0 + KG_TAB - (0x4300 - 144) * 2);
        asm volatile("" : "+v"(ia[2 * j]), "+v"(ia[2 * j + 1]));
      }
    }
    __builtin_amdgcn_sched_barrier(0);
    lds_bar();
    __builtin_amdgcn_sched_barrier(0);
    const u32 ea = pk_bf16(exp_acc(bf2f(l_dt)), exp_acc(bf2f(l_a)));
    const float dt = bflo(ea);
    const float a2 = -(bfhi(ea) * dt) * 1.44269504088896340736f;
    const float bi = bf2f(l_b) * dt;
    const float rh = bi * 0.15915494309189533577f;
    const float rl = fmaf(bi, 6.420638e-9f, fmaf(bi, 0.15915494309189533577f, -rh));
    u32 mx = max(max((u32)l_cr & 0x7fffu, (u32)l_ci & 0x7fffu), max((u32)l_cr2 & 0x7fffu, (u32)l_ci2 & 0x7fffu));
    mx = max(mx, (u32)__builtin_amdgcn_update_dpp(0, (int)mx, 0xB1, 0xf, 0xf, true));
    mx = max(mx, (u32)__builtin_amdgcn_update_dpp(0, (int)mx, 0x4E, 0xf, 0xf, true));
    mx = max(mx, (u32)__builtin_amdgcn_update_dpp(0, (int)mx, 0x141, 0xf, 0xf, true));
    mx = max(mx, (u32)__builtin_amdgcn_update_dpp(0, (int)mx, 0x140, 0xf, 0xf, true));
    mx = max(max((u32)__builtin_amdgcn_readlane((int)mx, 0), (u32)__builtin_amdgcn_readlane((int)mx, 16)),
             max((u32)__builtin_amdgcn_readlane((int)mx, 32), (u32)__builtin_amdgcn_readlane((int)mx, 48)));
    const u32 ex = min(max(mx >> 7, 2u), 252u) + 1u;
    const float fs = 2.0f * dt * __uint_as_float(ex << 23), isc = __uint_as_float((254u - ex) << 23);
    cpx z[12];
#pragma unroll
    for (int p = 0; p < 12; p += 3) {
      z[p] = cpow2(a2, rh, rl, (float)(1 << p));
      z[p + 1] = cmul(z[p], z[p]);
      z[p + 2] = cmul(z[p + 1], z[p + 1]);
    }
    {
      cpx V[56];
      auto put = [&](int i) { *(LAS u32*)(wr[i & 7] + (i >> 3) * 4096) = pk_f16(V[i][0], V[i][1]); };
      V[0] = cpx{1.0f, 0.0f};
      V[1] = z[3]; V[2] = z[4]; V[4] = z[5]; V[8] = z[6]; V[16] = z[7];
      V[32] = cpx{bf2f(l_cr) * isc, bf2f(l_ci) * -isc};
      constexpr int OP[47][3] = {
          {3, 1, 4},   {5, 1, 5},   {6, 2, 5},   {33, 32, 0}, {34, 32, 1}, {36, 32, 2}, {40, 32, 8}, {44, 32, 9},
          {7, 3, 5},   {9, 1, 6},   {10, 2, 6},  {11, 3, 6},  {35, 34, 0}, {37, 36, 0}, {38, 36, 1}, {41, 34, 8},
          {12, 4, 6},  {13, 5, 6},  {14, 6, 6},  {15, 7, 6},  {39, 38, 0}, {42, 36, 8}, {43, 38, 8}, {45, 36, 9},
          {17, 1, 7},  {18, 2, 7},  {19, 3, 7},  {20, 4, 7},  {46, 40, 9}, {47, 42, 9}, {48, 32, 10}, {49, 40, 10},
          {21, 5, 7},  {22, 6, 7},  {23, 7, 7},  {24, 8, 7},  {50, 44, 10}, {51, 46, 10}, {52, 32, 11}, {25, 9, 7},
          {26, 10, 7}, {27, 11, 7}, {28, 12, 7}, {29, 13, 7}, {30, 14, 7}, {31, 15, 7}, {0, 0, 0}};
      put(0); put(1); put(2); put(4); put(8); put(16); put(32);
      static_for<0, 13>([&](auto G) {
        constexpr int g4 = decltype(G)::value * 4;
        f32x2 t[4];
#pragma unroll
        for (int k = 0; k < 4; ++k)
          if (g4 + k < 46) t[k] = V[OP[g4 + k][1]] * f32x2{z[OP[g4 + k][2]][0], z[OP[g4 + k][2]][0]};
#pragma unroll
        for (int k = 0; k < 4; ++k)
          if (g4 >= 4 && g4 - 4 + k < 46) put(OP[g4 - 4 + k][0]);
#pragma unroll
        for (int k = 0; k < 4; ++k)
          if (g4 + k < 46) {
            const cpx a = V[OP[g4 + k][1]];
            const float zi = z[OP[g4 + k][2]][1];
            V[OP[g4 + k][0]] = __builtin_elementwise_fma(f32x2{a[1], a[0]}, OP[g4 + k][0] < 32 ? f32x2{-zi, zi} : f32x2{zi, -zi}, t[k]);
          }
        __builtin_amdgcn_sched_barrier(0);
      });
    }
    lds_post(lds0 + L_MISC + 8 + ch * 4, lane);
    lds_wait(lds0 + L_MISC + 8 + ch * 4, 2u);
    {
      f32x4 acc0 = {0, 0, 0, 0}, acc1 = {0, 0, 0, 0};
#pragma unroll
      for (int s = 0; s < 8; ++s) {
        const u32x4 a = lds_rd128(fa[s & 1] + (s >> 1) * 128);
        acc0 = mfma_f16(a, lds_rd128(fb[s & 1] + (s >> 1) * 128), acc0);
        acc1 = mfma_f16(a, lds_rd128(fc[s & 1] + (s >> 1) * 128), acc1);
      }
      early = __builtin_amdgcn_readfirstlane(lds_ld(lds0 + L_MISC + 44)) != 0u;
      if (early) {
#pragma unroll
        for (int j = 0; j < 4; ++j)
          xg[j] = __builtin_amdgcn_raw_buffer_load_b128(rsw, OFF_XG + (d0 >> 1) * XG_ROW + tid * 64 + j * 16, 0, XG_AUX);
#pragma unroll
        for (int j = 0; j < 4; ++j)
          gr[j] = __builtin_amdgcn_raw_buffer_load_b128(rsw, OFF_GRAN + (b & 7) * GR_SZ + tid * 16 + j * 4096, 0, GR_AUX);
      }
      acc0 *= fs; acc1 *= fs;
      const u32 v01 = pk_bf16(acc0[0], acc0[1]), v23 = pk_bf16(acc0[2], acc0[3]);
      *(LAS u16*)ta[0] = (u16)v01; *(LAS u16*)ta[1] = (u16)(v01 >> 16);
      *(LAS u16*)ta[2] = (u16)v23; *(LAS u16*)ta[3] = (u16)(v23 >> 16);
      *(LAS u32x2*)tc = u32x2{pk_bf16(acc1[0], acc1[1]), pk_bf16(acc1[2], acc1[3])};
    }
    lds_post(lds0 + L_MISC + 16, lane);
    lds_wait(lds0 + L_MISC + 16, 4u);
#pragma unroll
    for (int cc = 0; cc < 2; ++cc) {
      u32 kv[5];
#pragma unroll
      for (int j = 0; j < 5; ++j)
        kv[j] = (u32)*(const LAS u16*)(ia[2 * j] + cc * KG_TROW) | ((u32)*(const LAS u16*)(ia[2 * j + 1] + cc * KG_TROW) << 16);
      char* o0 = smem + cc * KIMG_CH + 32 + tid * 16;
      *(u32x4*)o0 = u32x4{kv[1], kv[2], kv[3], kv[4]};
      *(u32x4*)(o0 + KIMG_C1) = u32x4{kv[0], kv[1], kv[2], kv[3]};
    }
    if (tid < 8) *(u32x4*)(smem + (tid >> 2) * KIMG_CH + ((tid >> 1) & 1) * KIMG_C1 + (tid & 1) * 16) = u32x4{0, 0, 0, 0};
  }
  const u32 l_dsk = __builtin_amdgcn_readfirstlane(v_dsk);
  lds_wait(lds0 + L_MISC, 1u);
  const u32 tag = __builtin_amdgcn_readfirstlane(lds_ld(lds0 + L_MISC + 4));
  TS(1)
  const int xcd = b & 7, idx = b >> 3;
  const int rb = (xcd & 1) * 8 + (idx & 7), c = (xcd >> 1) * 4 + (idx >> 3);
  const int rb64 = xcd * 4 + (idx & 3), ct = idx >> 2;

#pragma unroll 1
  for (int layer = 0; layer < 2; ++layer) {
    {
      lane = fresh_lane();
      tid = w * 64 + lane;
      const u32 goff = OFF_GRAN + (b & 7) * GR_SZ + tid * 16, xoff = OFF_XG + (d0 >> 1) * XG_ROW + tid * 64;
      bool needx = !early, needg = !early;
      early = false;
      lds_wait_slow(lds0 + L_MISC + 44, 1u + layer);
#pragma unroll 1
      for (int sp = 0; sp < (1 << 18); ++sp) {
        if (needx) {
#pragma unroll
          for (int j = 0; j < 4; ++j) xg[j] = __builtin_amdgcn_raw_buffer_load_b128(rsw, xoff + layer * XG_SZ + j * 16, 0, XG_AUX);
        }
        if (needg) {
          __builtin_amdgcn_s_sleep(GR_DELAY2);
#pragma unroll
          for (int j = 0; j < 4; ++j) gr[j] = __builtin_amdgcn_raw_buffer_load_b128(rsw, goff + layer * (8 * GR_SZ) + j * 4096, 0, GR_AUX);
        }
        u32 bx = 0, bg = 0;
#pragma unroll
        for (int j = 0; j < 4; ++j) { bx |= (xg[j][1] ^ tag) | (xg[j][3] ^ tag); bg |= (gr[j][1] ^ tag) | (gr[j][3] ^ tag); }
        needx = __builtin_amdgcn_ballot_w64(bx != 0u) != 0ull;
        needg = __builtin_amdgcn_ballot_w64(bg != 0u) != 0ull;
        if (!needx && !needg) break;
        __builtin_amdgcn_s_sleep(GR_SLEEP);
      }
      if (lane == 0) __hip_atomic_fetch_add((LAS u32*)(lds0 + L_MISC + 36), 1u, __ATOMIC_RELAXED, __HIP_MEMORY_SCOPE_WORKGROUP);
      TSL(2)
      float base[2][8];
#pragma unroll
      for (int r = 0; r < 2; ++r) {
        u32 up[2][2];
#pragma unroll
        for (int j = 0; j < 2; ++j) {
          const u32 g0 = gr[2 * j][2 * r], g1 = gr[2 * j + 1][2 * r], x0 = xg[2 * j][2 * r], x1 = xg[2 * j + 1][2 * r];
          const f32x2 rs = {__builtin_amdgcn_rcpf(bfhi(g0)), __builtin_amdgcn_rcpf(bfhi(g1))};
          const f32x2 mm = {bflo(g0), bflo(g1)};
#pragma unroll
          for (int cc = 0; cc < 2; ++cc) {
            const f32x2 xx = cc ? f32x2{bfhi(x0), bfhi(x1)} : f32x2{bflo(x0), bflo(x1)};
            const float dk = cc ? bfhi(l_dsk) : bflo(l_dsk);
            const f32x2 u = (xx - mm) * rs;
            up[cc][j] = pk_bf16(u[0], u[1]);
            const f32x2 bs = __builtin_elementwise_fma(u, f32x2{dk, dk}, xx);
            base[cc][4 * j + r] = bs[0]; base[cc][4 * j + r + 2] = bs[1];
          }
        }
#pragma unroll
        for (int cc = 0; cc < 2; ++cc)
          *(u32x2*)(smem + CV_U + (cc * 2 + r) * CV_UROW + 256 + tid * 8) = u32x2{up[cc][0], up[cc][1]};
      }
      {
        const u32 zz = opq(0u);
        *(u32*)(smem + CV_U + w * CV_UROW + lane * 4) = zz;
        *(u32*)(smem + CV_P + w * CV_PROW + 12) = zz;
      }
      lds_post(lds0 + L_MISC + 32, lane);
      lds_wait(lds0 + L_MISC + 32, 4u + 5u * layer);
      TSL(3)
      conv_mfma(lds0, smem, lane, w);
      lds_post(lds0 + L_MISC + 20, lane);
      lds_wait(lds0 + L_MISC + 20, 4u * (layer + 1));
      TSL(4)
      const int off = CV_P + 16 + tid * 32 + (tid >> 2) * 16;
      const int offm = off + CV_PROW - (((tid & 3) == 0 && tid > 0) ? 20 : 4);
      const u32 xo = OFF_X + layer * X_SZ + ((tid >> 4) * SD + d0) * 256 + (tid & 15) * 16;
#pragma unroll
      for (int cc = 0; cc < 2; ++cc) {
        const char* p0 = smem + off + (2 * cc) * CV_PROW;
        const f32x4 a0 = *(const f32x4*)p0, a1 = *(const f32x4*)(p0 + 16);
        const f32x4 b0 = *(const f32x4*)(p0 + CV_PROW), b1 = *(const f32x4*)(p0 + CV_PROW + 16);
        const float bm = *(const float*)(smem + offm + (2 * cc) * CV_PROW);
        const float o[8] = {base[cc][0] + a0[0] + bm,    base[cc][1] + a0[1] + b0[0], base[cc][2] + a0[2] + b0[1],
                            base[cc][3] + a0[3] + b0[2], base[cc][4] + a1[0] + b0[3], base[cc][5] + a1[1] + b1[0],
                            base[cc][6] + a1[2] + b1[1], base[cc][7] + a1[3] + b1[2]};
        const u32x4 ov = {pk_bf16(o[0], o[1]), pk_bf16(o[2], o[3]), pk_bf16(o[4], o[5]), pk_bf16(o[6], o[7])};
        __builtin_amdgcn_raw_buffer_store_b128(ov, rsw, xo + cc * 256, 0, 16);
      }
      wait_vm<0>();
      if (lds_arrive(lds0 + L_MISC + 24, lane) == 4u * layer + 3u) grid_signal(ws, layer, b, lane);
      TSL(5)
    }
    lane = fresh_lane();
    asm volatile("" : "+s"(ws));
    const int mq = w;
    int col = lane & 15, g = lane >> 4, q = (lane & 15) >> 2, p = lane & 3;
    const u32 hoff = OFF_H + layer * H_SZ;
    const int key = q | ((g & 1) << 2);
    u32 xa[2], wa[3];
#pragma unroll
    for (int j = 0; j < 2; ++j) {
      xa[j] = lds0 + (8 * g + q) * 256 + (((2 * mq + j) ^ key) * 32) + 8 * p;
      asm volatile("" : "+v"(xa[j]));
    }
#pragma unroll
    for (int r = 0; r < 3; ++r) {
      wa[r] = lds0 + r * F_BASE + lane * 16;
      asm volatile("" : "+v"(wa[r]));
    }
    auto lw128 = [&](auto O) { constexpr int o = decltype(O)::value; return lds_rd128(wa[o / F_BASE] + o % F_BASE); };
    f32x4 acc[11][2];
#pragma unroll
    for (int t = 0; t < 11; ++t)
#pragma unroll
      for (int j = 0; j < 2; ++j) acc[t][j] = f32x4{0, 0, 0, 0};
    f32x4 s1[2] = {{0, 0, 0, 0}, {0, 0, 0, 0}}, s2[2] = {{0, 0, 0, 0}, {0, 0, 0, 0}};
    const u32x4 ones = {0x3f803f80u, 0x3f803f80u, 0x3f803f80u, 0x3f803f80u};
    u32x4 xf[16][2], wf[2][6];
    auto rd = [&](auto K) {
      constexpr int k = decltype(K)::value, o = ((k >> 1) % 3) * 16384 + (k & 1) * 8192;
#pragma unroll
      for (int j = 0; j < 2; ++j) {
        const u32x2 lo = lds_tr32(xa[j] + o), hi = lds_tr32(xa[j] + o + 1024);
        xf[k][j] = u32x4{lo[0], lo[1], hi[0], hi[1]};
      }
      static_for<0, 6>([&](auto T) {
        constexpr int t = decltype(T)::value;
        wf[k & 1][t] = lw128(std::integral_constant<int, F_W1 + (k * 6 + t) * 1024>{});
      });
    };
    auto mm = [&](auto K) {
      constexpr int k = decltype(K)::value;
#pragma unroll
      for (int j = 0; j < 2; ++j) {
        s2[j] = mfma_bf16(xf[k][j], xf[k][j], s2[j]);
        s1[j] = mfma_bf16(ones, xf[k][j], s1[j]);
      }
#pragma unroll
      for (int t = 0; t < 6; ++t)
#pragma unroll
        for (int j = 0; j < 2; ++j) acc[t][j] = mfma_bf16(wf[k & 1][t], xf[k][j], acc[t][j]);
    };
    auto weave = [&]() {
#pragma unroll
      for (int k = 0; k < 10; ++k) {
        __builtin_amdgcn_sched_group_barrier(0x008, 1, 0);
        __builtin_amdgcn_sched_group_barrier(0x100, 1, 0);
      }
      __builtin_amdgcn_sched_group_barrier(0x008, 6, 0);
      __builtin_amdgcn_sched_barrier(0);
    };
    static_for<0, 8>([&](auto I) {
      constexpr int i = decltype(I)::value;
      wait_lgkm0();
      __builtin_amdgcn_sched_barrier(0);
      bar();
      if constexpr (i == 0) { TSL(6) }
      rd(std::integral_constant<int, 2 * i>{});
      if constexpr (i > 0) mm(std::integral_constant<int, 2 * i - 1>{});
      weave();
      rd(std::integral_constant<int, 2 * i + 1>{});
      mm(std::integral_constant<int, 2 * i>{});
      weave();
    });
    wait_lgkm0();
    __builtin_amdgcn_sched_barrier(0);
    bar();
    TSL(7)
    mm(std::integral_constant<int, 15>{});
    lane = fresh_lane();
    col = lane & 15; g = lane >> 4; q = (lane & 15) >> 2; p = lane & 3;
    u32 so[6];
    {
      const int lt = rb * 8 + 2 * mq + (g & 1);
      const u32 hb = hoff + (((lt >> 2) * NKF * 4 + (lt & 3)) << 10) + col * 16;
#pragma unroll
      for (int i = 0; i < 6; ++i) {
        const int o0 = i < 3 ? c * 6 + 2 * i : i == 3 ? 96 + c * 2 : 127 + c * 3 + 2 * (i - 4), o1 = o0 + 1;
        const u32 f0 = (i < 4 || c < 15) ? (o0 >> 2) * 4096 + (o0 & 3) * 256 : 0x70000000u;
        const u32 f1 = (i < 3 || (i < 5 && c < 15)) ? (o1 >> 2) * 4096 + (o1 & 3) * 256 : 0x70000000u;
        so[i] = hb + (g >= 2 ? f1 : f0);
      }
    }
    const u32 csa = lds0 + L_CS + g * 16;
    u32x4 wv[2][16];
    static_for<0, 4>([&](auto K) { wv[0][decltype(K)::value] = lw128(std::integral_constant<int, f_tile(6) + decltype(K)::value * 1024>{}); });
    __builtin_amdgcn_sched_barrier(0);
    float lm[2], ls[2], lc[2];
#pragma unroll
    for (int j = 0; j < 2; ++j) {
      const float d01 = (p & 1) ? s2[j][1] : s2[j][0], d23 = (p & 1) ? s2[j][3] : s2[j][2];
      const float dv = sum_groups((g == q) ? ((p & 2) ? d23 : d01) : 0.f);
      ln_consts(s1[j][0], dv, lm[j], ls[j]);
      lc[j] = -1.44269504088896340736f * __builtin_amdgcn_rcpf(ls[j]);
    }
    u32 hv[12][2];
    hv[11][0] = 0; hv[11][1] = 0;
    auto hstore = [&](int t) {
      __builtin_amdgcn_raw_buffer_store_b128(tr4(hv[t][0], hv[t][1], hv[t + 1][0], hv[t + 1][1]), rsw, so[t >> 1], 0, 16);
    };
    auto epi = [&](int t) {
      const f32x4 cs = __builtin_bit_cast(f32x4, lds_rd128(csa + t * 64));
#pragma unroll
      for (int j = 0; j < 2; ++j) hv[t][j] = glu_tile(acc[t][j], cs, lm[j], ls[j], lc[j]);
    };
#pragma unroll
    for (int t = 0; t < 6; t += 2) { epi(t); epi(t + 1); hstore(t); }
    __builtin_amdgcn_sched_barrier(0);
    TSL(13)
    constexpr int PRE = F_PRE, WIN = F_WIN;
    const u32 ho = hoff + (rb64 * NKF * 4 + mq) * 1024 + lane * 16;
    u32x4 hf[NKF];
    static_for<6, 11>([&](auto T) {
      constexpr int t = decltype(T)::value, e = t - 1, wb = t & 1;
      f32x4 cv;
      float ga[4], ua[4];
      static_for<0, 32>([&](auto S) {
        constexpr int sg = decltype(S)::value, k = sg >> 1, j = sg & 1;
        if constexpr ((sg & 7) == 0) { wait_lgkm0(); __builtin_amdgcn_sched_barrier(0); }
        acc[t][j] = mfma_bf16(wv[wb][k], xf[k][j], acc[t][j]);
        if constexpr (t > 6 && sg == 0) cv = __builtin_bit_cast(f32x4, lds_rd128(csa + e * 64));
        if constexpr ((sg & 7) < 4) {
          constexpr int kk = 4 + (sg >> 3) * 4 + (sg & 3);
          if constexpr (kk < 16) wv[wb][kk] = lw128(std::integral_constant<int, f_tile(t) + kk * 1024>{});
          else if constexpr (t < 10) wv[wb ^ 1][kk - 16] = lw128(std::integral_constant<int, f_tile(t + 1) + (kk - 16) * 1024>{});
        }
        if constexpr (t > 6) {
          constexpr int st = sg >> 2, a = sg & 3, jj = a >> 1, ee = a & 1;
          if constexpr (st == 1) { ga[a] = s_nfma(lm[jj], cv[ee], acc[e][jj][ee]); ua[a] = s_nfma(lm[jj], cv[2 + ee], acc[e][jj][2 + ee]); }
          if constexpr (st == 2) ga[a] = s_mul(ga[a], lc[jj]);
          if constexpr (st == 3) ga[a] = __builtin_amdgcn_exp2f(ga[a]);
          if constexpr (st == 4) ga[a] = s_fma(ga[a], ls[jj], ls[jj]);
          if constexpr (st == 5) ga[a] = __builtin_amdgcn_rcpf(ga[a]);
          if constexpr (st == 6) ua[a] = s_mul(ua[a], ga[a]);
        }
        if constexpr (t > 6 && sg == 28) { hv[e][0] = pk_bf16(ua[0], ua[1]); hv[e][1] = pk_bf16(ua[2], ua[3]); }
        if constexpr (sg == 30 && (t == 8 || t == 10)) hstore(t - 2);
        if constexpr (t == 10 && sg >= 4 && sg < 4 + 2 * PRE && (sg & 1) == 0)
          gload16_sc1(hf[(sg - 4) >> 1], ho, ws + ((sg - 4) >> 1) * 4096);
        __builtin_amdgcn_sched_barrier(0);
      });
      if constexpr (t == 10) { epi(10); hstore(10); __builtin_amdgcn_sched_barrier(0); }
      else wait_lgkm<4>();
      if constexpr (t == 6 || t == 9) wait_vm<0>();
      if constexpr (t == 10) wait_lgkm0();
      bar();
      __builtin_amdgcn_sched_barrier(0);
    });
    TSL(8)
    f32x4 ac2[4] = {{0, 0, 0, 0}, {0, 0, 0, 0}, {0, 0, 0, 0}, {0, 0, 0, 0}};
    u32x4 wf2[2][4];
    auto rd2 = [&](auto U) {
      constexpr int u = decltype(U)::value;
      static_for<0, 4>([&](auto I) {
        constexpr int i = decltype(I)::value;
        wf2[u & 1][i] = lw128(std::integral_constant<int, f_wd(u >> 1) + (u & 1) * 4096 + i * 1024>{});
      });
    };
    rd2(std::integral_constant<int, 0>{});
    __builtin_amdgcn_sched_barrier(0);
    constexpr auto issued = [](int u) {
      int n = PRE;
      for (int v = 1; v <= u; ++v) {
        int lim = v + WIN;
        if (v <= F_U2 && lim > 24) lim = 24;
        if (v <= F_U3 && lim > 31) lim = 31;
        if (lim > NKF) lim = NKF;
        const int rate = v > F_U3 ? 6 : F_RATE;
        n = n + rate < lim ? n + rate : (lim > n ? lim : n);
      }
      return n;
    };
    static_for<0, NKF>([&](auto U) {
      constexpr int u = decltype(U)::value;
      static_assert(issued(u) > u && F_U1 >= 6 && F_U2 >= 12 && F_U2 < 25 && F_U3 < 29, "h window / W_down residency");
      asm volatile("s_waitcnt vmcnt(%1)" : "+v"(hf[u]) : "n"(issued(u) - 1 - u + (u < PRE ? 2 : 0)) : "memory");
      if constexpr (u == F_U1) { wait_vm<issued(u) - PRE>(); bar(); TSL(9) }
      if constexpr (u == F_U2) bar();
      if constexpr (u == F_U3) { bar(); TSL(10) }
      __builtin_amdgcn_sched_barrier(0);
#pragma unroll
      for (int v = issued(u); v < issued(u + 1); ++v) gload16_sc1(hf[v], ho, ws + v * 4096);
      if constexpr (u + 1 < NKF) rd2(std::integral_constant<int, u + 1>{});
#pragma unroll
      for (int i = 0; i < 4; ++i) ac2[i] = mfma_bf16(wf2[u & 1][i], hf[u], ac2[i]);
#pragma unroll
      for (int k = 0; k < 4; ++k) {
        __builtin_amdgcn_sched_group_barrier(0x008, 1, 0);
        __builtin_amdgcn_sched_group_barrier(0x100, 1, 0);
      }
      __builtin_amdgcn_sched_barrier(0);
    });
    TSL(11)
    lane = fresh_lane();
    {
    const int col = lane & 15, g = lane >> 4, q = (lane & 15) >> 2, p = lane & 3;
    const int l = rb64 * 64 + mq * 16 + col;
    u32x2 ov[4];
#pragma unroll
    for (int i = 0; i < 4; ++i) {
      const u32x2 rv = lds_tr32(lds0 + F_RES + (i * 16 + 4 * g + q) * 144 + mq * 32 + p * 8);
      const u32 o0 = pk_bf16(ac2[i][0], ac2[i][1]), o1 = pk_bf16(ac2[i][2], ac2[i][3]);
      ov[i] = u32x2{pk_bf16(bflo(rv[0]) + bflo(o0), bfhi(rv[0]) + bfhi(o0)),
                    pk_bf16(bflo(rv[1]) + bflo(o1), bfhi(rv[1]) + bfhi(o1))};
    }
    if (layer == 0) {
      lds_bar();
      float sa = 0.f, sq = 0.f;
#pragma unroll
      for (int i = 0; i < 4; ++i)
#pragma unroll
        for (int e = 0; e < 2; ++e) { sa = dot2bf(ov[i][e], 0x3f803f80u, sa); sq = dot2bf(ov[i][e], ov[i][e], sq); }
      sa = sum_groups(sa); sq = sum_groups(sq);
      __builtin_amdgcn_raw_buffer_store_b128(u32x4{__float_as_uint(sa), tag, __float_as_uint(sq), tag}, rsw,
                                             g == 0 ? OFF_PART + (l * 8 + ct) * 16 : 0x7ffffff0u, 0, 16);
#pragma unroll
      for (int i = 0; i < 4; ++i) {
        const u32 give = (col & 1) ? ov[i][0] : ov[i][1];
        const u32 got = (u32)__builtin_amdgcn_update_dpp(0, (int)give, 0xB1, 0xf, 0xf, true);
        const u32x4 gv = {(col & 1) ? got : ov[i][0], tag, (col & 1) ? ov[i][1] : got, tag};
        const u32 go = OFF_XG + XG_SZ + (ct * 32 + i * 8 + g * 2 + (col & 1)) * XG_ROW + (l & ~1) * 8;
        __builtin_amdgcn_raw_buffer_store_b128(gv, rsw, go, 0, 16);
      }
    } else {
      const u32x4 t0 = tr4(ov[0][0], ov[1][0], ov[2][0], ov[3][0]), t1 = tr4(ov[0][1], ov[1][1], ov[2][1], ov[3][1]);
      const auto rso = __builtin_amdgcn_make_buffer_rsrc(A.out, 0, SL * SD * 2, 0x00020000);
      const u32 oo = (l * SD + ct * 64 + g * 16) * 2;
      __builtin_amdgcn_raw_buffer_store_b128(u32x4{t0[0], t1[0], t0[1], t1[1]}, rso, oo, 0, OUT_AUX);
      __builtin_amdgcn_raw_buffer_store_b128(u32x4{t0[2], t1[2], t0[3], t1[3]}, rso, oo + 16, 0, OUT_AUX);
    }
    }
#pragma unroll
    for (int j = 0; j < 4; ++j) asm volatile("" : "=v"(gr[j]), "=v"(xg[j]));
    TSL(12)
    TSL_FLUSH
  }
  if (b == 0 && w == 0 && fresh_lane() == 0) __hip_atomic_store((gu32*)(ws + OFF_EPOCH), tag, __ATOMIC_RELAXED, __HIP_MEMORY_SCOPE_AGENT);
}

DEV void publish_ln(char* ws, u32 off, float sum, float sumsq, u32 tag, int b, int lane) {
  float m, sd;
  ln_consts(sum8(sum), sum8(sumsq), m, sd);
  const u32 val = (__float_as_uint(m) >> 16) | (__float_as_uint(sd) & 0xffff0000u);
  __hip_atomic_store((gu64*)(ws + off + (lane & 7) * GR_SZ) + (lane >> 4) * 512 + 2 * b + ((lane >> 3) & 1),
                     ((unsigned long long)tag << 32) | val, __ATOMIC_RELAXED, __HIP_MEMORY_SCOPE_AGENT);
}

DEV void helper_waves(const Args& A, char* smem, int tid, int lw, int b KIDP) {
  TS_DECL
  char* ws = A.ws;
  const int lane = tid & 63;
  const u32 lds0 = (u32)(uintptr_t)smem;
  const auto rsw = __builtin_amdgcn_make_buffer_rsrc(ws, 0, (int)WS_USED, 0x00020000);
  const int kr = lane >> 3, no = lane & 7;
  u32 tag;
  {
    char* img = smem + KG_PACK + (lw - 1) * PK_IMG;
    if (lw <= 1) {
      const u16* src = lw == 0 ? A.x + (size_t)(8 * b + kr) * SD + no * 8
                               : A.x + (size_t)(64 * (b >> 3) + kr) * SD + (b & 7) * 64 + no * 8;
      const int stp = lw == 0 ? 64 : 8 * SD;
      u32x4 v[8];
#pragma unroll
      for (int i = 0; i < 8; ++i) v[i] = *(const u32x4*)(src + i * stp);
      tag = __builtin_amdgcn_raw_buffer_load_b32(rsw, OFF_EPOCH, 0, 16);
      if (lw == 0 && b < 10) __builtin_amdgcn_raw_buffer_store_b32(0u, rsw, OFF_CNT + (b * 64 + lane) * 128, 0, 16);
      __builtin_amdgcn_sched_barrier(0);
      bar();
      __builtin_amdgcn_sched_barrier(0);
      if (lw == 1) {
        tag += 1u;
        if (lane == 0) { lds_st(lds0 + L_MISC + 4, tag); lds_st(lds0 + L_MISC, 1u); }
#pragma unroll
        for (int i = 0; i < 8; ++i)
          *(u32x4*)(img + (8 * i + kr) * 128 + ((no ^ ((4 * i + (kr >> 1)) & 7)) * 16)) = v[i];
        wait_lgkm0();
        asm volatile("" ::: "memory");
        const int pq = kr & 3, hi = lane >> 5;
        const char* rp = img + (no + 8 * hi) * 256 + pq * 4;
        const u32 go = OFF_XG + ((b & 7) * 32 + pq) * XG_ROW + (b >> 3) * 512 + (no + 8 * hi) * 16;
#pragma unroll
        for (int i = 0; i < 16; ++i) {
          const char* a = rp + (((i & 7) ^ no) * 16) + (i >> 3) * 4096;
          const u32 r0 = *(const u32*)a, r1 = *(const u32*)(a + 128);
          __builtin_amdgcn_raw_buffer_store_b128(u32x4{r0, tag, r1, tag}, rsw, go + (i & 7) * 4 * XG_ROW + (i >> 3) * 256, 0, 16);
        }
        wait_vm<0>();
        if (lane == 0) lds_st(lds0 + L_MISC + 44, 1u);
      } else {
        float a[2] = {0.f, 0.f}, q[2] = {0.f, 0.f};
#pragma unroll
        for (int i = 0; i < 8; ++i)
#pragma unroll
          for (int j = 0; j < 4; ++j) {
            a[j & 1] = dot2bf(v[i][j], 0x3f803f80u, a[j & 1]);
            q[j & 1] = dot2bf(v[i][j], v[i][j], q[j & 1]);
          }
        lds_wait(lds0 + L_MISC, 1u);
        tag = lds_ld(lds0 + L_MISC + 4);
        wait_vm<0>();
        publish_ln(ws, OFF_GRAN, a[0] + a[1], q[0] + q[1], tag, b, lane);
      }
    } else {
      if (lw == 2) {
        const u32 t0 = __builtin_amdgcn_raw_buffer_load_b32(rsw, ((b >> 3) * TLB_N + (lane % TLB_N)) * TLB_STRIDE + TLB_STRIDE - 256, 0, 16);
        const u16 t1 = A.out[((b >> 3) * 32768) + 16384];
        __builtin_amdgcn_sched_barrier(0);
        bar();
        asm volatile("" ::"v"(t0), "v"((u32)t1));
      } else
      bar();
      __builtin_amdgcn_s_sleep(PK_DELAY);
    }
    TS(12)
    const int id = (3 - lw) * 256 + b;
    const int col = lane & 15, g = lane >> 4, q = (lane & 15) >> 2, p = lane & 3;
    const u32 ra = (u32)(uintptr_t)img + (8 * g + q) * 256 + 8 * p;
    const int rkey = q | ((g & 1) << 2);
    u32x4 fr[9];
    u32 so9[9];
#pragma unroll
    for (int i = 0; i < 9; ++i) so9[i] = 0x7ffffff0u;
    if (id < 352) {
      const int ps = id & 15, ng = id >> 4;
      const size_t so = (size_t)(32 * ps + kr) * SF + 8 * (8 * ng + no < 172 ? 8 * ng + no : 0);
      u32x4 gv[4], uv[4];
#pragma unroll
      for (int i = 0; i < 4; ++i) {
        gv[i] = *(const u32x4*)(A.wg + so + (size_t)i * 8 * SF);
        uv[i] = *(const u32x4*)(A.wu + so + (size_t)i * 8 * SF);
      }
#pragma unroll
      for (int i = 0; i < 4; ++i) {
        char* dst = img + (8 * i + kr) * 256 + ((no ^ ((kr & 3) | ((i & 1) << 2))) * 32);
        *(u32x4*)dst = u32x4{gv[i][0], uv[i][0], gv[i][1], uv[i][1]};
        *(u32x4*)(dst + 16) = u32x4{gv[i][2], uv[i][2], gv[i][3], uv[i][3]};
      }
      wait_lgkm0();
      asm volatile("" ::: "memory");
      f32x4 cs = {0, 0, 0, 0};
#pragma unroll
      for (int t8 = 0; t8 < 8; ++t8) {
        const u32x2 lo = lds_tr32(ra + ((t8 ^ rkey) * 32)), hi = lds_tr32(ra + ((t8 ^ rkey) * 32) + 1024);
        fr[t8] = u32x4{lo[0], lo[1], hi[0], hi[1]};
        const int Tt = 8 * ng + t8;
        so9[t8] = lane * 16 + OFF_WGU + wgu_off(Tt / 11, ps, Tt % 11);
        const u32 sel = col == t8 ? 0x3f803f80u : 0u;
        cs = mfma_bf16(fr[t8], u32x4{sel, sel, sel, sel}, cs);
      }
      fr[8] = __builtin_bit_cast(u32x4, cs);
      if (col < 8) so9[8] = OFF_CSP + ((ps * 176 + 8 * ng + col) * 16 + g * 4) * 4;
    } else if (id < 352 + 8 * NKF) {
      const int ct = (id - 352) & 7, kp = (id - 352) >> 3;
      u32x4 gv[4];
#pragma unroll
      for (int i = 0; i < 4; ++i)
        gv[i] = *(const u32x4*)(A.wd + (size_t)(8 * tile_of_oct(4 * kp + i) + kr) * SD + ct * 64 + no * 8);
#pragma unroll
      for (int i = 0; i < 4; ++i)
        *(u32x4*)(img + (8 * i + kr) * 256 + (((no >> 1) ^ ((kr & 3) | ((i & 1) << 2))) * 32) + (no & 1) * 16) = gv[i];
      wait_lgkm0();
      asm volatile("" ::: "memory");
#pragma unroll
      for (int nt = 0; nt < 4; ++nt) {
        const u32x2 lo = lds_tr32(ra + ((nt ^ rkey) * 32)), hi = lds_tr32(ra + ((nt ^ rkey) * 32) + 1024);
        fr[nt] = u32x4{lo[0], lo[1], hi[0], hi[1]};
        so9[nt] = lane * 16 + OFF_WD + ((ct * NKF + kp) * 4 + nt) * 1024;
      }
    }
    if (id < 352) {
#pragma unroll
      for (int i = 0; i < 8; ++i)
        if ((8 * (id >> 4) + i) % 11 < 6) __builtin_amdgcn_raw_buffer_store_b128(fr[i], rsw, so9[i], 0, 16);
      __builtin_amdgcn_raw_buffer_store_b128(fr[8], rsw, so9[8], 0, 16);
    }
    TS(13)
    lds_post(lds0 + L_MISC + 28, lane);
    lds_wait_slow(lds0 + L_MISC + 36, 4u);
    tag = lds_ld(lds0 + L_MISC + 4);
    wait_vm<0>();
    if (id < 352) {
      if (lane == 0)
        __hip_atomic_fetch_add((gu32*)(ws + OFF_CNT) + (96 + (id >> 4)) * 32, 1u, __ATOMIC_RELAXED, __HIP_MEMORY_SCOPE_AGENT);
#pragma unroll
      for (int i = 0; i < 8; ++i)
        if ((8 * (id >> 4) + i) % 11 >= 6) __builtin_amdgcn_raw_buffer_store_b128(fr[i], rsw, so9[i], 0, 16);
    } else if (id < 352 + 8 * NKF) {
#pragma unroll
      for (int i = 0; i < 4; ++i) __builtin_amdgcn_raw_buffer_store_b128(fr[i], rsw, so9[i], 0, 16);
    }
    wait_vm<0>();
    if (lds_arrive(lds0 + L_MISC + 40, lane) == 3u && lane == 0)
      __hip_atomic_fetch_add((gu32*)(ws + OFF_CNT) + (120 + (b & 7)) * 32, 1u, __ATOMIC_RELAXED, __HIP_MEMORY_SCOPE_AGENT);
  }
  TS(1)
  const int xcd = b & 7, idx = b >> 3;
  const int rb = (xcd & 1) * 8 + (idx & 7), c = (xcd >> 1) * 4 + (idx >> 3);
  const int rb64 = xcd * 4 + (idx & 3), ct = idx >> 2;
  const u32 wbase = OFF_WGU + c * (176 * 1024) + lane * 16;
  const u32 wdo = OFF_WD + ct * NKF * 4096 + (lw - 1) * 8192 + lane * 16;
  if (lw == 0) {
    u32x4 kimg[17];
    const u32 ko = lds0 + lane * 16;
#pragma unroll 1
    for (int layer = 0; layer < 2; ++layer) {
      asm volatile("" : "+s"(ws));
      const u32 xin = OFF_X + layer * X_SZ;
      if (layer == 1) {
#pragma unroll
        for (int i = 0; i < 17; ++i)
          if (i < 16 || lane < 32) *(LAS u32x4*)(ko + i * 1024) = kimg[i];
        lds_post(lds0 + L_MISC + 32, lane);
        u32x4 pv;
        __builtin_amdgcn_s_sleep(GR_DELAY);
#pragma unroll 1
        for (int sp = 0; sp < (1 << 18); ++sp) {
          pv = __builtin_amdgcn_raw_buffer_load_b128(rsw, OFF_PART + b * 1024 + lane * 16, 0, XG_AUX);
          if (__builtin_amdgcn_ballot_w64(((pv[1] ^ tag) | (pv[3] ^ tag)) != 0u) == 0ull) break;
          __builtin_amdgcn_s_sleep(3);
        }
        publish_ln(ws, OFF_GRAN + 8 * GR_SZ, __uint_as_float(pv[0]), __uint_as_float(pv[2]), tag, b, lane);
        if (lane == 0) lds_st(lds0 + L_MISC + 44, 2u);
        TSL(8)
      } else {
        lds_wait_slow(lds0 + L_MISC + 32, 4u);
#pragma unroll
        for (int i = 0; i < 17; ++i)
          if (i < 16 || lane < 32) kimg[i] = lds_rd128(ko + i * 1024);
        gu32* fp = (gu32*)(ws + OFF_CNT) + (96 + min((c * 11 >> 3) + lane, (c * 11 + 10) >> 3)) * 32;
        lds_wait_slow(lds0 + L_MISC + 28, 4u);
        __builtin_amdgcn_s_sleep(FL_DELAY);
#pragma unroll 1
        for (int sp = 0; sp < (1 << 20); ++sp) {
          u32 v = 16u;
          if (lane < 3) v = __hip_atomic_load(fp, __ATOMIC_RELAXED, __HIP_MEMORY_SCOPE_AGENT);
          if (__builtin_amdgcn_ballot_w64(v < 16u) == 0ull) break;
          __builtin_amdgcn_s_sleep(2);
        }
        asm volatile("" ::: "memory");
        if (lane == 0) lds_st(lds0 + L_MISC + 28, 5u);
        TS(2)
        f32x4 cv[6];
#pragma unroll
        for (int r = 0; r < 6; ++r) {
          const u32 co = OFF_CSP + (no * 176 + c * 11) * 64 + min(r * 8 + kr, 43) * 16;
          cv[r] = __builtin_bit_cast(f32x4, __builtin_amdgcn_raw_buffer_load_b128(rsw, co, 0, 16)) +
                  __builtin_bit_cast(f32x4, __builtin_amdgcn_raw_buffer_load_b128(rsw, co + 8 * 176 * 64, 0, 16));
        }
#pragma unroll
        for (int r = 0; r < 6; ++r) {
          const f32x4 v = {sum8(cv[r][0]), sum8(cv[r][1]), sum8(cv[r][2]), sum8(cv[r][3])};
          if (no == 0) *(f32x4*)(smem + L_CS + min(r * 8 + kr, 43) * 16) = v;
        }
        wait_lgkm0();
#pragma unroll 1
        for (int sp = 0; sp < (1 << 20); ++sp) {
          u32 v = 32u;
          if (lane < 8) v = __hip_atomic_load((gu32*)(ws + OFF_CNT) + (120 + lane) * 32, __ATOMIC_RELAXED, __HIP_MEMORY_SCOPE_AGENT);
          if (__builtin_amdgcn_ballot_w64(v < 32u) == 0ull) break;
          __builtin_amdgcn_s_sleep(8);
        }
        asm volatile("" ::: "memory");
      }
      TSL(3)
      if (lds_ld(lds0 + L_MISC + 24) < 4u * (layer + 1)) {
        lds_wait_slow(lds0 + L_MISC + 24, 4u * (layer + 1));
        __builtin_amdgcn_s_sleep(X_DELAY);
      }
      grid_wait(ws, layer, b, lane);
      TSL(4)
      u32 vo[2];
#pragma unroll
      for (int o = 0; o < 2; ++o) {
        const int seg = ((lane & 15) >> 1) ^ ((lane >> 4) | (o << 2));
        vo[o] = xin + rb * (SD * 256) + (lane >> 4) * 256 + seg * 32 + (lane & 1) * 16;
      }
      auto stage = [&](int s) {
#pragma unroll
        for (int i = 0; i < 4; ++i)
          asm volatile(
              "s_mov_b32 m0, %3\n\ts_nop 0\n\tglobal_load_lds_dwordx4 %0, %2 sc1\n\tglobal_load_lds_dwordx4 %0, %2 offset:1024 sc1\n\t"
              "global_load_lds_dwordx4 %1, %2 offset:2048 sc1\n\tglobal_load_lds_dwordx4 %1, %2 offset:3072 sc1"
              ::"v"(vo[0]), "v"(vo[1]), "s"(ws + s * 16384 + i * 4096), "s"(lds0 + (s % 3) * 16384 + i * 4096) : "memory");
      };
      stage(0); stage(1);
      static_for<0, 8>([&](auto I) {
        constexpr int i = decltype(I)::value;
        wait_vm<(i < 7 ? 16 : 0)>();
        bar();
        if constexpr (i < 6) stage(i + 2);
      });
      gu32* cs_ = (gu32*)(ws + OFF_CNT) + (layer * 48 + rb * 3) * 32;
      gu32* cw_ = (gu32*)(ws + OFF_CNT) + (layer * 48 + (rb64 >> 1) * 3) * 32;
      bar();
      bar();
      if (lane == 0) __hip_atomic_fetch_add(cs_, 1u, __ATOMIC_RELAXED, __HIP_MEMORY_SCOPE_AGENT);
      bar();
      bar();
      if (lane == 0) poll_ge(cw_, 16u);
      TSL(5)
      bar();
      if (lane == 0) __hip_atomic_fetch_add(cs_ + 32, 1u, __ATOMIC_RELAXED, __HIP_MEMORY_SCOPE_AGENT);
      bar();
      bar();
      if (lane == 0) {
        __hip_atomic_fetch_add(cs_ + 64, 1u, __ATOMIC_RELAXED, __HIP_MEMORY_SCOPE_AGENT);
        poll_ge(cw_ + 32, 16u);
      }
      bar();
      __builtin_amdgcn_s_sleep(AB_DELAY);
      if (lane == 0) poll_ge(cw_ + 64, 16u);
      TSL(6)
      bar();
      TSL(7)
      TSL_FLUSH
      if (layer == 0) bar();
    }
  } else {
    u32x4 px[8], py[8], pq[4];
#pragma unroll 1
    for (int layer = 0; layer < 2; ++layer) {
      asm volatile("" : "+s"(ws));
      const u32 xin = OFF_X + layer * X_SZ;
      lds_wait_slow(layer == 0 ? lds0 + L_MISC + 28 : lds0 + L_MISC + 36, layer == 0 ? 5u : 8u);
      TS(2)
      const u32 wo = wbase + (lw - 1) * 4096;
#pragma unroll
      for (int i = 0; i < 8; ++i) {
        if (lds_ld(lds0 + L_MISC + 48) > 4u * layer) lds_wait_slow(lds0 + L_MISC + 24, 4u * (layer + 1));
        dma4(wo, ws + i * 12288, lds0 + F_W1 + i * 12288 + (lw - 1) * 4096);
      }
      TSL(3)
      static_for<0, 8>([&](auto I) {
        constexpr int i = decltype(I)::value;
        wait_vm<(i == 0 ? 28 : 24)>();
        bar();
        if constexpr (i >= 1)
          dma4(wo, ws + (96 + (i - 1) * 12) * 1024,
               (i == 7 && lw == 3) ? lds0 + F_BASE : lds0 + F_W1 + (i - 1) * 12288 + (lw - 1) * 4096);
      });
      auto wd3 = [&](auto U, int n) {
        constexpr int u0 = decltype(U)::value;
        if (lw <= n) {
          const u32 home = lds0 + (lw == 1 ? f_wd(u0) : lw == 2 ? f_wd(u0 + 1) : f_wd(u0 + 2));
          dma4(wdo, ws + u0 * 8192, home);
          dma4(wdo, ws + u0 * 8192 + 4096, home + 4096);
        }
      };
      using std::integral_constant;
      wait_vm<16>();
      bar();
      wd3(integral_constant<int, 0>{}, 3); wd3(integral_constant<int, 3>{}, 3); wd3(integral_constant<int, 6>{}, 3);
      if (layer == 0) {
#pragma unroll
        for (int i = 0; i < 8; ++i) gload16_sc1(px[i], wdo, ws + 15 * 8192 + i * 1024);
      }
      wait_vm<24>();
      bar();
      wd3(integral_constant<int, 9>{}, 2);
      if (layer == 0) {
#pragma unroll
        for (int i = 0; i < 4; ++i) gload16_sc1(py[i], wdo, ws + 18 * 8192 + i * 1024);
      }
      bar();
      wd3(integral_constant<int, 11>{}, 2);
      if (layer == 0) {
#pragma unroll
        for (int i = 4; i < 8; ++i) gload16_sc1(py[i], wdo, ws + 18 * 8192 + i * 1024);
      }
      bar();
      wd3(integral_constant<int, 13>{}, 2);
      if (layer == 0 && lw == 3) {
#pragma unroll
        for (int i = 0; i < 4; ++i) gload16_sc1(pq[i], wdo, ws + 19 * 8192 + i * 1024);
      }
      bar();
      u32x4 rr[4];
      {
        const u32 ro = xin + ((rb64 >> 1) * SD + ct * 64 + (lw & 1) * 32 + kr) * 256 + (rb64 & 1) * 128 + no * 16;
#pragma unroll
        for (int i = 0; i < 4; ++i) rr[i] = __builtin_amdgcn_raw_buffer_load_b128(rsw, lw == 3 ? 0x7ffffff0u : ro + i * 2048, 0, 16);
      }
      wait_vm<4>();
#pragma unroll
      for (int i = 0; i < 8; ++i) asm volatile("" : "+v"(px[i]), "+v"(py[i]), "+v"(pq[i & 3]));
      bar();
      const u32 hx = lds0 + f_wd(15) + (lw - 1) * 8192 + lane * 16;
#pragma unroll
      for (int i = 0; i < 8; ++i) *(LAS u32x4*)(hx + i * 1024) = px[i];
      bar();
      const u32 hy = lds0 + (lw == 1 ? f_wd(18) : lw == 2 ? f_wd(19) : f_wd(20)) + lane * 16;
#pragma unroll
      for (int i = 0; i < 8; ++i) *(LAS u32x4*)(hy + i * 1024) = py[i];
      bar();
      if (lw == 3) {
#pragma unroll
        for (int i = 0; i < 4; ++i) *(LAS u32x4*)(lds0 + f_wd(21) + lane * 16 + i * 1024) = pq[i];
      } else {
#pragma unroll
        for (int i = 0; i < 4; ++i) *(LAS u32x4*)(lds0 + F_RES + ((lw & 1) * 32 + i * 8 + kr) * 144 + no * 16) = rr[i];
      }
      lds_bar();
      TSL(7)
      TSL_FLUSH
      if (layer == 0) bar();
    }
  }
}

__global__ __launch_bounds__(512, 2) void k_all(Args A KIDP) {
  extern __shared__ __attribute__((aligned(16))) char smem[];
  const int tid = threadIdx.x, w = __builtin_amdgcn_readfirstlane(tid >> 6), b = blockIdx.x;
  if (w == 7) {
    const char* pc;
    asm volatile("s_getpc_b64 %0" : "=s"(pc));
    const u32 line = (b >> 3) * CODE_N + (tid & 63) % CODE_N;
    asm volatile("s_mov_b32 m0, %2\n\ts_nop 0\n\tglobal_load_lds_dword %0, %1"
                 ::"v"(line * 128u), "s"(pc), "s"((u32)(uintptr_t)smem + KG_PACK + 3 * PK_IMG) : "memory");
  }
  if (w < 4) compute_waves(A, smem, tid, w, b KIDA(kid_));
  else helper_waves(A, smem, tid, w - 4, b KIDA(kid_));
}

extern "C" void kernel_launch(void* const* d_in, const int* in_sizes, int n_in, void* d_out, int out_size, void* d_ws,
                              size_t ws_size, hipStream_t stream) {
  Args A;
  A.x = (const u16*)d_in[0];
  A.logA = (const u16*)d_in[1];
  A.Aim = (const u16*)d_in[2];
  A.Cre = (const u16*)d_in[3];
  A.Cim = (const u16*)d_in[4];
  A.logdt = (const u16*)d_in[5];
  A.dskip = (const u16*)d_in[6];
  A.wg = (const u16*)d_in[8];
  A.wu = (const u16*)d_in[9];
  A.wd = (const u16*)d_in[10];
  A.out = (u16*)d_out;
  A.ws = (char*)d_ws;
  (void)hipFuncSetAttribute((const void*)k_all, hipFuncAttributeMaxDynamicSharedMemorySize, LDS_BYTES);
  k_all<<<256, 512, LDS_BYTES, stream>>>(A KIDA(0));
}
```

```cpp
#include <hip/hip_runtime.h>
#include <hip/hip_bf16.h>
#include <stdint.h>
#include <type_traits>

typedef unsigned short u16;
typedef unsigned int u32;
typedef __attribute__((ext_vector_type(8))) __bf16 bf16x8;
typedef __attribute__((ext_vector_type(2))) __bf16 bf16x2;
typedef __attribute__((ext_vector_type(8))) _Float16 f16x8;
typedef __attribute__((ext_vector_type(2))) _Float16 f16x2;
typedef __attribute__((ext_vector_type(4))) float f32x4;
typedef __attribute__((ext_vector_type(2))) float f32x2;
typedef __attribute__((ext_vector_type(4))) u32 u32x4;
typedef __attribute__((ext_vector_type(2))) u32 u32x2;
typedef __attribute__((ext_vector_type(4))) short s16x4;
typedef __attribute__((ext_vector_type(2))) double f64x2;
#define LAS __attribute__((address_space(3)))
#define DEV __device__ __forceinline__

#define TSL(i)
#define TSL_FLUSH
#define KIDP
#define KIDA(n)
#define TSP
#define TSA
#define TS_DECL
#define TS(i)
#define TS_FLUSH

constexpr int SL = 2048, SD = 512, SN = 128, SF = 1376;
constexpr int NKF = 43;
constexpr int KIMG_CH = 8448;
constexpr int KIMG_C1 = 4224;
constexpr int XG_ROW = SL * 8 + 256;

constexpr u32 OFF_WGU = 0;
constexpr u32 OFF_WD = OFF_WGU + 16 * 176 * 1024;
constexpr u32 OFF_CSP = OFF_WD + 8 * NKF * 4096;
constexpr u32 OFF_X = OFF_CSP + 16 * 176 * 64;
constexpr u32 X_SZ = SL * SD * 2;
constexpr u32 OFF_H = OFF_X + 2 * X_SZ;
#define H_SEP 1
constexpr u32 H_SZ = H_SEP * 32 * NKF * 4096;
constexpr u32 OFF_GRAN = OFF_H + 2 * 32 * NKF * 4096;
constexpr u32 GR_SZ = SL * 8 + 256;
constexpr u32 OFF_XG = OFF_GRAN + 8 * GR_SZ;
constexpr u32 OFF_PART = OFF_XG + 256 * XG_ROW;
constexpr u32 OFF_CNT = OFF_PART + SL * 128;
constexpr u32 OFF_EPOCH = OFF_CNT + 640 * 128;
constexpr u32 WS_USED = OFF_EPOCH + 256;

DEV float bflo(u32 v) { return __uint_as_float(v << 16); }
DEV float bfhi(u32 v) { return __uint_as_float(v & 0xffff0000u); }
DEV float bf2f(u16 v) { return __uint_as_float((u32)v << 16); }
DEV u32 pk_bf16(float a, float b) {
  f32x2 v = {a, b};
  return __builtin_bit_cast(u32, __builtin_convertvector(v, bf16x2));
}
DEV u32 pk_f16(float a, float b) {
  f32x2 v = {a, b};
  return __builtin_bit_cast(u32, __builtin_convertvector(v, f16x2));
}
DEV float bfr(float a) { return bflo(pk_bf16(a, a)); }
DEV float dot2bf(u32 a, u32 b, float c) {
  return __builtin_amdgcn_fdot2_f32_bf16(__builtin_bit_cast(bf16x2, a), __builtin_bit_cast(bf16x2, b), c, false);
}
DEV f32x4 mfma_bf16(u32x4 a, u32x4 b, f32x4 c) {
  return __builtin_amdgcn_mfma_f32_16x16x32_bf16(__builtin_bit_cast(bf16x8, a), __builtin_bit_cast(bf16x8, b), c, 0, 0, 0);
}
DEV f32x4 mfma_f16(u32x4 a, u32x4 b, f32x4 c) {
  return __builtin_amdgcn_mfma_f32_16x16x32_f16(__builtin_bit_cast(f16x8, a), __builtin_bit_cast(f16x8, b), c, 0, 0, 0);
}
DEV u32x2 lds_tr(const char* p) {
  return __builtin_bit_cast(u32x2, __builtin_amdgcn_ds_read_tr16_b64_v4i16((LAS s16x4*)p));
}
DEV u32x2 lds_tr32(u32 a) {
  return __builtin_bit_cast(u32x2, __builtin_amdgcn_ds_read_tr16_b64_v4i16((LAS s16x4*)a));
}
DEV u32x4 lds_rd128(u32 a) { return *(const LAS u32x4*)a; }
template <int CTRL>
DEV float dpp_f(float v) {
  return __builtin_bit_cast(float, __builtin_amdgcn_update_dpp(0, __builtin_bit_cast(int, v), CTRL, 0xf, 0xf, true));
}
DEV float sum8(float v) {
  v += dpp_f<0xB1>(v);
  v += dpp_f<0x4E>(v);
  v += dpp_f<0x141>(v);
  return v;
}
DEV float sum16(float v) { v = sum8(v); return v + dpp_f<0x140>(v); }
DEV void ln_consts(float sum, float sumsq, float& m, float& s) {
  float mean = sum * (1.0f / SD);
  float var = fmaxf(sumsq * (1.0f / SD) - mean * mean, 0.0f);
  m = bfr(mean);
  s = bfr(__builtin_amdgcn_sqrtf(bfr(bfr(var) + 1e-5f)));
}

typedef f32x2 cpx;
DEV cpx cmul(cpx a, cpx b) {
  const f32x2 t = a * f32x2{b[0], b[0]};
  return __builtin_elementwise_fma(f32x2{a[1], a[0]}, f32x2{-b[1], b[1]}, t);
}
DEV cpx cmulc(cpx a, cpx b) {
  const f32x2 t = a * f32x2{b[0], b[0]};
  return __builtin_elementwise_fma(f32x2{a[1], a[0]}, f32x2{b[1], -b[1]}, t);
}
DEV float exp_acc(float x) {
  const float t = x * 1.44269504088896340736f;
  const float e = fmaf(x, 1.44269504088896340736f, -t) + x * 1.92596299e-8f;
  const float r = __builtin_amdgcn_exp2f(t);
  return fmaf(r, e * 0.69314718056f, r);
}

typedef __attribute__((address_space(1))) u32 gu32;
typedef __attribute__((address_space(1))) unsigned long long gu64;
template <int N>
DEV void wait_vm() { asm volatile("s_waitcnt vmcnt(%0)" ::"n"(N) : "memory"); }
DEV void wait_lgkm0() { __builtin_amdgcn_s_waitcnt(0xC07F); }
DEV void bar() {
  asm volatile("" ::: "memory");
  __builtin_amdgcn_s_barrier();
  asm volatile("" ::: "memory");
}
template <int N>
DEV void wait_lgkm() { __builtin_amdgcn_s_waitcnt(0xC07F | (N << 8)); }
DEV void lds_bar() { wait_lgkm0(); bar(); }
template <int I, int N, class Fn>
DEV void static_for(Fn&& f) {
  if constexpr (I < N) { f(std::integral_constant<int, I>{}); static_for<I + 1, N>(f); }
}
DEV int wgu_off(int c, int s, int t) { return (c * 176 + (t < 6 ? s * 6 + t : 96 + (t - 6) * 16 + s)) << 10; }
DEV int tile_of_oct(int oct) {
  return oct < 96 ? (oct / 6) * 11 + oct % 6
       : oct < 127 ? ((oct - 96) >> 1) * 11 + 6 + ((oct - 96) & 1) : ((oct - 127) / 3) * 11 + 8 + (oct - 127) % 3;
}
DEV float s_fma(float a, float b, float c) { float d; asm("v_fma_f32 %0, %1, %2, %3" : "=v"(d) : "v"(a), "v"(b), "v"(c)); return d; }
DEV float s_nfma(float a, float b, float c) { float d; asm("v_fma_f32 %0, -%1, %2, %3" : "=v"(d) : "v"(a), "v"(b), "v"(c)); return d; }
DEV float s_mul(float a, float b) { float d; asm("v_mul_f32 %0, %1, %2" : "=v"(d) : "v"(a), "v"(b)); return d; }

constexpr int CV_U = 2 * KIMG_CH;
constexpr int CV_UROW = (128 + 1024) * 2;
constexpr int CV_P = CV_U + 4 * CV_UROW;
constexpr int CV_PROW = 2048 * 4 + 64 * 16 + 16;
constexpr int L_MISC = CV_P + 4 * CV_PROW;
constexpr int L_CS = L_MISC + 64;
constexpr int L_DUMP = L_CS + 704;
constexpr int KG_CH = 64 * 512;
constexpr int KG_TAB = KG_CH + 53 * 512;
constexpr int KG_TROW = 672 * 2;
constexpr int KG_PACK = 65536;
constexpr int PK_IMG = 8192;
constexpr int F_W1 = 65536;
constexpr int LDS_BYTES = 163840;
constexpr int F_BASE = 57344;
static_assert(L_DUMP + 256 <= 65536 && KG_TAB + 2 * KG_TROW <= L_MISC, "lds map");
constexpr int f_tile(int t) { return F_W1 + (t - 6) * 16384; }
constexpr int f_wd(int u) {
  return u < 7 ? u * 8192 : u < 9 ? F_W1 + 81920 + (u - 7) * 8192 : u < 19 ? F_W1 + (u - 9) * 8192 : f_wd(u - 19);
}
constexpr int F_RES = f_wd(4);

DEV int fresh_lane() {
  int l;
  asm volatile("v_mbcnt_lo_u32_b32 %0, -1, 0\n\tv_mbcnt_hi_u32_b32 %0, -1, %0" : "=v"(l));
  return l;
}
DEV u32 opq(u32 v) { asm volatile("" : "+v"(v)); return v; }
DEV int chan_pair(int b) { return (b & 7) * 32 + (b >> 3); }
DEV u32 lds_ld(u32 a) { return *(volatile LAS u32*)a; }
DEV void lds_st(u32 a, u32 v) { *(volatile LAS u32*)a = v; }
DEV u32 lds_arrive(u32 word, int lane) {
  wait_lgkm0();
  asm volatile("" ::: "memory");
  u32 old = 0;
  if (lane == 0) old = __hip_atomic_fetch_add((LAS u32*)word, 1u, __ATOMIC_RELAXED, __HIP_MEMORY_SCOPE_WORKGROUP);
  return __builtin_amdgcn_readfirstlane(old);
}
DEV void lds_bump(u32 word) {
  asm volatile("s_mov_b64 exec, 1\n\tds_add_u32 %0, %1\n\ts_mov_b64 exec, -1" ::"v"(word), "v"(1u) : "memory");
}
DEV void lds_post(u32 word, int lane) { lds_bump(word); }
DEV void lds_wait(u32 word, u32 want) {
#pragma unroll 1
  for (int sp = 0; sp < (1 << 24); ++sp)
    if (lds_ld(word) >= want) break;
  asm volatile("" ::: "memory");
}
DEV void lds_wait_slow(u32 word, u32 want) {
#pragma unroll 1
  for (int sp = 0; sp < (1 << 20); ++sp) {
    if (lds_ld(word) >= want) break;
    __builtin_amdgcn_s_sleep(2);
  }
  asm volatile("" ::: "memory");
}
#define XG_AUX 16
#define GR_AUX 16
#define TLB_WARM 1
#define TLB_STRIDE 65536
#define TLB_N 12
#define OUT_AUX 16
#define CODE_WARM 1
#define CODE_N 11
#define PRE_TOUCH 0
#define GRID2 1
#define FL_DELAY 10
#define G_REP 32
#define G_SH 8
#define GW_SLEEP 0
#define GR_DELAY2 6
#define X_DELAY 8
#define HPRIO 3
#define PF_DELAY 16
#define PK_DELAY 0
#define GR_SLEEP 8
#define GR_DELAY 8
DEV void grid_signal(char* ws, int k, int b, int lane) {
  u32 old = 0;
  if (lane == 0)
    old = __hip_atomic_fetch_add((gu32*)(ws + OFF_CNT) + (128 + k * 256 + (b % G_SH)) * 32, 1u, __ATOMIC_RELAXED,
                                 __HIP_MEMORY_SCOPE_AGENT);
  if (__builtin_amdgcn_readfirstlane(old) == 256u / G_SH - 1u && lane < G_REP)
    __hip_atomic_fetch_add((gu32*)(ws + OFF_CNT) + (128 + k * 256 + 64 + lane) * 32, 1u, __ATOMIC_RELAXED,
                           __HIP_MEMORY_SCOPE_AGENT);
}
DEV void grid_wait(char* ws, int k, int b, int lane) {
  gu32* p = (gu32*)(ws + OFF_CNT) + (128 + k * 256 + 64 + (b % G_REP)) * 32;
#pragma unroll 1
  for (int sp = 0; sp < (1 << 20); ++sp) {
    u32 v = G_SH;
    if (lane == 0) v = __hip_atomic_load(p, __ATOMIC_RELAXED, __HIP_MEMORY_SCOPE_AGENT);
    if (__builtin_amdgcn_readfirstlane(v) >= G_SH) break;
    __builtin_amdgcn_s_sleep(GW_SLEEP);
  }
  asm volatile("" ::: "memory");
}
DEV void poll_ge(gu32* cp, u32 want) {
#pragma unroll 1
  for (int sp = 0; sp < (1 << 20); ++sp) {
    if (__hip_atomic_load(cp, __ATOMIC_RELAXED, __HIP_MEMORY_SCOPE_AGENT) >= want) break;
  }
}
DEV void gload16_sc1(u32x4& dst, u32 voff, const char* base) {
  asm volatile("global_load_dwordx4 %0, %1, %2 sc1" : "=v"(dst) : "v"(voff), "s"(base) : "memory");
}
DEV void dma2(u32 voff, const char* base, u32 lds) {
  asm volatile("s_mov_b32 m0, %2\n\ts_nop 0\n\tglobal_load_lds_dwordx4 %0, %1 sc1\n\t"
               "global_load_lds_dwordx4 %0, %1 offset:1024 sc1" ::"v"(voff), "s"(base), "s"(lds) : "memory");
}
DEV void dma4(u32 voff, const char* base, u32 lds) {
  asm volatile("s_mov_b32 m0, %2\n\ts_nop 0\n\tglobal_load_lds_dwordx4 %0, %1 sc1\n\t"
               "global_load_lds_dwordx4 %0, %1 offset:1024 sc1\n\tglobal_load_lds_dwordx4 %0, %1 offset:2048 sc1\n\t"
               "global_load_lds_dwordx4 %0, %1 offset:3072 sc1" ::"v"(voff), "s"(base), "s"(lds) : "memory");
}
DEV cpx cpow2(float a2, float rh, float rl, float s) {
  const float mag = __builtin_amdgcn_exp2f(a2 * s);
  const float rev = fmaf(rl, s, __builtin_amdgcn_fractf(rh * s));
  return f32x2{__builtin_amdgcn_cosf(rev), __builtin_amdgcn_sinf(rev)} * f32x2{mag, mag};
}
DEV u32 glu_tile(f32x4 acc, f32x4 cs, float m, float s, float c1) {
  const f32x2 nm = {-m, -m};
  const f32x2 gg = __builtin_elementwise_fma(nm, f32x2{cs[0], cs[1]}, f32x2{acc[0], acc[1]}) * f32x2{c1, c1};
  const f32x2 uu = __builtin_elementwise_fma(nm, f32x2{cs[2], cs[3]}, f32x2{acc[2], acc[3]});
  const f32x2 ex = {__builtin_amdgcn_exp2f(gg[0]), __builtin_amdgcn_exp2f(gg[1])};
  const f32x2 dn = __builtin_elementwise_fma(ex, f32x2{s, s}, f32x2{s, s});
  const f32x2 hh = uu * f32x2{__builtin_amdgcn_rcpf(dn[0]), __builtin_amdgcn_rcpf(dn[1])};
  return pk_bf16(hh[0], hh[1]);
}
DEV u32x4 tr4(u32 r0, u32 r1, u32 r2, u32 r3) {
  auto a = __builtin_amdgcn_permlane32_swap(r0, r2, false, false);
  auto b = __builtin_amdgcn_permlane32_swap(r1, r3, false, false);
  auto c = __builtin_amdgcn_permlane16_swap(a[0], b[0], false, false);
  auto d = __builtin_amdgcn_permlane16_swap(a[1], b[1], false, false);
  return u32x4{c[0], c[1], d[0], d[1]};
}
DEV float sum_groups(float v) {
  auto a = __builtin_amdgcn_permlane32_swap(__float_as_uint(v), __float_as_uint(v), false, false);
  v = __uint_as_float(a[0]) + __uint_as_float(a[1]);
  auto b = __builtin_amdgcn_permlane16_swap(__float_as_uint(v), __float_as_uint(v), false, false);
  return __uint_as_float(b[0]) + __uint_as_float(b[1]);
}

#define F_WIN 18
#define F_PRE 10
#define F_RATE 2
#define F_U1 6
#define F_U2 16
#define F_U3 26
#define AB_DELAY 0

struct Args {
  const u16 *x, *logA, *Aim, *Cre, *Cim, *logdt, *dskip, *wg, *wu, *wd;
  u16* out;
  char* ws;
};

DEV u32 conv_ka(u32 lds0, int lane, int w) {
  const int h = lane >> 4, q = (lane & 15) >> 2, p = lane & 3;
  return lds0 + (w >> 1) * KIMG_CH + ((q & 1) ? KIMG_C1 + 2 * 18 : 2 * 16) + 2 * (48 - 16 * h - 2 * q + 4 * p) - 16;
}
DEV void conv_mfma(u32 lds0, u32 ka, const u32x4 (&k0)[8], int lane, int w TSP) {
  const int np = lane & 15, h = lane >> 4;
  u32 ua = lds0 + CV_U + w * CV_UROW + 256 + 2 * (8 * (np + h) - 24) - 192;
  u32 pp = lds0 + CV_P + w * CV_PROW + 16 + np * 64 + h * 16 + (np >> 1) * 16;
  asm volatile("" : "+v"(ua), "+v"(pp));
  f32x4 acc[8];
#pragma unroll
  for (int j = 0; j < 8; ++j) acc[j] = f32x4{0, 0, 0, 0};
  u32x4 kf[2][8], uf[2][8];
  constexpr auto pair_i = [](int n) { int i = 0; while (n >= 8 - i) { n -= 8 - i; ++i; } return i; };
  constexpr auto pair_j = [](int n) { int i = 0; while (n >= 8 - i) { n -= 8 - i; ++i; } return i + n; };
  auto rd1 = [&](auto D, auto N) {
    constexpr int dl = decltype(D)::value, n = decltype(N)::value;
    if constexpr (n >= 2 && n < 10) uf[dl & 1][n - 2] = lds_rd128(ua + (n - 2) * 256 + (3 - dl) * 64);
    else {
      constexpr int m = n < 2 ? n : n - 8, i = m >> 1;
      const u32x2 v = lds_tr32(ka + (4 * i + dl) * 128 + ((m & 1) ? 0 : 16));
      kf[dl & 1][i][2 * (m & 1)] = v[0]; kf[dl & 1][i][2 * (m & 1) + 1] = v[1];
    }
  };
#pragma unroll
  for (int i = 0; i < 8; ++i) kf[0][i] = k0[i];
  static_for<2, 10>([&](auto N) { rd1(std::integral_constant<int, 0>{}, N); });
  __builtin_amdgcn_sched_barrier(0);
  static_for<0, 4>([&](auto D) {
    constexpr int dl = decltype(D)::value;
    if constexpr (dl > 0) wait_lgkm0();
    if constexpr (dl == 3) lds_bump(lds0 + L_MISC + 48);
    __builtin_amdgcn_sched_barrier(0);
    static_for<0, 36>([&](auto N) {
      constexpr int n = decltype(N)::value, g0 = n < 15 ? 0 : n < 26 ? 15 : n < 33 ? 26 : 33, jh = n < 15 ? 7 : n < 26 ? 5 : n < 33 ? 3 : 1;
      constexpr int i = dl < 3 ? pair_i(n) : (n - g0) >> 1, j = dl < 3 ? pair_j(n) : jh - ((n - g0) & 1);
      acc[j] = mfma_bf16(kf[dl & 1][i], uf[dl & 1][j - i], acc[j]);
      constexpr int r0 = dl == 0 ? 4 : 0;
      if constexpr (dl < 3 && n >= r0 && n < r0 + 24)
        rd1(std::integral_constant<int, dl + 1>{}, std::integral_constant<int, n - r0>{});
      if constexpr (dl == 3 && jh < 7 && n - g0 == 1) *(LAS f32x4*)(pp + (jh + 1) * (1024 + 128)) = acc[jh + 1];
      if constexpr (dl == 3 && jh < 7 && n - g0 == (jh > 1 ? 3 : 2)) *(LAS f32x4*)(pp + (jh + 2) * (1024 + 128)) = acc[jh + 2];
      __builtin_amdgcn_sched_barrier(0);
    });
  });
  *(LAS f32x4*)pp = acc[0];
  *(LAS f32x4*)(pp + 1024 + 128) = acc[1];
}

DEV void compute_waves(const Args& A, char* smem, int tid, int w, int b KIDP) {
  TS_DECL
  char* ws = A.ws;
  int lane = tid & 63;
  const u32 lds0 = (u32)(uintptr_t)smem;
  const auto rsw = __builtin_amdgcn_make_buffer_rsrc(ws, 0, (int)WS_USED, 0x00020000);
  const int d0 = 2 * chan_pair(b);
  const u32 v_dsk = *(const u32*)(A.dskip + d0);
  u32x4 gr[4], xg[4];
  bool early = false;
  auto try_early = [&]() {
    if (!early && __builtin_amdgcn_readfirstlane(lds_ld(lds0 + L_MISC + 44)) != 0u) {
      early = true;
#pragma unroll
      for (int j = 0; j < 4; ++j)
        xg[j] = __builtin_amdgcn_raw_buffer_load_b128(rsw, OFF_XG + (d0 >> 1) * XG_ROW + tid * 64 + j * 16, 0, XG_AUX);
#pragma unroll
      for (int j = 0; j < 4; ++j)
        gr[j] = __builtin_amdgcn_raw_buffer_load_b128(rsw, OFF_GRAN + (b & 7) * GR_SZ + tid * 16 + j * 4096, 0, GR_AUX);
    }
  };
  {
    const int ch = w >> 1, n = tid & 127;
    const u32 pi = d0 * SN + tid;
    const u16 l_dt = A.logdt[d0 + ch], l_a = A.logA[pi], l_b = A.Aim[pi];
    const u16 l_cr = A.Cre[pi], l_ci = A.Cim[pi], l_cr2 = A.Cre[pi ^ 64], l_ci2 = A.Cim[pi ^ 64];
    if (tid < 16) *(u32*)(smem + L_MISC + tid * 4) = 0u;
    if (n < 8) *(u32*)(smem + KG_TAB + ch * KG_TROW + n * 4) = 0u;
    __builtin_amdgcn_sched_barrier(0);
    u32 wr[8];
#pragma unroll
    for (int r = 0; r < 8; ++r) { wr[r] = lds0 + ch * KG_CH + r * 512 + ((n * 4) ^ (r << 4)); asm volatile("" : "+v"(wr[r])); }
    const int xt = w & 1, r16 = lane & 15, g = lane >> 4;
    u32 fa[2], fb[2], fc[2];
#pragma unroll
    for (int o = 0; o < 2; ++o) {
      const int rc = min(r16, 4);
      fa[o] = lds0 + ch * KG_CH + (16 * xt + r16) * 512 + (((o * 4 + g) ^ (r16 & 7)) << 4);
      fb[o] = lds0 + ch * KG_CH + (32 + r16) * 512 + (((o * 4 + g) ^ (r16 & 7)) << 4);
      fc[o] = lds0 + ch * KG_CH + (48 + rc) * 512 + (((o * 4 + g) ^ rc) << 4);
      asm volatile("" : "+v"(fa[o]), "+v"(fb[o]), "+v"(fc[o]));
    }
    const int i0 = 16 * xt + 4 * g;
    const int ts2 = r16 < 8 ? 16 : (r16 < 12 ? 8 : 4);
    const int tb = r16 < 8 ? r16 : (r16 < 12 ? 248 + r16 : 384 + 64 * ((r16 >> 1) & 1) + (r16 & 1));
    u32 ta[4];
#pragma unroll
    for (int e = 0; e < 4; ++e) { ta[e] = lds0 + KG_TAB + ch * KG_TROW + 32 + tb * 2 + ts2 * (i0 + e); asm volatile("" : "+v"(ta[e])); }
    u32 tc = lds0 + KG_TAB + ch * KG_TROW + 32 + 2 * ((r16 < 4 || i0 == 0) ? 512 + 32 * min(r16, 4) + i0 : 648);
    asm volatile("" : "+v"(tc));
    u32 ia[10];
    {
      const float f0 = (float)(8 * tid);
#pragma unroll
      for (int j = 0; j < 5; ++j) {
        const u32 bits = pk_bf16(f0 + (float)(2 * j - 2), f0 + (float)(2 * j - 1));
        const u32 lin = lds0 + KG_TAB + (8 * tid + 2 * j - 2 + 16) * 2;
        ia[2 * j] = tid < 24 ? lin : (bits & 0xffffu) * 2 + (lds0 + KG_TAB - (0x4300 - 144) * 2);
        ia[2 * j + 1] = tid < 24 ? lin + 2 : (bits >> 16) * 2 + (lds0 + KG_TAB - (0x4300 - 144) * 2);
        asm volatile("" : "+v"(ia[2 * j]), "+v"(ia[2 * j + 1]));
      }
    }
    __builtin_amdgcn_sched_barrier(0);
    lds_bar();
    __builtin_amdgcn_sched_barrier(0);
    const u32 ea = pk_bf16(exp_acc(bf2f(l_dt)), exp_acc(bf2f(l_a)));
    const float dt = bflo(ea);
    const float a2 = -(bfhi(ea) * dt) * 1.44269504088896340736f;
    const float bi = bf2f(l_b) * dt;
    const float rh = bi * 0.15915494309189533577f;
    const float rl = fmaf(bi, 6.420638e-9f, fmaf(bi, 0.15915494309189533577f, -rh));
    u32 mx = max(max((u32)l_cr & 0x7fffu, (u32)l_ci & 0x7fffu), max((u32)l_cr2 & 0x7fffu, (u32)l_ci2 & 0x7fffu));
    mx = max(mx, (u32)__builtin_amdgcn_update_dpp(0, (int)mx, 0xB1, 0xf, 0xf, true));
    mx = max(mx, (u32)__builtin_amdgcn_update_dpp(0, (int)mx, 0x4E, 0xf, 0xf, true));
    mx = max(mx, (u32)__builtin_amdgcn_update_dpp(0, (int)mx, 0x141, 0xf, 0xf, true));
    mx = max(mx, (u32)__builtin_amdgcn_update_dpp(0, (int)mx, 0x140, 0xf, 0xf, true));
    mx = max(max((u32)__builtin_amdgcn_readlane((int)mx, 0), (u32)__builtin_amdgcn_readlane((int)mx, 16)),
             max((u32)__builtin_amdgcn_readlane((int)mx, 32), (u32)__builtin_amdgcn_readlane((int)mx, 48)));
    const u32 ex = min(max(mx >> 7, 2u), 252u) + 1u;
    const float fs = 2.0f * dt * __uint_as_float(ex << 23), isc = __uint_as_float((254u - ex) << 23);
    cpx z[12];
#pragma unroll
    for (int p = 0; p < 12; p += 3) {
      z[p] = cpow2(a2, rh, rl, (float)(1 << p));
      z[p + 1] = cmul(z[p], z[p]);
      z[p + 2] = cmul(z[p + 1], z[p + 1]);
    }
    {
      cpx V[56];
      auto put = [&](int i) { *(LAS u32*)(wr[i & 7] + (i >> 3) * 4096) = pk_f16(V[i][0], V[i][1]); };
      V[0] = cpx{1.0f, 0.0f};
      V[1] = z[3]; V[2] = z[4]; V[4] = z[5]; V[8] = z[6]; V[16] = z[7];
      V[32] = cpx{bf2f(l_cr) * isc, bf2f(l_ci) * -isc};
      constexpr int OP[47][3] = {
          {3, 1, 4},   {5, 1, 5},   {6, 2, 5},   {33, 32, 0}, {34, 32, 1}, {36, 32, 2}, {40, 32, 8}, {44, 32, 9},
          {7, 3, 5},   {9, 1, 6},   {10, 2, 6},  {11, 3, 6},  {35, 34, 0}, {37, 36, 0}, {38, 36, 1}, {41, 34, 8},
          {12, 4, 6},  {13, 5, 6},  {14, 6, 6},  {15, 7, 6},  {39, 38, 0}, {42, 36, 8}, {43, 38, 8}, {45, 36, 9},
          {17, 1, 7},  {18, 2, 7},  {19, 3, 7},  {20, 4, 7},  {46, 40, 9}, {47, 42, 9}, {48, 32, 10}, {49, 40, 10},
          {21, 5, 7},  {22, 6, 7},  {23, 7, 7},  {24, 8, 7},  {50, 44, 10}, {51, 46, 10}, {52, 32, 11}, {25, 9, 7},
          {26, 10, 7}, {27, 11, 7}, {28, 12, 7}, {29, 13, 7}, {30, 14, 7}, {31, 15, 7}, {0, 0, 0}};
      put(0); put(1); put(2); put(4); put(8); put(16); put(32);
      static_for<0, 13>([&](auto G) {
        constexpr int g4 = decltype(G)::value * 4;
        f32x2 t[4];
#pragma unroll
        for (int k = 0; k < 4; ++k)
          if (g4 + k < 46) t[k] = V[OP[g4 + k][1]] * f32x2{z[OP[g4 + k][2]][0], z[OP[g4 + k][2]][0]};
#pragma unroll
        for (int k = 0; k < 4; ++k)
          if (g4 >= 4 && g4 - 4 + k < 46) put(OP[g4 - 4 + k][0]);
#pragma unroll
        for (int k = 0; k < 4; ++k)
          if (g4 + k < 46) {
            const cpx a = V[OP[g4 + k][1]];
            const float zi = z[OP[g4 + k][2]][1];
            V[OP[g4 + k][0]] = __builtin_elementwise_fma(f32x2{a[1], a[0]}, OP[g4 + k][0] < 32 ? f32x2{-zi, zi} : f32x2{zi, -zi}, t[k]);
          }
        __builtin_amdgcn_sched_barrier(0);
      });
    }
    lds_post(lds0 + L_MISC + 8 + ch * 4, lane);
    lds_wait(lds0 + L_MISC + 8 + ch * 4, 2u);
    {
      f32x4 acc0 = {0, 0, 0, 0}, acc1 = {0, 0, 0, 0};
#pragma unroll
      for (int s = 0; s < 8; ++s) {
        const u32x4 a = lds_rd128(fa[s & 1] + (s >> 1) * 128);
        acc0 = mfma_f16(a, lds_rd128(fb[s & 1] + (s >> 1) * 128), acc0);
        acc1 = mfma_f16(a, lds_rd128(fc[s & 1] + (s >> 1) * 128), acc1);
      }
      try_early();
      acc0 *= fs; acc1 *= fs;
      const u32 v01 = pk_bf16(acc0[0], acc0[1]), v23 = pk_bf16(acc0[2], acc0[3]);
      *(LAS u16*)ta[0] = (u16)v01; *(LAS u16*)ta[1] = (u16)(v01 >> 16);
      *(LAS u16*)ta[2] = (u16)v23; *(LAS u16*)ta[3] = (u16)(v23 >> 16);
      *(LAS u32x2*)tc = u32x2{pk_bf16(acc1[0], acc1[1]), pk_bf16(acc1[2], acc1[3])};
    }
    lds_post(lds0 + L_MISC + 16, lane);
    lds_wait(lds0 + L_MISC + 16, 4u);
    try_early();
#pragma unroll
    for (int cc = 0; cc < 2; ++cc) {
      u32 kv[5];
#pragma unroll
      for (int j = 0; j < 5; ++j)
        kv[j] = (u32)*(const LAS u16*)(ia[2 * j] + cc * KG_TROW) | ((u32)*(const LAS u16*)(ia[2 * j + 1] + cc * KG_TROW) << 16);
      char* o0 = smem + cc * KIMG_CH + 32 + tid * 16;
      *(u32x4*)o0 = u32x4{kv[1], kv[2], kv[3], kv[4]};
      *(u32x4*)(o0 + KIMG_C1) = u32x4{kv[0], kv[1], kv[2], kv[3]};
    }
    if (tid < 8) *(u32x4*)(smem + (tid >> 2) * KIMG_CH + ((tid >> 1) & 1) * KIMG_C1 + (tid & 1) * 16) = u32x4{0, 0, 0, 0};
    lds_post(lds0 + L_MISC + 52, lane);
    lds_wait_slow(lds0 + L_MISC + 44, 1u);
    try_early();
  }
  const u32 l_dsk = __builtin_amdgcn_readfirstlane(v_dsk);
  lds_wait(lds0 + L_MISC, 1u);
  const u32 tag = __builtin_amdgcn_readfirstlane(lds_ld(lds0 + L_MISC + 4));
  TS(1)
  const int xcd = b & 7, idx = b >> 3;
  const int rb = (xcd & 1) * 8 + (idx & 7), c = (xcd >> 1) * 4 + (idx >> 3);
  const int rb64 = xcd * 4 + (idx & 3), ct = idx >> 2;

#pragma unroll 1
  for (int layer = 0; layer < 2; ++layer) {
    {
      lane = fresh_lane();
      tid = w * 64 + lane;
      const u32 goff = OFF_GRAN + (b & 7) * GR_SZ + tid * 16, xoff = OFF_XG + (d0 >> 1) * XG_ROW + tid * 64;
      lds_wait_slow(lds0 + L_MISC + 52, 4u + layer);
      u32 ka = conv_ka(lds0, lane, w);
      asm volatile("" : "+v"(ka));
      u32x4 k0[8];
#pragma unroll
      for (int i = 0; i < 8; ++i) {
        const u32x2 lo = lds_tr32(ka + i * 512 + 16), hi = lds_tr32(ka + i * 512);
        k0[i] = u32x4{lo[0], lo[1], hi[0], hi[1]};
      }
      __builtin_amdgcn_sched_barrier(0);
      const u32 ltag = tag + layer;
      bool needx = !early, needg = !early;
      early = false;
      lds_wait_slow(lds0 + L_MISC + 44, 1u + layer);
#pragma unroll 1
      for (int sp = 0; sp < (1 << 18); ++sp) {
        if (needx) {
#pragma unroll
          for (int j = 0; j < 4; ++j) xg[j] = __builtin_amdgcn_raw_buffer_load_b128(rsw, xoff + j * 16, 0, XG_AUX);
        }
        if (needg) {
          __builtin_amdgcn_s_sleep(GR_DELAY2);
#pragma unroll
          for (int j = 0; j < 4; ++j) gr[j] = __builtin_amdgcn_raw_buffer_load_b128(rsw, goff + j * 4096, 0, GR_AUX);
        }
        u32 bx = 0, bg = 0;
#pragma unroll
        for (int j = 0; j < 4; ++j) { bx |= (xg[j][1] ^ ltag) | (xg[j][3] ^ ltag); bg |= (gr[j][1] ^ ltag) | (gr[j][3] ^ ltag); }
        needx = __builtin_amdgcn_ballot_w64(bx != 0u) != 0ull;
        needg = __builtin_amdgcn_ballot_w64(bg != 0u) != 0ull;
        if (!needx && !needg) break;
        __builtin_amdgcn_s_sleep(GR_SLEEP);
      }
      lds_bump(lds0 + L_MISC + 36);
      TSL(2)
      float base[2][8];
#pragma unroll
      for (int r = 0; r < 2; ++r) {
        u32 up[2][2];
#pragma unroll
        for (int j = 0; j < 2; ++j) {
          const u32 g0 = gr[2 * j][2 * r], g1 = gr[2 * j + 1][2 * r], x0 = xg[2 * j][2 * r], x1 = xg[2 * j + 1][2 * r];
          const f32x2 rs = {__builtin_amdgcn_rcpf(bfhi(g0)), __builtin_amdgcn_rcpf(bfhi(g1))};
          const f32x2 mm = {bflo(g0), bflo(g1)};
#pragma unroll
          for (int cc = 0; cc < 2; ++cc) {
            const f32x2 xx = cc ? f32x2{bfhi(x0), bfhi(x1)} : f32x2{bflo(x0), bflo(x1)};
            const float dk = cc ? bfhi(l_dsk) : bflo(l_dsk);
            const f32x2 u = (xx - mm) * rs;
            up[cc][j] = pk_bf16(u[0], u[1]);
            const f32x2 bs = __builtin_elementwise_fma(u, f32x2{dk, dk}, xx);
            base[cc][4 * j + r] = bs[0]; base[cc][4 * j + r + 2] = bs[1];
          }
        }
#pragma unroll
        for (int cc = 0; cc < 2; ++cc)
          *(u32x2*)(smem + CV_U + (cc * 2 + r) * CV_UROW + 256 + tid * 8) = u32x2{up[cc][0], up[cc][1]};
      }
      {
        const u32 zz = opq(0u);
        *(u32*)(smem + CV_U + w * CV_UROW + lane * 4) = zz;
        *(u32*)(smem + CV_P + w * CV_PROW + 12) = zz;
      }
      lds_post(lds0 + L_MISC + 32, lane);
      lds_wait(lds0 + L_MISC + 32, 4u * (layer + 1));
      TSL(3)
      conv_mfma(lds0, ka, k0, lane, w TSA);
      lds_post(lds0 + L_MISC + 20, lane);
      lds_wait(lds0 + L_MISC + 20, 4u * (layer + 1));
      TSL(4)
      const int off = CV_P + 16 + tid * 32 + (tid >> 2) * 16;
      const int offm = off + CV_PROW - (((tid & 3) == 0 && tid > 0) ? 20 : 4);
      const u32 xo = OFF_X + layer * X_SZ + ((tid >> 4) * SD + d0) * 256 + (tid & 15) * 16;
#pragma unroll
      for (int cc = 0; cc < 2; ++cc) {
        const char* p0 = smem + off + (2 * cc) * CV_PROW;
        const f32x4 a0 = *(const f32x4*)p0, a1 = *(const f32x4*)(p0 + 16);
        const f32x4 b0 = *(const f32x4*)(p0 + CV_PROW), b1 = *(const f32x4*)(p0 + CV_PROW + 16);
        const float bm = *(const float*)(smem + offm + (2 * cc) * CV_PROW);
        const float o[8] = {base[cc][0] + a0[0] + bm,    base[cc][1] + a0[1] + b0[0], base[cc][2] + a0[2] + b0[1],
                            base[cc][3] + a0[3] + b0[2], base[cc][4] + a1[0] + b0[3], base[cc][5] + a1[1] + b1[0],
                            base[cc][6] + a1[2] + b1[1], base[cc][7] + a1[3] + b1[2]};
        const u32x4 ov = {pk_bf16(o[0], o[1]), pk_bf16(o[2], o[3]), pk_bf16(o[4], o[5]), pk_bf16(o[6], o[7])};
        __builtin_amdgcn_raw_buffer_store_b128(ov, rsw, xo + cc * 256, 0, 16);
      }
      wait_vm<0>();
      if (lds_arrive(lds0 + L_MISC + 24, lane) == 4u * layer + 3u) grid_signal(ws, layer, b, lane);
      TSL(5)
    }
    lane = fresh_lane();
    asm volatile("" : "+s"(ws));
    const int mq = w;
    int col = lane & 15, g = lane >> 4, q = (lane & 15) >> 2, p = lane & 3;
    const u32 hoff = OFF_H + layer * H_SZ;
    const int key = q | ((g & 1) << 2);
    u32 xa[2], wa[3];
#pragma unroll
    for (int j = 0; j < 2; ++j) {
      xa[j] = lds0 + (8 * g + q) * 256 + (((2 * mq + j) ^ key) * 32) + 8 * p;
      asm volatile("" : "+v"(xa[j]));
    }
#pragma unroll
    for (int r = 0; r < 3; ++r) {
      wa[r] = lds0 + r * F_BASE + lane * 16;
      asm volatile("" : "+v"(wa[r]));
    }
    auto lw128 = [&](auto O) { constexpr int o = decltype(O)::value; return lds_rd128(wa[o / F_BASE] + o % F_BASE); };
    f32x4 acc[11][2];
#pragma unroll
    for (int t = 0; t < 11; ++t)
#pragma unroll
      for (int j = 0; j < 2; ++j) acc[t][j] = f32x4{0, 0, 0, 0};
    f32x4 s1[2] = {{0, 0, 0, 0}, {0, 0, 0, 0}}, s2[2] = {{0, 0, 0, 0}, {0, 0, 0, 0}};
    const u32x4 ones = {0x3f803f80u, 0x3f803f80u, 0x3f803f80u, 0x3f803f80u};
    u32x4 xf[16][2], wf[2][6];
    auto rd = [&](auto K) {
      constexpr int k = decltype(K)::value, o = ((k >> 1) % 3) * 16384 + (k & 1) * 8192;
#pragma unroll
      for (int j = 0; j < 2; ++j) {
        const u32x2 lo = lds_tr32(xa[j] + o), hi = lds_tr32(xa[j] + o + 1024);
        xf[k][j] = u32x4{lo[0], lo[1], hi[0], hi[1]};
      }
      static_for<0, 6>([&](auto T) {
        constexpr int t = decltype(T)::value;
        wf[k & 1][t] = lw128(std::integral_constant<int, F_W1 + (k * 6 + t) * 1024>{});
      });
    };
    auto mm = [&](auto K) {
      constexpr int k = decltype(K)::value;
#pragma unroll
      for (int j = 0; j < 2; ++j) {
        s2[j] = mfma_bf16(xf[k][j], xf[k][j], s2[j]);
        s1[j] = mfma_bf16(ones, xf[k][j], s1[j]);
      }
#pragma unroll
      for (int t = 0; t < 6; ++t)
#pragma unroll
        for (int j = 0; j < 2; ++j) acc[t][j] = mfma_bf16(wf[k & 1][t], xf[k][j], acc[t][j]);
    };
    auto weave = [&]() {
#pragma unroll
      for (int k = 0; k < 10; ++k) {
        __builtin_amdgcn_sched_group_barrier(0x008, 1, 0);
        __builtin_amdgcn_sched_group_barrier(0x100, 1, 0);
      }
      __builtin_amdgcn_sched_group_barrier(0x008, 6, 0);
      __builtin_amdgcn_sched_barrier(0);
    };
    static_for<0, 8>([&](auto I) {
      constexpr int i = decltype(I)::value;
      wait_lgkm0();
      __builtin_amdgcn_sched_barrier(0);
      bar();
      if constexpr (i == 0) { TSL(6) }
      rd(std::integral_constant<int, 2 * i>{});
      if constexpr (i > 0) mm(std::integral_constant<int, 2 * i - 1>{});
      weave();
      rd(std::integral_constant<int, 2 * i + 1>{});
      mm(std::integral_constant<int, 2 * i>{});
      weave();
    });
    wait_lgkm0();
    __builtin_amdgcn_sched_barrier(0);
    bar();
    TSL(7)
#pragma unroll
    for (int j = 0; j < 2; ++j) {
      s2[j] = mfma_bf16(xf[15][j], xf[15][j], s2[j]);
      s1[j] = mfma_bf16(ones, xf[15][j], s1[j]);
    }
    lane = fresh_lane();
    col = lane & 15; g = lane >> 4; q = (lane & 15) >> 2; p = lane & 3;
    const u32 csa = lds0 + L_CS + g * 16;
    f32x4 cs6[6];
    __builtin_amdgcn_sched_barrier(0);
    u32 so[6];
    float lm[2], ls[2], lc[2], dv[2];
    u32 pk0, pk1;
    static_for<0, 12>([&](auto N) {
      constexpr int n = decltype(N)::value;
      acc[n >> 1][n & 1] = mfma_bf16(wf[1][n >> 1], xf[15][n & 1], acc[n >> 1][n & 1]);
      if constexpr (n >= 6) cs6[n - 6] = __builtin_bit_cast(f32x4, lds_rd128(csa + (n - 6) * 64));
      if constexpr (n < 3) {
        const int lt = rb * 8 + 2 * mq + (g & 1);
        const u32 hb = hoff + (((lt >> 2) * NKF * 4 + (lt & 3)) << 10) + col * 16;
#pragma unroll
        for (int i = 2 * n; i < 2 * n + 2; ++i) {
          const int o0 = i < 3 ? c * 6 + 2 * i : i == 3 ? 96 + c * 2 : 127 + c * 3 + 2 * (i - 4), o1 = o0 + 1;
          const u32 f0 = (i < 4 || c < 15) ? (o0 >> 2) * 4096 + (o0 & 3) * 256 : 0x70000000u;
          const u32 f1 = (i < 3 || (i < 5 && c < 15)) ? (o1 >> 2) * 4096 + (o1 & 3) * 256 : 0x70000000u;
          so[i] = hb + f0 + (g >> 1) * (f1 - f0);
        }
      }
      if constexpr (n == 3 || n == 4) {
        constexpr int j = n - 3;
        const float e0 = s2[j][0], e1 = s2[j][1], e2 = s2[j][2], e3 = s2[j][3];
        const float d01 = (lane & 1) ? e1 : e0, d23 = (lane & 1) ? e3 : e2, dd = (lane & 2) ? d23 : d01;
        dv[j] = (g == q) ? dd : 0.f;
      }
      if constexpr (n == 5 || n == 6) dv[n - 5] = sum_groups(dv[n - 5]);
      if constexpr (n == 7) {
        const float m0 = s_mul(s1[0][0], 1.0f / SD), m1 = s_mul(s1[1][0], 1.0f / SD);
        pk0 = pk_bf16(m0, m1);
        dv[0] = s_nfma(m0, m0, s_mul(dv[0], 1.0f / SD));
        dv[1] = s_nfma(m1, m1, s_mul(dv[1], 1.0f / SD));
      }
      if constexpr (n == 8) { pk1 = pk_bf16(fmaxf(dv[0], 0.f), fmaxf(dv[1], 0.f)); lm[0] = bflo(pk0); lm[1] = bfhi(pk0); }
      if constexpr (n == 9) pk1 = pk_bf16(s_fma(bflo(pk1), 1.0f, 1e-5f), s_fma(bfhi(pk1), 1.0f, 1e-5f));
      if constexpr (n == 10) pk1 = pk_bf16(__builtin_amdgcn_sqrtf(bflo(pk1)), __builtin_amdgcn_sqrtf(bfhi(pk1)));
      if constexpr (n == 11) {
        ls[0] = bflo(pk1); ls[1] = bfhi(pk1);
        lc[0] = __builtin_amdgcn_rcpf(ls[0]); lc[1] = __builtin_amdgcn_rcpf(ls[1]);
      }
      __builtin_amdgcn_sched_barrier(0);
    });
    lc[0] *= -1.44269504088896340736f; lc[1] *= -1.44269504088896340736f;
    u32x4 wv[2][16];
    static_for<0, 4>([&](auto K) { wv[0][decltype(K)::value] = lw128(std::integral_constant<int, f_tile(6) + decltype(K)::value * 1024>{}); });
    u32 hv[12][2];
    hv[11][0] = 0; hv[11][1] = 0;
    auto hstore = [&](int t) {
      __builtin_amdgcn_raw_buffer_store_b128(tr4(hv[t][0], hv[t][1], hv[t + 1][0], hv[t + 1][1]), rsw, so[t >> 1], 0, 16);
    };
    auto epi = [&](int t) {
      const f32x4 cs = __builtin_bit_cast(f32x4, lds_rd128(csa + t * 64));
#pragma unroll
      for (int j = 0; j < 2; ++j) hv[t][j] = glu_tile(acc[t][j], cs, lm[j], ls[j], lc[j]);
    };
#pragma unroll
    for (int t = 0; t < 6; ++t) {
#pragma unroll
      for (int j = 0; j < 2; ++j) hv[t][j] = glu_tile(acc[t][j], cs6[t], lm[j], ls[j], lc[j]);
      if (t & 1) hstore(t - 1);
    }
    __builtin_amdgcn_sched_barrier(0);
    TSL(13)
    constexpr int PRE = F_PRE, WIN = F_WIN;
    const u32 ho = hoff + (rb64 * NKF * 4 + mq) * 1024 + lane * 16;
    u32x4 hf[NKF];
    static_for<6, 11>([&](auto T) {
      constexpr int t = decltype(T)::value, e = t - 1, wb = t & 1;
      f32x4 cv;
      float ga[4], ua[4];
      static_for<0, 32>([&](auto S) {
        constexpr int sg = decltype(S)::value, k = sg >> 1, j = sg & 1;
        if constexpr ((sg & 7) == 0) { wait_lgkm0(); __builtin_amdgcn_sched_barrier(0); }
        acc[t][j] = mfma_bf16(wv[wb][k], xf[k][j], acc[t][j]);
        if constexpr (t > 6 && sg == 0) cv = __builtin_bit_cast(f32x4, lds_rd128(csa + e * 64));
        if constexpr ((sg & 7) < 4) {
          constexpr int kk = 4 + (sg >> 3) * 4 + (sg & 3);
          if constexpr (kk < 16) wv[wb][kk] = lw128(std::integral_constant<int, f_tile(t) + kk * 1024>{});
          else if constexpr (t < 10) wv[wb ^ 1][kk - 16] = lw128(std::integral_constant<int, f_tile(t + 1) + (kk - 16) * 1024>{});
        }
        if constexpr (t > 6) {
          constexpr int st = sg >> 2, a = sg & 3, jj = a >> 1, ee = a & 1;
          if constexpr (st == 1) { ga[a] = s_nfma(lm[jj], cv[ee], acc[e][jj][ee]); ua[a] = s_nfma(lm[jj], cv[2 + ee], acc[e][jj][2 + ee]); }
          if constexpr (st == 2) ga[a] = s_mul(ga[a], lc[jj]);
          if constexpr (st == 3) ga[a] = __builtin_amdgcn_exp2f(ga[a]);
          if constexpr (st == 4) ga[a] = s_fma(ga[a], ls[jj], ls[jj]);
          if constexpr (st == 5) ga[a] = __builtin_amdgcn_rcpf(ga[a]);
          if constexpr (st == 6) ua[a] = s_mul(ua[a], ga[a]);
        }
        if constexpr (t > 6 && sg == 28) { hv[e][0] = pk_bf16(ua[0], ua[1]); hv[e][1] = pk_bf16(ua[2], ua[3]); }
        if constexpr (sg == 30 && (t == 8 || t == 10)) hstore(t - 2);
        if constexpr (t == 10 && sg >= 4 && sg < 4 + 2 * PRE && (sg & 1) == 0)
          gload16_sc1(hf[(sg - 4) >> 1], ho, ws + ((sg - 4) >> 1) * 4096);
        __builtin_amdgcn_sched_barrier(0);
      });
      if constexpr (t == 10) { epi(10); hstore(10); __builtin_amdgcn_sched_barrier(0); }
      else wait_lgkm<4>();
      if constexpr (t == 6) wait_vm<0>();
      if constexpr (t == 10) wait_vm<PRE + 2>();
      if constexpr (t == 10) wait_lgkm0();
      bar();
      __builtin_amdgcn_sched_barrier(0);
    });
    TSL(8)
    f32x4 ac2[4] = {{0, 0, 0, 0}, {0, 0, 0, 0}, {0, 0, 0, 0}, {0, 0, 0, 0}};
    u32x4 wf2[2][4];
    auto rd2 = [&](auto U) {
      constexpr int u = decltype(U)::value;
      static_for<0, 4>([&](auto I) {
        constexpr int i = decltype(I)::value;
        wf2[u & 1][i] = lw128(std::integral_constant<int, f_wd(u >> 1) + (u & 1) * 4096 + i * 1024>{});
      });
    };
    rd2(std::integral_constant<int, 0>{});
    __builtin_amdgcn_sched_barrier(0);
    constexpr auto issued = [](int u) {
      int n = PRE;
      for (int v = 1; v <= u; ++v) {
        int lim = v + WIN;
        if (v <= F_U2 && lim > 24) lim = 24;
        if (v <= F_U3 && lim > 31) lim = 31;
        if (lim > NKF) lim = NKF;
        const int rate = v > F_U3 ? 6 : F_RATE;
        n = n + rate < lim ? n + rate : (lim > n ? lim : n);
      }
      return n;
    };
    static_for<0, NKF>([&](auto U) {
      constexpr int u = decltype(U)::value;
      static_assert(issued(u) > u && F_U1 >= 6 && F_U2 >= 12 && F_U2 < 25 && F_U3 < 29, "h window / W_down residency");
      asm volatile("s_waitcnt vmcnt(%1)" : "+v"(hf[u]) : "n"(issued(u) - 1 - u + (u < PRE ? 2 : 0)) : "memory");
      if constexpr (u == F_U1) { wait_vm<issued(u) - PRE>(); bar(); TSL(9) }
      if constexpr (u == F_U2) bar();
      if constexpr (u == F_U3) { bar(); TSL(10) }
      __builtin_amdgcn_sched_barrier(0);
#pragma unroll
      for (int v = issued(u); v < issued(u + 1); ++v) gload16_sc1(hf[v], ho, ws + v * 4096);
      if constexpr (u + 1 < NKF) rd2(std::integral_constant<int, u + 1>{});
#pragma unroll
      for (int i = 0; i < 4; ++i) ac2[i] = mfma_bf16(wf2[u & 1][i], hf[u], ac2[i]);
#pragma unroll
      for (int k = 0; k < 4; ++k) {
        __builtin_amdgcn_sched_group_barrier(0x008, 1, 0);
        __builtin_amdgcn_sched_group_barrier(0x100, 1, 0);
      }
      __builtin_amdgcn_sched_barrier(0);
    });
    TSL(11)
    lane = fresh_lane();
    {
    const int col = lane & 15, g = lane >> 4, q = (lane & 15) >> 2, p = lane & 3;
    const int l = rb64 * 64 + mq * 16 + col;
    u32x2 ov[4];
#pragma unroll
    for (int i = 0; i < 4; ++i) {
      const u32x2 rv = lds_tr32(lds0 + F_RES + (i * 16 + 4 * g + q) * 144 + mq * 32 + p * 8);
      const u32 o0 = pk_bf16(ac2[i][0], ac2[i][1]), o1 = pk_bf16(ac2[i][2], ac2[i][3]);
      ov[i] = u32x2{pk_bf16(bflo(rv[0]) + bflo(o0), bfhi(rv[0]) + bfhi(o0)),
                    pk_bf16(bflo(rv[1]) + bflo(o1), bfhi(rv[1]) + bfhi(o1))};
    }
    if (layer == 0) {
      lds_bar();
      float sa = 0.f, sq = 0.f;
#pragma unroll
      for (int i = 0; i < 4; ++i)
#pragma unroll
        for (int e = 0; e < 2; ++e) { sa = dot2bf(ov[i][e], 0x3f803f80u, sa); sq = dot2bf(ov[i][e], ov[i][e], sq); }
      sa = sum_groups(sa); sq = sum_groups(sq);
      __builtin_amdgcn_raw_buffer_store_b128(u32x4{__float_as_uint(sa), tag, __float_as_uint(sq), tag}, rsw,
                                             g == 0 ? OFF_PART + (l * 8 + ct) * 16 : 0x7ffffff0u, 0, 16);
#pragma unroll
      for (int i = 0; i < 4; ++i) {
        const u32 give = (col & 1) ? ov[i][0] : ov[i][1];
        const u32 got = (u32)__builtin_amdgcn_update_dpp(0, (int)give, 0xB1, 0xf, 0xf, true);
        const u32x4 gv = {(col & 1) ? got : ov[i][0], tag + 1u, (col & 1) ? ov[i][1] : got, tag + 1u};
        const u32 go = OFF_XG + (ct * 32 + i * 8 + g * 2 + (col & 1)) * XG_ROW + (l & ~1) * 8;
        __builtin_amdgcn_raw_buffer_store_b128(gv, rsw, go, 0, 16);
      }
    } else {
      const u32x4 t0 = tr4(ov[0][0], ov[1][0], ov[2][0], ov[3][0]), t1 = tr4(ov[0][1], ov[1][1], ov[2][1], ov[3][1]);
      const auto rso = __builtin_amdgcn_make_buffer_rsrc(A.out, 0, SL * SD * 2, 0x00020000);
      const u32 oo = (l * SD + ct * 64 + g * 16) * 2;
      __builtin_amdgcn_raw_buffer_store_b128(u32x4{t0[0], t1[0], t0[1], t1[1]}, rso, oo, 0, OUT_AUX);
      __builtin_amdgcn_raw_buffer_store_b128(u32x4{t0[2], t1[2], t0[3], t1[3]}, rso, oo + 16, 0, OUT_AUX);
    }
    }
#pragma unroll
    for (int j = 0; j < 4; ++j) asm volatile("" : "=v"(gr[j]), "=v"(xg[j]));
    TSL(12)
    TSL_FLUSH
  }
  if (b == 0 && w == 0 && fresh_lane() == 0) __hip_atomic_store((gu32*)(ws + OFF_EPOCH), tag + 1u, __ATOMIC_RELAXED, __HIP_MEMORY_SCOPE_AGENT);
}

DEV void publish_ln(char* ws, u32 off, float sum, float sumsq, u32 tag, int b, int lane) {
  float m, sd;
  ln_consts(sum8(sum), sum8(sumsq), m, sd);
  const u32 val = (__float_as_uint(m) >> 16) | (__float_as_uint(sd) & 0xffff0000u);
  __hip_atomic_store((gu64*)(ws + off + (lane & 7) * GR_SZ) + (lane >> 4) * 512 + 2 * b + ((lane >> 3) & 1),
                     ((unsigned long long)tag << 32) | val, __ATOMIC_RELAXED, __HIP_MEMORY_SCOPE_AGENT);
}

DEV void helper_waves(const Args& A, char* smem, int tid, int lw, int b KIDP) {
  TS_DECL
  char* ws = A.ws;
  const int lane = tid & 63;
  const u32 lds0 = (u32)(uintptr_t)smem;
  const auto rsw = __builtin_amdgcn_make_buffer_rsrc(ws, 0, (int)WS_USED, 0x00020000);
  const int kr = lane >> 3, no = lane & 7;
  u32 tag;
  {
    char* img = smem + KG_PACK + (lw - 1) * PK_IMG;
    if (lw <= 1) {
      const u16* src = lw == 0 ? A.x + (size_t)(8 * b + kr) * SD + no * 8
                               : A.x + (size_t)(64 * (b >> 3) + 2 * no) * SD + (b & 7) * 64 + kr * 8;
      const int st1 = lw == 0 ? 64 : SD, st2 = lw == 0 ? 128 : 16 * SD;
      u32x4 v[8];
#pragma unroll
      for (int i = 0; i < 8; ++i) v[i] = *(const u32x4*)(src + (i & 1) * st1 + (i >> 1) * st2);
      tag = __builtin_amdgcn_raw_buffer_load_b32(rsw, OFF_EPOCH, 0, 16);
      if (lw == 0 && b < 10) __builtin_amdgcn_raw_buffer_store_b32(0u, rsw, OFF_CNT + (b * 64 + lane) * 128, 0, 16);
      __builtin_amdgcn_sched_barrier(0);
      bar();
      __builtin_amdgcn_sched_barrier(0);
      if (lw == 1) {
        tag += 1u;
        if (lane == 0) { lds_st(lds0 + L_MISC + 4, tag); lds_st(lds0 + L_MISC, 1u); }
        const u32 go = OFF_XG + ((b & 7) * 32 + kr * 4) * XG_ROW + (b >> 3) * 512 + no * 16;
#pragma unroll
        for (int i = 0; i < 4; ++i)
#pragma unroll
          for (int e = 0; e < 4; ++e)
            __builtin_amdgcn_raw_buffer_store_b128(u32x4{v[2 * i][e], tag, v[2 * i + 1][e], tag}, rsw,
                                                   go + e * XG_ROW + i * 128, 0, 16);
        wait_vm<0>();
        if (lane == 0) lds_st(lds0 + L_MISC + 44, 1u);
      } else {
        float a[2] = {0.f, 0.f}, q[2] = {0.f, 0.f};
#pragma unroll
        for (int i = 0; i < 8; ++i)
#pragma unroll
          for (int j = 0; j < 4; ++j) {
            a[j & 1] = dot2bf(v[i][j], 0x3f803f80u, a[j & 1]);
            q[j & 1] = dot2bf(v[i][j], v[i][j], q[j & 1]);
          }
        lds_wait(lds0 + L_MISC, 1u);
        tag = lds_ld(lds0 + L_MISC + 4);
        wait_vm<0>();
        publish_ln(ws, OFF_GRAN, a[0] + a[1], q[0] + q[1], tag, b, lane);
      }
    } else {
      if (lw == 2) {
        const u32 t0 = __builtin_amdgcn_raw_buffer_load_b32(rsw, ((b >> 3) * TLB_N + (lane % TLB_N)) * TLB_STRIDE + TLB_STRIDE - 256, 0, 16);
        const u16 t1 = A.out[((b >> 3) * 32768) + 16384];
        __builtin_amdgcn_sched_barrier(0);
        bar();
        asm volatile("" ::"v"(t0), "v"((u32)t1));
      } else
      bar();
      __builtin_amdgcn_s_sleep(PK_DELAY);
    }
    TS(12)
    const int id = (3 - lw) * 256 + b;
    const int col = lane & 15, g = lane >> 4, q = (lane & 15) >> 2, p = lane & 3;
    const u32 ra = (u32)(uintptr_t)img + (8 * g + q) * 256 + 8 * p;
    const int rkey = q | ((g & 1) << 2);
    u32x4 fr[9];
    u32 so9[9];
#pragma unroll
    for (int i = 0; i < 9; ++i) so9[i] = 0x7ffffff0u;
    if (id < 352) {
      const int ps = id & 15, ng = id >> 4;
      const size_t so = (size_t)(32 * ps + kr) * SF + 8 * (8 * ng + no < 172 ? 8 * ng + no : 0);
      u32x4 gv[4], uv[4];
#pragma unroll
      for (int i = 0; i < 4; ++i) {
        gv[i] = *(const u32x4*)(A.wg + so + (size_t)i * 8 * SF);
        uv[i] = *(const u32x4*)(A.wu + so + (size_t)i * 8 * SF);
      }
#pragma unroll
      for (int i = 0; i < 4; ++i) {
        char* dst = img + (8 * i + kr) * 256 + ((no ^ ((kr & 3) | ((i & 1) << 2))) * 32);
        *(u32x4*)dst = u32x4{gv[i][0], uv[i][0], gv[i][1], uv[i][1]};
        *(u32x4*)(dst + 16) = u32x4{gv[i][2], uv[i][2], gv[i][3], uv[i][3]};
      }
      wait_lgkm0();
      asm volatile("" ::: "memory");
      f32x4 cs = {0, 0, 0, 0};
#pragma unroll
      for (int t8 = 0; t8 < 8; ++t8) {
        const u32x2 lo = lds_tr32(ra + ((t8 ^ rkey) * 32)), hi = lds_tr32(ra + ((t8 ^ rkey) * 32) + 1024);
        fr[t8] = u32x4{lo[0], lo[1], hi[0], hi[1]};
        const int Tt = 8 * ng + t8;
        so9[t8] = lane * 16 + OFF_WGU + wgu_off(Tt / 11, ps, Tt % 11);
        const u32 sel = col == t8 ? 0x3f803f80u : 0u;
        cs = mfma_bf16(fr[t8], u32x4{sel, sel, sel, sel}, cs);
      }
      fr[8] = __builtin_bit_cast(u32x4, cs);
      if (col < 8) so9[8] = OFF_CSP + ((ps * 176 + 8 * ng + col) * 16 + g * 4) * 4;
    } else if (id < 352 + 8 * NKF) {
      const int ct = (id - 352) & 7, kp = (id - 352) >> 3;
      u32x4 gv[4];
#pragma unroll
      for (int i = 0; i < 4; ++i)
        gv[i] = *(const u32x4*)(A.wd + (size_t)(8 * tile_of_oct(4 * kp + i) + kr) * SD + ct * 64 + no * 8);
#pragma unroll
      for (int i = 0; i < 4; ++i)
        *(u32x4*)(img + (8 * i + kr) * 256 + (((no >> 1) ^ ((kr & 3) | ((i & 1) << 2))) * 32) + (no & 1) * 16) = gv[i];
      wait_lgkm0();
      asm volatile("" ::: "memory");
#pragma unroll
      for (int nt = 0; nt < 4; ++nt) {
        const u32x2 lo = lds_tr32(ra + ((nt ^ rkey) * 32)), hi = lds_tr32(ra + ((nt ^ rkey) * 32) + 1024);
        fr[nt] = u32x4{lo[0], lo[1], hi[0], hi[1]};
        so9[nt] = lane * 16 + OFF_WD + ((ct * NKF + kp) * 4 + nt) * 1024;
      }
    }
    if (id < 352) {
#pragma unroll
      for (int i = 0; i < 8; ++i)
        if ((8 * (id >> 4) + i) % 11 < 6) __builtin_amdgcn_raw_buffer_store_b128(fr[i], rsw, so9[i], 0, 16);
      __builtin_amdgcn_raw_buffer_store_b128(fr[8], rsw, so9[8], 0, 16);
    }
    TS(13)
    lds_post(lds0 + L_MISC + 28, lane);
    lds_wait_slow(lds0 + L_MISC + 36, 4u);
    tag = lds_ld(lds0 + L_MISC + 4);
    wait_vm<0>();
    if (id < 352) {
      if (lane == 0)
        __hip_atomic_fetch_add((gu32*)(ws + OFF_CNT) + (96 + (id >> 4)) * 32, 1u, __ATOMIC_RELAXED, __HIP_MEMORY_SCOPE_AGENT);
#pragma unroll
      for (int i = 0; i < 8; ++i)
        if ((8 * (id >> 4) + i) % 11 >= 6) __builtin_amdgcn_raw_buffer_store_b128(fr[i], rsw, so9[i], 0, 16);
    } else if (id < 352 + 8 * NKF) {
#pragma unroll
      for (int i = 0; i < 4; ++i) __builtin_amdgcn_raw_buffer_store_b128(fr[i], rsw, so9[i], 0, 16);
    }
    wait_vm<0>();
    if (lds_arrive(lds0 + L_MISC + 40, lane) == 3u && lane == 0)
      __hip_atomic_fetch_add((gu32*)(ws + OFF_CNT) + (120 + (b & 7)) * 32, 1u, __ATOMIC_RELAXED, __HIP_MEMORY_SCOPE_AGENT);
  }
  TS(1)
  const int xcd = b & 7, idx = b >> 3;
  const int rb = (xcd & 1) * 8 + (idx & 7), c = (xcd >> 1) * 4 + (idx >> 3);
  const int rb64 = xcd * 4 + (idx & 3), ct = idx >> 2;
  const u32 wbase = OFF_WGU + c * (176 * 1024) + lane * 16;
  const u32 wdo = OFF_WD + ct * NKF * 4096 + (lw - 1) * 8192 + lane * 16;
  if (lw == 0) {
    u32x4 kimg[17];
    const u32 ko = lds0 + lane * 16;
#pragma unroll 1
    for (int layer = 0; layer < 2; ++layer) {
      asm volatile("" : "+s"(ws));
      const u32 xin = OFF_X + layer * X_SZ;
      if (layer == 1) {
#pragma unroll
        for (int i = 0; i < 17; ++i)
          if (i < 16 || lane < 32) *(LAS u32x4*)(ko + i * 1024) = kimg[i];
        lds_post(lds0 + L_MISC + 52, lane);
        u32x4 pv;
        __builtin_amdgcn_s_sleep(GR_DELAY);
#pragma unroll 1
        for (int sp = 0; sp < (1 << 18); ++sp) {
          pv = __builtin_amdgcn_raw_buffer_load_b128(rsw, OFF_PART + b * 1024 + lane * 16, 0, XG_AUX);
          if (__builtin_amdgcn_ballot_w64(((pv[1] ^ tag) | (pv[3] ^ tag)) != 0u) == 0ull) break;
          __builtin_amdgcn_s_sleep(3);
        }
        publish_ln(ws, OFF_GRAN, __uint_as_float(pv[0]), __uint_as_float(pv[2]), tag + 1u, b, lane);
        if (lane == 0) lds_st(lds0 + L_MISC + 44, 2u);
        TSL(8)
      } else {
        lds_wait_slow(lds0 + L_MISC + 52, 4u);
#pragma unroll
        for (int i = 0; i < 17; ++i)
          if (i < 16 || lane < 32) kimg[i] = lds_rd128(ko + i * 1024);
        gu32* fp = (gu32*)(ws + OFF_CNT) + (96 + min((c * 11 >> 3) + lane, (c * 11 + 10) >> 3)) * 32;
        lds_wait_slow(lds0 + L_MISC + 28, 4u);
        __builtin_amdgcn_s_sleep(FL_DELAY);
#pragma unroll 1
        for (int sp = 0; sp < (1 << 20); ++sp) {
          u32 v = 16u;
          if (lane < 3) v = __hip_atomic_load(fp, __ATOMIC_RELAXED, __HIP_MEMORY_SCOPE_AGENT);
          if (__builtin_amdgcn_ballot_w64(v < 16u) == 0ull) break;
          __builtin_amdgcn_s_sleep(2);
        }
        asm volatile("" ::: "memory");
        if (lane == 0) lds_st(lds0 + L_MISC + 28, 5u);
        TS(2)
        f32x4 cv[6];
#pragma unroll
        for (int r = 0; r < 6; ++r) {
          const u32 co = OFF_CSP + (no * 176 + c * 11) * 64 + min(r * 8 + kr, 43) * 16;
          cv[r] = __builtin_bit_cast(f32x4, __builtin_amdgcn_raw_buffer_load_b128(rsw, co, 0, 16)) +
                  __builtin_bit_cast(f32x4, __builtin_amdgcn_raw_buffer_load_b128(rsw, co + 8 * 176 * 64, 0, 16));
        }
#pragma unroll
        for (int r = 0; r < 6; ++r) {
          const f32x4 v = {sum8(cv[r][0]), sum8(cv[r][1]), sum8(cv[r][2]), sum8(cv[r][3])};
          if (no == 0) *(f32x4*)(smem + L_CS + min(r * 8 + kr, 43) * 16) = v;
        }
        wait_lgkm0();
#pragma unroll 1
        for (int sp = 0; sp < (1 << 20); ++sp) {
          u32 v = 32u;
          if (lane < 8) v = __hip_atomic_load((gu32*)(ws + OFF_CNT) + (120 + lane) * 32, __ATOMIC_RELAXED, __HIP_MEMORY_SCOPE_AGENT);
          if (__builtin_amdgcn_ballot_w64(v < 32u) == 0ull) break;
          __builtin_amdgcn_s_sleep(8);
        }
        asm volatile("" ::: "memory");
      }
      TSL(3)
      if (lds_ld(lds0 + L_MISC + 24) < 4u * (layer + 1)) {
        lds_wait_slow(lds0 + L_MISC + 24, 4u * (layer + 1));
        __builtin_amdgcn_s_sleep(X_DELAY);
      }
      grid_wait(ws, layer, b, lane);
      TSL(4)
      u32 vo[2];
#pragma unroll
      for (int o = 0; o < 2; ++o) {
        const int seg = ((lane & 15) >> 1) ^ ((lane >> 4) | (o << 2));
        vo[o] = xin + rb * (SD * 256) + (lane >> 4) * 256 + seg * 32 + (lane & 1) * 16;
      }
      auto stage = [&](int s) {
#pragma unroll
        for (int i = 0; i < 4; ++i)
          asm volatile(
              "s_mov_b32 m0, %3\n\ts_nop 0\n\tglobal_load_lds_dwordx4 %0, %2 sc1\n\tglobal_load_lds_dwordx4 %0, %2 offset:1024 sc1\n\t"
              "global_load_lds_dwordx4 %1, %2 offset:2048 sc1\n\tglobal_load_lds_dwordx4 %1, %2 offset:3072 sc1"
              ::"v"(vo[0]), "v"(vo[1]), "s"(ws + s * 16384 + i * 4096), "s"(lds0 + (s % 3) * 16384 + i * 4096) : "memory");
      };
      stage(0); stage(1);
      static_for<0, 8>([&](auto I) {
        constexpr int i = decltype(I)::value;
        wait_vm<(i < 7 ? 16 : 0)>();
        bar();
        if constexpr (i < 6) stage(i + 2);
      });
      gu32* cs_ = (gu32*)(ws + OFF_CNT) + (layer * 48 + rb * 3) * 32;
      gu32* cw_ = (gu32*)(ws + OFF_CNT) + (layer * 48 + (rb64 >> 1) * 3) * 32;
      bar();
      bar();
      if (lane == 0) __hip_atomic_fetch_add(cs_, 1u, __ATOMIC_RELAXED, __HIP_MEMORY_SCOPE_AGENT);
      bar();
      bar();
      if (lane == 0) poll_ge(cw_, 16u);
      TSL(5)
      bar();
      bar();
      if (lane == 0) __hip_atomic_fetch_add(cs_ + 32, 1u, __ATOMIC_RELAXED, __HIP_MEMORY_SCOPE_AGENT);
      bar();
      if (lane == 0) {
        __hip_atomic_fetch_add(cs_ + 64, 1u, __ATOMIC_RELAXED, __HIP_MEMORY_SCOPE_AGENT);
        poll_ge(cw_ + 32, 16u);
      }
      bar();
      __builtin_amdgcn_s_sleep(AB_DELAY);
      if (lane == 0) poll_ge(cw_ + 64, 16u);
      TSL(6)
      bar();
      TSL(7)
      TSL_FLUSH
      if (layer == 0) bar();
    }
  } else {
    u32x4 px[8], py[8], pq[4];
    auto touch = [&](u32 vo, const void* base) {
      asm volatile("s_mov_b32 m0, %2\n\ts_nop 0\n\tglobal_load_lds_dword %0, %1 sc1"
                   ::"v"(vo), "s"(base), "s"(lds0 + L_DUMP) : "memory");
    };
    auto touch_xh = [&](int layer) {
      touch(OFF_X + layer * X_SZ + ((lane >> 2) * SD + 2 * chan_pair(b)) * 256 + (lane & 3) * 128, ws);
#pragma unroll
      for (int k = 0; k < 3; ++k) {
        const int t = min(4 * k + (lane >> 4), c < 15 ? 10 : 6), lt = rb * 8 + ((lane >> 1) & 7);
        const int oct = t < 6 ? c * 6 + t : t < 8 ? 96 + c * 2 + (t - 6) : 127 + c * 3 + (t - 8);
        touch(OFF_H + layer * H_SZ + (((lt >> 2) * NKF * 4 + (lt & 3)) << 10) + (oct >> 2) * 4096 + (oct & 3) * 256 + (lane & 1) * 128, ws);
      }
    };
    if ((PRE_TOUCH & 1) && lw == 1) touch_xh(0);
#pragma unroll 1
    for (int layer = 0; layer < 2; ++layer) {
      asm volatile("" : "+s"(ws));
      const u32 xin = OFF_X + layer * X_SZ;
      lds_wait_slow(layer == 0 ? lds0 + L_MISC + 28 : lds0 + L_MISC + 36, layer == 0 ? 5u : 8u);
      TS(2)
      const u32 wo = wbase + (lw - 1) * 4096;
#pragma unroll
      for (int i = 0; i < 8; ++i) {
        if (lds_ld(lds0 + L_MISC + 48) > 4u * layer) lds_wait_slow(lds0 + L_MISC + 24, 4u * (layer + 1));
        dma4(wo, ws + i * 12288, lds0 + F_W1 + i * 12288 + (lw - 1) * 4096);
      }
      TSL(3)
      static_for<0, 8>([&](auto I) {
        constexpr int i = decltype(I)::value;
        wait_vm<(i == 0 ? 28 : 24)>();
        bar();
        if constexpr (i >= 1)
          dma4(wo, ws + (96 + (i - 1) * 12) * 1024,
               (i == 7 && lw == 3) ? lds0 + F_BASE : lds0 + F_W1 + (i - 1) * 12288 + (lw - 1) * 4096);
      });
      auto wd3 = [&](auto U, int n) {
        constexpr int u0 = decltype(U)::value;
        if (lw <= n) {
          const u32 home = lds0 + (lw == 1 ? f_wd(u0) : lw == 2 ? f_wd(u0 + 1) : f_wd(u0 + 2));
          dma4(wdo, ws + u0 * 8192, home);
          dma4(wdo, ws + u0 * 8192 + 4096, home + 4096);
        }
      };
      using std::integral_constant;
      wait_vm<16>();
      bar();
      wd3(integral_constant<int, 0>{}, 3); wd3(integral_constant<int, 3>{}, 3); wd3(integral_constant<int, 6>{}, 3);
      if (layer == 0) {
#pragma unroll
        for (int i = 0; i < 8; ++i) gload16_sc1(px[i], wdo, ws + 15 * 8192 + i * 1024);
      }
      wait_vm<24>();
      bar();
      wd3(integral_constant<int, 9>{}, 2);
      if (layer == 0) {
#pragma unroll
        for (int i = 0; i < 4; ++i) gload16_sc1(py[i], wdo, ws + 18 * 8192 + i * 1024);
      }
      if ((PRE_TOUCH & 2) && layer == 0 && lw == 3) {
        touch(OFF_PART + (rb64 * 64 + lane) * 128, ws);
        touch(((rb64 * 64 + lane) * SD + ct * 64) * 2, A.out);
      }
      bar();
      wd3(integral_constant<int, 11>{}, 2);
      if (layer == 0) {
#pragma unroll
        for (int i = 4; i < 8; ++i) gload16_sc1(py[i], wdo, ws + 18 * 8192 + i * 1024);
      }
      bar();
      wd3(integral_constant<int, 13>{}, 2);
      if (layer == 0 && lw == 3) {
#pragma unroll
        for (int i = 0; i < 4; ++i) gload16_sc1(pq[i], wdo, ws + 19 * 8192 + i * 1024);
      }
      bar();
      u32x4 rr[4];
      {
        const u32 ro = xin + ((rb64 >> 1) * SD + ct * 64 + (lw & 1) * 32 + kr) * 256 + (rb64 & 1) * 128 + no * 16;
#pragma unroll
        for (int i = 0; i < 4; ++i) rr[i] = __builtin_amdgcn_raw_buffer_load_b128(rsw, lw == 3 ? 0x7ffffff0u : ro + i * 2048, 0, 16);
      }
      wait_vm<4>();
#pragma unroll
      for (int i = 0; i < 8; ++i) asm volatile("" : "+v"(px[i]), "+v"(py[i]), "+v"(pq[i & 3]));
      bar();
      const u32 hx = lds0 + f_wd(15) + (lw - 1) * 8192 + lane * 16;
#pragma unroll
      for (int i = 0; i < 8; ++i) *(LAS u32x4*)(hx + i * 1024) = px[i];
      bar();
      const u32 hy = lds0 + (lw == 1 ? f_wd(18) : lw == 2 ? f_wd(19) : f_wd(20)) + lane * 16;
#pragma unroll
      for (int i = 0; i < 8; ++i) *(LAS u32x4*)(hy + i * 1024) = py[i];
      bar();
      if (lw == 3) {
#pragma unroll
        for (int i = 0; i < 4; ++i) *(LAS u32x4*)(lds0 + f_wd(21) + lane * 16 + i * 1024) = pq[i];
      } else {
#pragma unroll
        for (int i = 0; i < 4; ++i) *(LAS u32x4*)(lds0 + F_RES + ((lw & 1) * 32 + i * 8 + kr) * 144 + no * 16) = rr[i];
      }
      lds_bar();
      if ((PRE_TOUCH & 4) && layer == 0 && lw == 3) touch_xh(1);
      TSL(7)
      TSL_FLUSH
      if (layer == 0) bar();
    }
  }
}

__global__ __launch_bounds__(512, 2) void k_all(Args A KIDP) {
  extern __shared__ __attribute__((aligned(16))) char smem[];
  const int tid = threadIdx.x, w = __builtin_amdgcn_readfirstlane(tid >> 6), b = blockIdx.x;
  if (w == 7) {
    const char* pc;
    asm volatile("s_getpc_b64 %0" : "=s"(pc));
    const u32 line = (b >> 3) * CODE_N + (tid & 63) % CODE_N;
    asm volatile("s_mov_b32 m0, %2\n\ts_nop 0\n\tglobal_load_lds_dword %0, %1"
                 ::"v"(line * 128u), "s"(pc), "s"((u32)(uintptr_t)smem + L_DUMP) : "memory");
  }
  if (w < 4) compute_waves(A, smem, tid, w, b KIDA(kid_));
  else helper_waves(A, smem, tid, w - 4, b KIDA(kid_));
}

extern "C" void kernel_launch(void* const* d_in, const int* in_sizes, int n_in, void* d_out, int out_size, void* d_ws,
                              size_t ws_size, hipStream_t stream) {
  Args A;
  A.x = (const u16*)d_in[0];
  A.logA = (const u16*)d_in[1];
  A.Aim = (const u16*)d_in[2];
  A.Cre = (const u16*)d_in[3];
  A.Cim = (const u16*)d_in[4];
  A.logdt = (const u16*)d_in[5];
  A.dskip = (const u16*)d_in[6];
  A.wg = (const u16*)d_in[8];
  A.wu = (const u16*)d_in[9];
  A.wd = (const u16*)d_in[10];
  A.out = (u16*)d_out;
  A.ws = (char*)d_ws;
  (void)hipFuncSetAttribute((const void*)k_all, hipFuncAttributeMaxDynamicSharedMemorySize, LDS_BYTES);
  k_all<<<256, 512, LDS_BYTES, stream>>>(A KIDA(0));
}
```

```cpp
#include <hip/hip_runtime.h>
#include <hip/hip_bf16.h>
#include <stdint.h>
#include <type_traits>

typedef unsigned short u16;
typedef unsigned int u32;
typedef __attribute__((ext_vector_type(8))) __bf16 bf16x8;
typedef __attribute__((ext_vector_type(2))) __bf16 bf16x2;
typedef __attribute__((ext_vector_type(8))) _Float16 f16x8;
typedef __attribute__((ext_vector_type(2))) _Float16 f16x2;
typedef __attribute__((ext_vector_type(4))) float f32x4;
typedef __attribute__((ext_vector_type(2))) float f32x2;
typedef __attribute__((ext_vector_type(4))) u32 u32x4;
typedef __attribute__((ext_vector_type(2))) u32 u32x2;
typedef __attribute__((ext_vector_type(4))) short s16x4;
typedef __attribute__((ext_vector_type(2))) double f64x2;
#define LAS __attribute__((address_space(3)))
#define DEV __device__ __forceinline__

#define TSL(i)
#define TSL_FLUSH
#define KIDP
#define KIDA(n)
#define TSP
#define TSA
#define TS_DECL
#define TS(i)

constexpr int SL = 2048, SD = 512, SN = 128, SF = 1376;
constexpr int NKF = 43;
constexpr int KIMG_CH = 8448;
constexpr int KIMG_C1 = 4224;
constexpr int XG_ROW = SL * 8 + 256;

constexpr u32 OFF_WGU = 0;
constexpr u32 OFF_WD = OFF_WGU + 16 * 176 * 1024;
constexpr u32 OFF_CSP = OFF_WD + 8 * NKF * 4096;
constexpr u32 OFF_X = OFF_CSP + 16 * 176 * 64;
constexpr u32 X_SZ = SL * SD * 2;
constexpr u32 OFF_H = OFF_X + 2 * X_SZ;
#define H_SEP 1
constexpr u32 H_SZ = H_SEP * 32 * NKF * 4096;
constexpr u32 OFF_GRAN = OFF_H + 2 * 32 * NKF * 4096;
constexpr u32 GR_SZ = SL * 8 + 256;
constexpr u32 OFF_XG = OFF_GRAN + 8 * GR_SZ;
constexpr u32 OFF_PART = OFF_XG + 256 * XG_ROW;
constexpr u32 OFF_CNT = OFF_PART + SL * 128;
constexpr u32 OFF_EPOCH = OFF_CNT + 640 * 128;
constexpr u32 WS_USED = OFF_EPOCH + 256;

DEV float bflo(u32 v) { return __uint_as_float(v << 16); }
DEV float bfhi(u32 v) { return __uint_as_float(v & 0xffff0000u); }
DEV float bf2f(u16 v) { return __uint_as_float((u32)v << 16); }
DEV u32 pk_bf16(float a, float b) {
  f32x2 v = {a, b};
  return __builtin_bit_cast(u32, __builtin_convertvector(v, bf16x2));
}
DEV u32 pk_f16(float a, float b) {
  f32x2 v = {a, b};
  return __builtin_bit_cast(u32, __builtin_convertvector(v, f16x2));
}
DEV float bfr(float a) { return bflo(pk_bf16(a, a)); }
DEV float dot2bf(u32 a, u32 b, float c) {
  return __builtin_amdgcn_fdot2_f32_bf16(__builtin_bit_cast(bf16x2, a), __builtin_bit_cast(bf16x2, b), c, false);
}
DEV f32x4 mfma_bf16(u32x4 a, u32x4 b, f32x4 c) {
  return __builtin_amdgcn_mfma_f32_16x16x32_bf16(__builtin_bit_cast(bf16x8, a), __builtin_bit_cast(bf16x8, b), c, 0, 0, 0);
}
DEV f32x4 mfma_f16(u32x4 a, u32x4 b, f32x4 c) {
  return __builtin_amdgcn_mfma_f32_16x16x32_f16(__builtin_bit_cast(f16x8, a), __builtin_bit_cast(f16x8, b), c, 0, 0, 0);
}
DEV u32x2 lds_tr(const char* p) {
  return __builtin_bit_cast(u32x2, __builtin_amdgcn_ds_read_tr16_b64_v4i16((LAS s16x4*)p));
}
DEV u32x2 lds_tr32(u32 a) {
  return __builtin_bit_cast(u32x2, __builtin_amdgcn_ds_read_tr16_b64_v4i16((LAS s16x4*)a));
}
DEV u32x4 lds_rd128(u32 a) { return *(const LAS u32x4*)a; }
template <int CTRL>
DEV float dpp_f(float v) {
  return __builtin_bit_cast(float, __builtin_amdgcn_update_dpp(0, __builtin_bit_cast(int, v), CTRL, 0xf, 0xf, true));
}
DEV float sum8(float v) {
  v += dpp_f<0xB1>(v);
  v += dpp_f<0x4E>(v);
  v += dpp_f<0x141>(v);
  return v;
}
DEV float sum16(float v) { v = sum8(v); return v + dpp_f<0x140>(v); }
DEV void ln_consts(float sum, float sumsq, float& m, float& s) {
  float mean = sum * (1.0f / SD);
  float var = fmaxf(sumsq * (1.0f / SD) - mean * mean, 0.0f);
  m = bfr(mean);
  s = bfr(__builtin_amdgcn_sqrtf(bfr(bfr(var) + 1e-5f)));
}

typedef f32x2 cpx;
DEV cpx cmul(cpx a, cpx b) {
  const f32x2 t = a * f32x2{b[0], b[0]};
  return __builtin_elementwise_fma(f32x2{a[1], a[0]}, f32x2{-b[1], b[1]}, t);
}
DEV cpx cmulc(cpx a, cpx b) {
  const f32x2 t = a * f32x2{b[0], b[0]};
  return __builtin_elementwise_fma(f32x2{a[1], a[0]}, f32x2{b[1], -b[1]}, t);
}
DEV float exp_acc(float x) {
  const float t = x * 1.44269504088896340736f;
  const float e = fmaf(x, 1.44269504088896340736f, -t) + x * 1.92596299e-8f;
  const float r = __builtin_amdgcn_exp2f(t);
  return fmaf(r, e * 0.69314718056f, r);
}

typedef __attribute__((address_space(1))) u32 gu32;
typedef __attribute__((address_space(1))) unsigned long long gu64;
template <int N>
DEV void wait_vm() { asm volatile("s_waitcnt vmcnt(%0)" ::"n"(N) : "memory"); }
DEV void wait_lgkm0() { __builtin_amdgcn_s_waitcnt(0xC07F); }
DEV void bar() {
  asm volatile("" ::: "memory");
  __builtin_amdgcn_s_barrier();
  asm volatile("" ::: "memory");
}
template <int N>
DEV void wait_lgkm() { __builtin_amdgcn_s_waitcnt(0xC07F | (N << 8)); }
DEV void lds_bar() { wait_lgkm0(); bar(); }
template <int I, int N, class Fn>
DEV void static_for(Fn&& f) {
  if constexpr (I < N) { f(std::integral_constant<int, I>{}); static_for<I + 1, N>(f); }
}
DEV int wgu_off(int c, int s, int t) { return (c * 176 + (t < 6 ? s * 6 + t : 96 + (t - 6) * 16 + s)) << 10; }
DEV int tile_of_oct(int oct) {
  return oct < 96 ? (oct / 6) * 11 + oct % 6
       : oct < 142 ? ((oct - 96) / 3) * 11 + 6 + (oct - 96) % 3 : ((oct - 142) >> 1) * 11 + 9 + ((oct - 142) & 1);
}
DEV float solo(float d) { asm volatile("" : "+v"(d)); return d; }
DEV float s_fma(float a, float b, float c) { return solo(fmaf(a, b, c)); }
DEV float s_nfma(float a, float b, float c) { return solo(fmaf(-a, b, c)); }
DEV float s_div512(float a) { return solo(a * (1.0f / SD)); }
DEV float s_addeps(float a) { return solo(a + 1e-5f); }
DEV float s_mul(float a, float b) { return solo(a * b); }

constexpr int CV_U = 2 * KIMG_CH;
constexpr int CV_UROW = (128 + 1024) * 2;
constexpr int CV_P = CV_U + 4 * CV_UROW;
constexpr int CV_PROW = 2048 * 4 + 64 * 16 + 16;
constexpr int L_MISC = CV_P + 4 * CV_PROW;
constexpr int L_CS = L_MISC + 64;
constexpr int L_DUMP = L_CS + 704;
constexpr int KG_CH = 64 * 512;
constexpr int KG_TAB = KG_CH + 53 * 512;
constexpr int KG_TROW = 672 * 2;
constexpr int KG_PACK = 65536;
constexpr int PK_IMG = 8192;
constexpr int F_W1 = 65536;
constexpr int LDS_BYTES = 163840;
constexpr int F_BASE = 57344;
static_assert(L_DUMP + 256 <= 65536 && KG_TAB + 2 * KG_TROW <= L_MISC, "lds map");
constexpr int f_tile(int t) { return F_W1 + (t - 6) * 16384; }
constexpr int f_wd(int u) {
  return u < 7 ? u * 8192 : u < 9 ? F_W1 + 81920 + (u - 7) * 8192 : u < 19 ? F_W1 + (u - 9) * 8192 : f_wd(u - 19);
}
constexpr int F_RES = f_wd(4);

DEV u32 opq(u32 v) { asm volatile("" : "+s"(v)); return v; }
DEV int fresh_lane() { return __builtin_amdgcn_mbcnt_hi(~0u, __builtin_amdgcn_mbcnt_lo(~0u, opq(0u))); }
DEV int chan_pair(int b) { return (b & 7) * 32 + (b >> 3); }
DEV u32 lds_ld(u32 a) { return *(volatile LAS u32*)opq(a); }
DEV void lds_st(u32 a, u32 v) { *(volatile LAS u32*)a = v; }
DEV u32 lds_arrive(u32 word, int lane) {
  wait_lgkm0();
  asm volatile("" ::: "memory");
  u32 old = 0;
  if (lane == 0) old = __hip_atomic_fetch_add((LAS u32*)opq(word), opq(1u), __ATOMIC_RELAXED, __HIP_MEMORY_SCOPE_WORKGROUP);
  return __builtin_amdgcn_readfirstlane(old);
}
DEV void lds_bump(u32 word) {
  asm volatile("s_mov_b64 exec, 1\n\tds_add_u32 %0, %1\n\ts_mov_b64 exec, -1" ::"v"(opq(word)), "v"(opq(1u)) : "memory");
}
DEV void lds_post(u32 word, int lane) { lds_bump(word); }
DEV void lds_wait(u32 word, u32 want) {
#pragma unroll 1
  for (int sp = 0; sp < (1 << 24); ++sp)
    if (__builtin_amdgcn_readfirstlane(lds_ld(word)) >= want) break;
  asm volatile("" ::: "memory");
}
DEV void lds_wait_slow(u32 word, u32 want) {
#pragma unroll 1
  for (int sp = 0; sp < (1 << 20); ++sp) {
    if (__builtin_amdgcn_readfirstlane(lds_ld(word)) >= want) break;
    __builtin_amdgcn_s_sleep(2);
  }
  asm volatile("" ::: "memory");
}
#define XG_AUX 16
#define GR_AUX 16
#define TLB_WARM 1
#define TLB_STRIDE 65536
#define TLB_N 12
#define OUT_AUX 16
#define CODE_WARM 1
#define CODE_N 10
#define PRE_TOUCH 0
#define GRID2 1
#define XPRIO 0
#define FL_DELAY 10
#define G_REP 32
#define G_SH 8
#define GW_SLEEP 0
#define GR_DELAY2 6
#define X_DELAY 8
#define HPRIO 3
#define PF_DELAY 16
#define PK_DELAY 0
#define GR_SLEEP 8
#define GR_DELAY 8
DEV void grid_signal(char* ws, int k, int b, int lane) {
  const u32 one = opq(1u), base = OFF_CNT + (128 + k * 256) * 128;
  u32 old = 0;
  if (lane == 0)
    old = __hip_atomic_fetch_add((gu32*)(ws + opq(base + (b % G_SH) * 128)), one, __ATOMIC_RELAXED, __HIP_MEMORY_SCOPE_AGENT);
  if (__builtin_amdgcn_readfirstlane(old) == 256u / G_SH - 1u && lane < G_REP)
    __hip_atomic_fetch_add((gu32*)(ws + (base + (64 + lane) * 128)), one, __ATOMIC_RELAXED, __HIP_MEMORY_SCOPE_AGENT);
}
DEV void grid_wait(char* ws, int k, int b, int lane) {
  gu32* p = (gu32*)(ws + OFF_CNT) + (128 + k * 256 + 64 + (b % G_REP)) * 32;
#pragma unroll 1
  for (int sp = 0; sp < (1 << 20); ++sp) {
    u32 v = G_SH;
    if (lane == 0) v = __hip_atomic_load(p, __ATOMIC_RELAXED, __HIP_MEMORY_SCOPE_AGENT);
    if (__builtin_amdgcn_readfirstlane(v) >= G_SH) break;
  }
  asm volatile("" ::: "memory");
}
DEV void poll_ge(gu32* cp, u32 want) {
#pragma unroll 1
  for (int sp = 0; sp < (1 << 20); ++sp) {
    if (__hip_atomic_load(cp, __ATOMIC_RELAXED, __HIP_MEMORY_SCOPE_AGENT) >= want) break;
  }
}
DEV void gload16_sc1(u32x4& dst, u32 voff, const char* base) {
  asm volatile("global_load_dwordx4 %0, %1, %2 sc1" : "=v"(dst) : "v"(voff), "s"(base) : "memory");
}
template <int OFF>
DEV void gload16_sc1_o(u32x4& dst, u32 voff, const char* base) {
  asm volatile("global_load_dwordx4 %0, %1, %2 offset:%3 sc1" : "=v"(dst) : "v"(voff), "s"(base), "n"(OFF) : "memory");
}
DEV void dma2(u32 voff, const char* base, u32 lds) {
  asm volatile("s_mov_b32 m0, %2\n\ts_nop 0\n\tglobal_load_lds_dwordx4 %0, %1 sc1\n\t"
               "global_load_lds_dwordx4 %0, %1 offset:1024 sc1" ::"v"(voff), "s"(base), "s"(lds) : "memory");
}
DEV void dma4(u32 voff, const char* base, u32 lds) {
  asm volatile("s_mov_b32 m0, %2\n\ts_nop 0\n\tglobal_load_lds_dwordx4 %0, %1 sc1\n\t"
               "global_load_lds_dwordx4 %0, %1 offset:1024 sc1\n\tglobal_load_lds_dwordx4 %0, %1 offset:2048 sc1\n\t"
               "global_load_lds_dwordx4 %0, %1 offset:3072 sc1" ::"v"(voff), "s"(base), "s"(lds) : "memory");
}
DEV cpx cpow2(float a2, float rh, float rl, float s) {
  const float mag = __builtin_amdgcn_exp2f(a2 * s);
  const float rev = fmaf(rl, s, __builtin_amdgcn_fractf(rh * s));
  return f32x2{__builtin_amdgcn_cosf(rev), __builtin_amdgcn_sinf(rev)} * f32x2{mag, mag};
}
DEV u32 glu_tile(f32x4 acc, f32x4 cs, float m, float s, float c1) {
  const f32x2 nm = {-m, -m};
  const f32x2 gg = __builtin_elementwise_fma(nm, f32x2{cs[0], cs[1]}, f32x2{acc[0], acc[1]}) * f32x2{c1, c1};
  const f32x2 uu = __builtin_elementwise_fma(nm, f32x2{cs[2], cs[3]}, f32x2{acc[2], acc[3]});
  const f32x2 ex = {__builtin_amdgcn_exp2f(gg[0]), __builtin_amdgcn_exp2f(gg[1])};
  const f32x2 dn = __builtin_elementwise_fma(ex, f32x2{s, s}, f32x2{s, s});
  const f32x2 hh = uu * f32x2{__builtin_amdgcn_rcpf(dn[0]), __builtin_amdgcn_rcpf(dn[1])};
  return pk_bf16(hh[0], hh[1]);
}
DEV u32x4 tr4(u32 r0, u32 r1, u32 r2, u32 r3) {
  auto a = __builtin_amdgcn_permlane32_swap(r0, r2, false, false);
  auto b = __builtin_amdgcn_permlane32_swap(r1, r3, false, false);
  auto c = __builtin_amdgcn_permlane16_swap(a[0], b[0], false, false);
  auto d = __builtin_amdgcn_permlane16_swap(a[1], b[1], false, false);
  return u32x4{c[0], c[1], d[0], d[1]};
}
DEV float sum_groups(float v) {
  auto a = __builtin_amdgcn_permlane32_swap(__float_as_uint(v), __float_as_uint(v), false, false);
  v = __uint_as_float(a[0]) + __uint_as_float(a[1]);
  auto b = __builtin_amdgcn_permlane16_swap(__float_as_uint(v), __float_as_uint(v), false, false);
  return __uint_as_float(b[0]) + __uint_as_float(b[1]);
}

#define F_WIN 18
#define F_PRE 10
#define F_RATE 2
#define F_U1 6
#define F_U2 16
#define F_U3 26
#define AB_DELAY 0

struct Args {
  const u16 *x, *logA, *Aim, *Cre, *Cim, *logdt, *dskip, *wg, *wu, *wd;
  u16* out;
  char* ws;
};

DEV u32 conv_ka(u32 lds0, int lane, int w) {
  const int h = lane >> 4, q = (lane & 15) >> 2, p = lane & 3;
  return lds0 + (w >> 1) * KIMG_CH + 2 * 16 + (q & 1) * (KIMG_C1 + 4) + 2 * (48 - 16 * h - 2 * q + 4 * p) - 16;
}
DEV void conv_mfma(u32 lds0, u32 ka, const u32x4 (&k0)[8], int lane, int w TSP) {
  const int np = lane & 15, h = lane >> 4;
  u32 ua = lds0 + CV_U + w * CV_UROW + 256 + 2 * (8 * (np + h) - 24) - 192;
  u32 pp = lds0 + CV_P + w * CV_PROW + 16 + np * 64 + h * 16 + (np >> 1) * 16;
  asm volatile("" : "+v"(ua), "+v"(pp));
  f32x4 acc[8];
#pragma unroll
  for (int j = 0; j < 8; ++j) acc[j] = f32x4{0, 0, 0, 0};
  u32x4 kf[2][8], uf[2][8];
  constexpr auto pair_i = [](int n) { int i = 0; while (n >= 8 - i) { n -= 8 - i; ++i; } return i; };
  constexpr auto pair_j = [](int n) { int i = 0; while (n >= 8 - i) { n -= 8 - i; ++i; } return i + n; };
  auto rd1 = [&](auto D, auto N) {
    constexpr int dl = decltype(D)::value, n = decltype(N)::value;
    if constexpr (n >= 2 && n < 10) uf[dl & 1][n - 2] = lds_rd128(ua + (n - 2) * 256 + (3 - dl) * 64);
    else {
      constexpr int m = n < 2 ? n : n - 8, i = m >> 1;
      const u32x2 v = lds_tr32(ka + (4 * i + dl) * 128 + ((m & 1) ? 0 : 16));
      kf[dl & 1][i][2 * (m & 1)] = v[0]; kf[dl & 1][i][2 * (m & 1) + 1] = v[1];
    }
  };
#pragma unroll
  for (int i = 0; i < 8; ++i) kf[0][i] = k0[i];
  static_for<2, 10>([&](auto N) { rd1(std::integral_constant<int, 0>{}, N); });
  __builtin_amdgcn_sched_barrier(0);
  static_for<0, 4>([&](auto D) {
    constexpr int dl = decltype(D)::value;
    if constexpr (dl > 0) wait_lgkm0();
    if constexpr (dl == 3) lds_bump(lds0 + L_MISC + 48);
    __builtin_amdgcn_sched_barrier(0);
    static_for<0, 36>([&](auto N) {
      constexpr int n = decltype(N)::value, g0 = n < 15 ? 0 : n < 26 ? 15 : n < 33 ? 26 : 33, jh = n < 15 ? 7 : n < 26 ? 5 : n < 33 ? 3 : 1;
      constexpr int i = dl < 3 ? pair_i(n) : (n - g0) >> 1, j = dl < 3 ? pair_j(n) : jh - ((n - g0) & 1);
      acc[j] = mfma_bf16(kf[dl & 1][i], uf[dl & 1][j - i], acc[j]);
      constexpr int r0 = dl == 0 ? 4 : 0;
      if constexpr (dl < 3 && n >= r0 && n < r0 + 24)
        rd1(std::integral_constant<int, dl + 1>{}, std::integral_constant<int, n - r0>{});
      if constexpr (dl == 3 && jh < 7 && n - g0 == 1) *(LAS f32x4*)(pp + (jh + 1) * (1024 + 128)) = acc[jh + 1];
      if constexpr (dl == 3 && jh < 7 && n - g0 == (jh > 1 ? 3 : 2)) *(LAS f32x4*)(pp + (jh + 2) * (1024 + 128)) = acc[jh + 2];
      __builtin_amdgcn_sched_barrier(0);
    });
  });
  *(LAS f32x4*)pp = acc[0];
  *(LAS f32x4*)(pp + 1024 + 128) = acc[1];
}

DEV void compute_waves(const Args& A, char* smem, int tid, int w, int b KIDP) {
  TS_DECL
  char* ws = A.ws;
  int lane = tid & 63;
  const u32 lds0 = (u32)(uintptr_t)smem;
  const auto rsw = __builtin_amdgcn_make_buffer_rsrc(ws, 0, (int)WS_USED, 0x00020000);
  const int d0 = 2 * chan_pair(b);
  const u32 v_dsk = *(const u32*)(A.dskip + d0);
  u32x4 gr[4], xg[4];
  bool early = false;
  auto try_early = [&]() {
    if (!early && __builtin_amdgcn_readfirstlane(lds_ld(lds0 + L_MISC + 44)) != 0u) {
      early = true;
#pragma unroll
      for (int j = 0; j < 4; ++j)
        xg[j] = __builtin_amdgcn_raw_buffer_load_b128(rsw, OFF_XG + (d0 >> 1) * XG_ROW + tid * 16 + j * 4096, 0, XG_AUX);
#pragma unroll
      for (int j = 0; j < 4; ++j)
        gr[j] = __builtin_amdgcn_raw_buffer_load_b128(rsw, OFF_GRAN + (b & 7) * GR_SZ + tid * 16 + j * 4096, 0, GR_AUX);
    }
  };
  {
    const int ch = w >> 1, n = tid & 127;
    const u32 pi = d0 * SN + tid;
    const u16 l_dt = A.logdt[d0 + ch], l_a = A.logA[pi], l_b = A.Aim[pi];
    const u16 l_cr = A.Cre[pi], l_ci = A.Cim[pi], l_cr2 = A.Cre[pi ^ 64], l_ci2 = A.Cim[pi ^ 64];
    if (tid < 16) *(u32*)(smem + L_MISC + tid * 4) = 0u;
    if (n < 8) *(u32*)(smem + KG_TAB + ch * KG_TROW + n * 4) = 0u;
    __builtin_amdgcn_sched_barrier(0);
    u32 wr[8];
#pragma unroll
    for (int r = 0; r < 8; ++r) { wr[r] = lds0 + ch * KG_CH + r * 512 + ((n * 4) ^ (r << 4)); asm volatile("" : "+v"(wr[r])); }
    const int xt = w & 1, r16 = lane & 15, g = lane >> 4;
    u32 fa[2], fb[2], fc[2];
#pragma unroll
    for (int o = 0; o < 2; ++o) {
      const int rc = min(r16, 4);
      fa[o] = lds0 + ch * KG_CH + (16 * xt + r16) * 512 + (((o * 4 + g) ^ (r16 & 7)) << 4);
      fb[o] = lds0 + ch * KG_CH + (32 + r16) * 512 + (((o * 4 + g) ^ (r16 & 7)) << 4);
      fc[o] = lds0 + ch * KG_CH + (48 + rc) * 512 + (((o * 4 + g) ^ rc) << 4);
      asm volatile("" : "+v"(fa[o]), "+v"(fb[o]), "+v"(fc[o]));
    }
    const int i0 = 16 * xt + 4 * g;
    const int ts2 = r16 < 8 ? 16 : (r16 < 12 ? 8 : 4);
    const int tb = r16 < 8 ? r16 : (r16 < 12 ? 248 + r16 : 384 + 64 * ((r16 >> 1) & 1) + (r16 & 1));
    u32 ta[4];
#pragma unroll
    for (int e = 0; e < 4; ++e) { ta[e] = lds0 + KG_TAB + ch * KG_TROW + 32 + tb * 2 + ts2 * (i0 + e); asm volatile("" : "+v"(ta[e])); }
    u32 tc = lds0 + KG_TAB + ch * KG_TROW + 32 + 2 * ((r16 < 4 || i0 == 0) ? 512 + 32 * min(r16, 4) + i0 : 648);
    asm volatile("" : "+v"(tc));
    u32 ia[10];
    {
      const float f0 = (float)(8 * tid);
#pragma unroll
      for (int j = 0; j < 5; ++j) {
        const u32 bits = pk_bf16(f0 + (float)(2 * j - 2), f0 + (float)(2 * j - 1));
        const u32 lin = lds0 + KG_TAB + (8 * tid + 2 * j - 2 + 16) * 2;
        ia[2 * j] = tid < 24 ? lin : (bits & 0xffffu) * 2 + (lds0 + KG_TAB - (0x4300 - 144) * 2);
        ia[2 * j + 1] = tid < 24 ? lin + 2 : (bits >> 16) * 2 + (lds0 + KG_TAB - (0x4300 - 144) * 2);
        asm volatile("" : "+v"(ia[2 * j]), "+v"(ia[2 * j + 1]));
      }
    }
    __builtin_amdgcn_sched_barrier(0);
    lds_bar();
    __builtin_amdgcn_sched_barrier(0);
    const u32 ea = pk_bf16(exp_acc(bf2f(l_dt)), exp_acc(bf2f(l_a)));
    const float dt = bflo(ea);
    const float a2 = -(bfhi(ea) * dt) * 1.44269504088896340736f;
    const float bi = bf2f(l_b) * dt;
    const float rh = bi * 0.15915494309189533577f;
    const float rl = fmaf(bi, 6.420638e-9f, fmaf(bi, 0.15915494309189533577f, -rh));
    u32 mx = max(max((u32)l_cr & 0x7fffu, (u32)l_ci & 0x7fffu), max((u32)l_cr2 & 0x7fffu, (u32)l_ci2 & 0x7fffu));
    mx = max(mx, (u32)__builtin_amdgcn_update_dpp(0, (int)mx, 0xB1, 0xf, 0xf, true));
    mx = max(mx, (u32)__builtin_amdgcn_update_dpp(0, (int)mx, 0x4E, 0xf, 0xf, true));
    mx = max(mx, (u32)__builtin_amdgcn_update_dpp(0, (int)mx, 0x141, 0xf, 0xf, true));
    mx = max(mx, (u32)__builtin_amdgcn_update_dpp(0, (int)mx, 0x140, 0xf, 0xf, true));
    mx = max(max((u32)__builtin_amdgcn_readlane((int)mx, 0), (u32)__builtin_amdgcn_readlane((int)mx, 16)),
             max((u32)__builtin_amdgcn_readlane((int)mx, 32), (u32)__builtin_amdgcn_readlane((int)mx, 48)));
    const u32 ex = min(max(mx >> 7, 2u), 252u) + 1u;
    const float fs = 2.0f * dt * __uint_as_float(ex << 23), isc = __uint_as_float((254u - ex) << 23);
    cpx z[12];
#pragma unroll
    for (int p = 0; p < 12; p += 3) {
      z[p] = cpow2(a2, rh, rl, (float)(1 << p));
      z[p + 1] = cmul(z[p], z[p]);
      z[p + 2] = cmul(z[p + 1], z[p + 1]);
    }
    {
      cpx V[56];
      auto put = [&](int i) { *(LAS u32*)(wr[i & 7] + (i >> 3) * 4096) = pk_f16(V[i][0], V[i][1]); };
      V[0] = cpx{1.0f, 0.0f};
      V[1] = z[3]; V[2] = z[4]; V[4] = z[5]; V[8] = z[6]; V[16] = z[7];
      V[32] = cpx{bf2f(l_cr) * isc, bf2f(l_ci) * -isc};
      constexpr int OP[47][3] = {
          {3, 1, 4},   {5, 1, 5},   {6, 2, 5},   {33, 32, 0}, {34, 32, 1}, {36, 32, 2}, {40, 32, 8}, {44, 32, 9},
          {7, 3, 5},   {9, 1, 6},   {10, 2, 6},  {11, 3, 6},  {35, 34, 0}, {37, 36, 0}, {38, 36, 1}, {41, 34, 8},
          {12, 4, 6},  {13, 5, 6},  {14, 6, 6},  {15, 7, 6},  {39, 38, 0}, {42, 36, 8}, {43, 38, 8}, {45, 36, 9},
          {17, 1, 7},  {18, 2, 7},  {19, 3, 7},  {20, 4, 7},  {46, 40, 9}, {47, 42, 9}, {48, 32, 10}, {49, 40, 10},
          {21, 5, 7},  {22, 6, 7},  {23, 7, 7},  {24, 8, 7},  {50, 44, 10}, {51, 46, 10}, {52, 32, 11}, {25, 9, 7},
          {26, 10, 7}, {27, 11, 7}, {28, 12, 7}, {29, 13, 7}, {30, 14, 7}, {31, 15, 7}, {0, 0, 0}};
      put(0); put(1); put(2); put(4); put(8); put(16); put(32);
      static_for<0, 13>([&](auto G) {
        constexpr int g4 = decltype(G)::value * 4;
        f32x2 t[4];
#pragma unroll
        for (int k = 0; k < 4; ++k)
          if (g4 + k < 46) t[k] = V[OP[g4 + k][1]] * f32x2{z[OP[g4 + k][2]][0], z[OP[g4 + k][2]][0]};
#pragma unroll
        for (int k = 0; k < 4; ++k)
          if (g4 >= 4 && g4 - 4 + k < 46) put(OP[g4 - 4 + k][0]);
#pragma unroll
        for (int k = 0; k < 4; ++k)
          if (g4 + k < 46) {
            const cpx a = V[OP[g4 + k][1]];
            const float zi = z[OP[g4 + k][2]][1];
            V[OP[g4 + k][0]] = __builtin_elementwise_fma(f32x2{a[1], a[0]}, OP[g4 + k][0] < 32 ? f32x2{-zi, zi} : f32x2{zi, -zi}, t[k]);
          }
        __builtin_amdgcn_sched_barrier(0);
      });
    }
    lds_post(lds0 + L_MISC + 8 + ch * 4, lane);
    lds_wait(lds0 + L_MISC + 8 + ch * 4, 2u);
    {
      f32x4 acc0 = {0, 0, 0, 0}, acc1 = {0, 0, 0, 0};
#pragma unroll
      for (int s = 0; s < 8; ++s) {
        const u32x4 a = lds_rd128(fa[s & 1] + (s >> 1) * 128);
        acc0 = mfma_f16(a, lds_rd128(fb[s & 1] + (s >> 1) * 128), acc0);
        acc1 = mfma_f16(a, lds_rd128(fc[s & 1] + (s >> 1) * 128), acc1);
      }
      try_early();
      acc0 *= fs; acc1 *= fs;
      const u32 v01 = pk_bf16(acc0[0], acc0[1]), v23 = pk_bf16(acc0[2], acc0[3]);
      *(LAS u16*)ta[0] = (u16)v01; *(LAS u16*)ta[1] = (u16)(v01 >> 16);
      *(LAS u16*)ta[2] = (u16)v23; *(LAS u16*)ta[3] = (u16)(v23 >> 16);
      *(LAS u32x2*)tc = u32x2{pk_bf16(acc1[0], acc1[1]), pk_bf16(acc1[2], acc1[3])};
    }
    lds_post(lds0 + L_MISC + 16, lane);
    lds_wait(lds0 + L_MISC + 16, 4u);
    try_early();
#pragma unroll
    for (int cc = 0; cc < 2; ++cc) {
      u32 kv[5];
#pragma unroll
      for (int j = 0; j < 5; ++j)
        kv[j] = (u32)*(const LAS u16*)(ia[2 * j] + cc * KG_TROW) | ((u32)*(const LAS u16*)(ia[2 * j + 1] + cc * KG_TROW) << 16);
      char* o0 = smem + cc * KIMG_CH + 32 + tid * 16;
      *(u32x4*)o0 = u32x4{kv[1], kv[2], kv[3], kv[4]};
      *(u32x4*)(o0 + KIMG_C1) = u32x4{kv[0], kv[1], kv[2], kv[3]};
    }
    if (tid < 8) *(u32x4*)(smem + (tid >> 2) * KIMG_CH + ((tid >> 1) & 1) * KIMG_C1 + (tid & 1) * 16) = u32x4{0, 0, 0, 0};
    lds_post(lds0 + L_MISC + 52, lane);
    lds_wait_slow(lds0 + L_MISC + 44, 1u);
    try_early();
  }
  const u32 l_dsk = __builtin_amdgcn_readfirstlane(v_dsk);
  lds_wait(lds0 + L_MISC, 1u);
  const u32 tag = __builtin_amdgcn_readfirstlane(lds_ld(lds0 + L_MISC + 4));
  TS(1)
  const int xcd = b & 7, idx = b >> 3;
  const int rb = (xcd & 1) * 8 + (idx & 7), c = (xcd >> 1) * 4 + (idx >> 3);
  const int rb64 = xcd * 4 + (idx & 3), ct = idx >> 2;

#pragma unroll 1
  for (int layer = 0; layer < 2; ++layer) {
    {
      lane = fresh_lane();
      tid = w * 64 + lane;
      const u32 goff = OFF_GRAN + (b & 7) * GR_SZ + tid * 16, xoff = OFF_XG + (d0 >> 1) * XG_ROW + tid * (layer ? 64 : 16);
      lds_wait_slow(lds0 + L_MISC + 52, 4u + layer);
      u32 ka = conv_ka(lds0, lane, w);
      asm volatile("" : "+v"(ka));
      u32x4 k0[8];
#pragma unroll
      for (int i = 0; i < 8; ++i) {
        const u32x2 lo = lds_tr32(ka + i * 512 + 16), hi = lds_tr32(ka + i * 512);
        k0[i] = u32x4{lo[0], lo[1], hi[0], hi[1]};
      }
      __builtin_amdgcn_sched_barrier(0);
      const u32 ltag = tag + layer;
      bool needx = !early, needg = !early;
      early = false;
      lds_wait_slow(lds0 + L_MISC + 44, 1u + layer);
#pragma unroll 1
      for (int sp = 0; sp < (1 << 18); ++sp) {
        if (needx) {
#pragma unroll
          for (int j = 0; j < 4; ++j) xg[j] = __builtin_amdgcn_raw_buffer_load_b128(rsw, xoff + j * (layer ? 16 : 4096), 0, XG_AUX);
        }
        if (needg) {
          __builtin_amdgcn_s_sleep(GR_DELAY2);
#pragma unroll
          for (int j = 0; j < 4; ++j) gr[j] = __builtin_amdgcn_raw_buffer_load_b128(rsw, goff + j * 4096, 0, GR_AUX);
        }
        u32 bx = 0, bg = 0;
#pragma unroll
        for (int j = 0; j < 4; ++j) { bx |= (xg[j][1] ^ ltag) | (xg[j][3] ^ ltag); bg |= (gr[j][1] ^ ltag) | (gr[j][3] ^ ltag); }
        needx = __builtin_amdgcn_ballot_w64(bx != 0u) != 0ull;
        needg = __builtin_amdgcn_ballot_w64(bg != 0u) != 0ull;
        if (!needx && !needg) break;
        __builtin_amdgcn_s_sleep(GR_SLEEP);
      }
      lds_bump(lds0 + L_MISC + 36);
      TSL(2)
      float base[2][8];
#pragma unroll
      for (int r = 0; r < 2; ++r) {
        u32 up[2][2];
#pragma unroll
        for (int j = 0; j < 2; ++j) {
          const u32 g0 = gr[2 * j][2 * r], g1 = gr[2 * j + 1][2 * r], x0 = xg[2 * j][2 * r], x1 = xg[2 * j + 1][2 * r];
          const f32x2 rs = {__builtin_amdgcn_rcpf(bfhi(g0)), __builtin_amdgcn_rcpf(bfhi(g1))};
          const f32x2 mm = {bflo(g0), bflo(g1)};
#pragma unroll
          for (int cc = 0; cc < 2; ++cc) {
            const f32x2 xx = cc ? f32x2{bfhi(x0), bfhi(x1)} : f32x2{bflo(x0), bflo(x1)};
            const float dk = cc ? bfhi(l_dsk) : bflo(l_dsk);
            const f32x2 u = (xx - mm) * rs;
            up[cc][j] = pk_bf16(u[0], u[1]);
            const f32x2 bs = __builtin_elementwise_fma(u, f32x2{dk, dk}, xx);
            base[cc][4 * j + r] = bs[0]; base[cc][4 * j + r + 2] = bs[1];
          }
        }
#pragma unroll
        for (int cc = 0; cc < 2; ++cc)
          *(u32x2*)(smem + CV_U + (cc * 2 + r) * CV_UROW + 256 + tid * 8) = u32x2{up[cc][0], up[cc][1]};
      }
      {
        const u32 zz = opq(0u);
        *(u32*)(smem + CV_U + w * CV_UROW + lane * 4) = zz;
        *(u32*)(smem + CV_P + w * CV_PROW + 12) = zz;
      }
      lds_post(lds0 + L_MISC + 32, lane);
      lds_wait(lds0 + L_MISC + 32, 4u * (layer + 1));
      TSL(3)
      conv_mfma(lds0, ka, k0, lane, w TSA);
      lds_post(lds0 + L_MISC + 20, lane);
      lds_wait(lds0 + L_MISC + 20, 4u * (layer + 1));
      TSL(4)
      const int off = CV_P + 16 + tid * 32 + (tid >> 2) * 16;
      const int offm = off + CV_PROW - 4 - (((tid & 3) == 0 && tid > 0) ? (int)opq(16u) : 0);
      const u32 xo = OFF_X + layer * X_SZ + ((tid >> 4) * SD + d0) * 256 + (tid & 15) * 16;
#pragma unroll
      for (int cc = 0; cc < 2; ++cc) {
        const char* p0 = smem + off + (2 * cc) * CV_PROW;
        const f32x4 a0 = *(const f32x4*)p0, a1 = *(const f32x4*)(p0 + 16);
        const f32x4 b0 = *(const f32x4*)(p0 + CV_PROW), b1 = *(const f32x4*)(p0 + CV_PROW + 16);
        const float bm = *(const float*)(smem + offm + (2 * cc) * CV_PROW);
        const float o[8] = {base[cc][0] + a0[0] + bm,    base[cc][1] + a0[1] + b0[0], base[cc][2] + a0[2] + b0[1],
                            base[cc][3] + a0[3] + b0[2], base[cc][4] + a1[0] + b0[3], base[cc][5] + a1[1] + b1[0],
                            base[cc][6] + a1[2] + b1[1], base[cc][7] + a1[3] + b1[2]};
        const u32x4 ov = {pk_bf16(o[0], o[1]), pk_bf16(o[2], o[3]), pk_bf16(o[4], o[5]), pk_bf16(o[6], o[7])};
        __builtin_amdgcn_raw_buffer_store_b128(ov, rsw, xo + cc * 256, 0, 16);
      }
      wait_vm<0>();
      if (lds_arrive(lds0 + L_MISC + 24, lane) == 4u * layer + 3u) grid_signal(ws, layer, b, lane);
      TSL(5)
    }
    lane = fresh_lane();
    asm volatile("" : "+s"(ws));
    const int mq = w;
    int col = lane & 15, g = lane >> 4, q = (lane & 15) >> 2, p = lane & 3;
    const u32 hoff = OFF_H + layer * H_SZ;
    const int key = q | ((g & 1) << 2);
    u32 xa[2], wa[3];
#pragma unroll
    for (int j = 0; j < 2; ++j) {
      xa[j] = lds0 + (8 * g + q) * 256 + (((2 * mq + j) ^ key) * 32) + 8 * p;
      asm volatile("" : "+v"(xa[j]));
    }
#pragma unroll
    for (int r = 0; r < 3; ++r) {
      wa[r] = lds0 + r * F_BASE + lane * 16;
      asm volatile("" : "+v"(wa[r]));
    }
    auto lw128 = [&](auto O) { constexpr int o = decltype(O)::value; return lds_rd128(wa[o / F_BASE] + o % F_BASE); };
    f32x4 acc[11][2];
#pragma unroll
    for (int t = 0; t < 11; ++t)
#pragma unroll
      for (int j = 0; j < 2; ++j) acc[t][j] = f32x4{0, 0, 0, 0};
    f32x4 s1[2] = {{0, 0, 0, 0}, {0, 0, 0, 0}}, s2[2] = {{0, 0, 0, 0}, {0, 0, 0, 0}};
    const u32 one2 = opq(0x3f803f80u);
    const u32x4 ones = {one2, one2, one2, one2};
    u32x4 xf[16][2], wf[2][6];
    auto rd = [&](auto K) {
      constexpr int k = decltype(K)::value, o = ((k >> 1) % 3) * 16384 + (k & 1) * 8192;
#pragma unroll
      for (int j = 0; j < 2; ++j) {
        const u32x2 lo = lds_tr32(xa[j] + o), hi = lds_tr32(xa[j] + o + 1024);
        xf[k][j] = u32x4{lo[0], lo[1], hi[0], hi[1]};
      }
      static_for<0, 6>([&](auto T) {
        constexpr int t = decltype(T)::value;
        wf[k & 1][t] = lw128(std::integral_constant<int, F_W1 + (k * 6 + t) * 1024>{});
      });
    };
    auto mm = [&](auto K) {
      constexpr int k = decltype(K)::value;
#pragma unroll
      for (int j = 0; j < 2; ++j) {
        s2[j] = mfma_bf16(xf[k][j], xf[k][j], s2[j]);
        s1[j] = mfma_bf16(ones, xf[k][j], s1[j]);
      }
#pragma unroll
      for (int t = 0; t < 6; ++t)
#pragma unroll
        for (int j = 0; j < 2; ++j) acc[t][j] = mfma_bf16(wf[k & 1][t], xf[k][j], acc[t][j]);
    };
    auto weave = [&]() {
#pragma unroll
      for (int k = 0; k < 10; ++k) {
        __builtin_amdgcn_sched_group_barrier(0x008, 1, 0);
        __builtin_amdgcn_sched_group_barrier(0x100, 1, 0);
      }
      __builtin_amdgcn_sched_group_barrier(0x008, 6, 0);
      __builtin_amdgcn_sched_barrier(0);
    };
    static_for<0, 8>([&](auto I) {
      constexpr int i = decltype(I)::value;
      wait_lgkm0();
      __builtin_amdgcn_sched_barrier(0);
      bar();
      if constexpr (i == 0) { TSL(6) }
      rd(std::integral_constant<int, 2 * i>{});
      if constexpr (i > 0) mm(std::integral_constant<int, 2 * i - 1>{});
      weave();
      rd(std::integral_constant<int, 2 * i + 1>{});
      mm(std::integral_constant<int, 2 * i>{});
      weave();
    });
    wait_lgkm0();
    __builtin_amdgcn_sched_barrier(0);
    bar();
    TSL(7)
#pragma unroll
    for (int j = 0; j < 2; ++j) {
      s2[j] = mfma_bf16(xf[15][j], xf[15][j], s2[j]);
      s1[j] = mfma_bf16(ones, xf[15][j], s1[j]);
    }
    lane = fresh_lane();
    col = lane & 15; g = lane >> 4; q = (lane & 15) >> 2; p = lane & 3;
    const u32 csa = lds0 + L_CS + g * 16;
    f32x4 cs6[6];
    __builtin_amdgcn_sched_barrier(0);
    u32 so[6];
    float lm[2], ls[2], lc[2], dv[2];
    u32 pk0, pk1;
    static_for<0, 12>([&](auto N) {
      constexpr int n = decltype(N)::value;
      acc[n >> 1][n & 1] = mfma_bf16(wf[1][n >> 1], xf[15][n & 1], acc[n >> 1][n & 1]);
      if constexpr (n >= 6) cs6[n - 6] = __builtin_bit_cast(f32x4, lds_rd128(csa + (n - 6) * 64));
      if constexpr (n < 3) {
        const int lt = rb * 8 + 2 * mq + (g & 1);
        const u32 hb = hoff + lt * (NKF * 1024) + col * 16;
#pragma unroll
        for (int i = 2 * n; i < 2 * n + 2; ++i) {
          const int o0 = i < 3 ? c * 6 + 2 * i : i < 5 ? 96 + c * 3 + 2 * (i - 3) : 142 + c * 2, o1 = o0 + 1;
          const u32 f0 = (i < 4 || c < 15) ? o0 * 256 : 0x70000000u;
          const u32 f1 = (i < 3 || (i != 4 && c < 15)) ? o1 * 256 : 0x70000000u;
          so[i] = hb + f0 + (g >> 1) * (f1 - f0);
        }
      }
      if constexpr (n == 3 || n == 4) {
        constexpr int j = n - 3;
        const float e0 = s2[j][0], e1 = s2[j][1], e2 = s2[j][2], e3 = s2[j][3];
        const float d01 = (lane & 1) ? e1 : e0, d23 = (lane & 1) ? e3 : e2, dd = (lane & 2) ? d23 : d01;
        dv[j] = (g == q) ? dd : 0.f;
      }
      if constexpr (n == 5 || n == 6) dv[n - 5] = sum_groups(dv[n - 5]);
      if constexpr (n == 7) {
        const float m0 = s_div512(s1[0][0]), m1 = s_div512(s1[1][0]);
        pk0 = pk_bf16(m0, m1);
        dv[0] = s_nfma(m0, m0, s_div512(dv[0]));
        dv[1] = s_nfma(m1, m1, s_div512(dv[1]));
      }
      if constexpr (n == 8) { pk1 = pk_bf16(fmaxf(dv[0], 0.f), fmaxf(dv[1], 0.f)); lm[0] = bflo(pk0); lm[1] = bfhi(pk0); }
      if constexpr (n == 9) pk1 = pk_bf16(s_addeps(bflo(pk1)), s_addeps(bfhi(pk1)));
      if constexpr (n == 10) pk1 = pk_bf16(__builtin_amdgcn_sqrtf(bflo(pk1)), __builtin_amdgcn_sqrtf(bfhi(pk1)));
      if constexpr (n == 11) {
        ls[0] = bflo(pk1); ls[1] = bfhi(pk1);
        lc[0] = __builtin_amdgcn_rcpf(ls[0]); lc[1] = __builtin_amdgcn_rcpf(ls[1]);
      }
      __builtin_amdgcn_sched_barrier(0);
    });
    lc[0] *= -1.44269504088896340736f; lc[1] *= -1.44269504088896340736f;
    u32x4 wv[2][16];
    static_for<0, 4>([&](auto K) { wv[0][decltype(K)::value] = lw128(std::integral_constant<int, f_tile(6) + decltype(K)::value * 1024>{}); });
    u32 hv[11][2];
    auto hstore = [&](int t) {
      const int t1 = t == 8 ? 8 : t + 1;
      __builtin_amdgcn_raw_buffer_store_b128(tr4(hv[t][0], hv[t][1], hv[t1][0], hv[t1][1]), rsw, so[(t + 1) >> 1], 0, 16);
    };
    auto epi = [&](int t) {
      const f32x4 cs = __builtin_bit_cast(f32x4, lds_rd128(csa + t * 64));
#pragma unroll
      for (int j = 0; j < 2; ++j) hv[t][j] = glu_tile(acc[t][j], cs, lm[j], ls[j], lc[j]);
    };
#pragma unroll
    for (int t = 0; t < 6; ++t) {
#pragma unroll
      for (int j = 0; j < 2; ++j) hv[t][j] = glu_tile(acc[t][j], cs6[t], lm[j], ls[j], lc[j]);
      if (t & 1) hstore(t - 1);
    }
    static_for<4, 8>([&](auto K) { wv[0][decltype(K)::value] = lw128(std::integral_constant<int, f_tile(6) + decltype(K)::value * 1024>{}); });
    __builtin_amdgcn_sched_barrier(0);
    TSL(13)
    constexpr int PRE = F_PRE, WIN = F_WIN;
    const u32 ho = hoff + (rb64 * 4 + mq) * (NKF * 1024) + lane * 16;
    auto hload = [&](auto V, u32x4& dst) {
      constexpr int v = decltype(V)::value;
      gload16_sc1_o<(v & 7) * 1024 - 4096>(dst, ho, ws + (v >> 3) * 8192 + 4096);
    };
    u32x4 hf[NKF];
    static_for<6, 11>([&](auto T) {
      constexpr int t = decltype(T)::value, e = t - 1, wb = t & 1;
      f32x4 cv;
      float ga[4], ua[4];
      static_for<0, 32>([&](auto S) {
        constexpr int sg = decltype(S)::value, k = sg >> 1, j = sg & 1;
        if constexpr ((sg & 7) == 0) {
          wait_lgkm<(t == 10 && sg == 24) ? 0 : 4>();
          __builtin_amdgcn_sched_barrier(0);
        }
        acc[t][j] = mfma_bf16(wv[wb][k], xf[k][j], acc[t][j]);
        if constexpr (t > 6 && sg == 0) cv = __builtin_bit_cast(f32x4, lds_rd128(csa + e * 64));
        if constexpr ((sg & 7) < 4) {
          constexpr int kk = 8 + (sg >> 3) * 4 + (sg & 3);
          if constexpr (kk < 16) wv[wb][kk] = lw128(std::integral_constant<int, f_tile(t) + kk * 1024>{});
          else if constexpr (t < 10) wv[wb ^ 1][kk - 16] = lw128(std::integral_constant<int, f_tile(t + 1) + (kk - 16) * 1024>{});
        }
        if constexpr (t > 6) {
          constexpr int st = sg >> 2, a = sg & 3, jj = a >> 1, ee = a & 1;
          if constexpr (st == 1) { ga[a] = s_nfma(lm[jj], cv[ee], acc[e][jj][ee]); ua[a] = s_nfma(lm[jj], cv[2 + ee], acc[e][jj][2 + ee]); }
          if constexpr (st == 2) ga[a] = s_mul(ga[a], lc[jj]);
          if constexpr (st == 3) ga[a] = __builtin_amdgcn_exp2f(ga[a]);
          if constexpr (st == 4) ga[a] = s_fma(ga[a], ls[jj], ls[jj]);
          if constexpr (st == 5) ga[a] = __builtin_amdgcn_rcpf(ga[a]);
          if constexpr (st == 6) ua[a] = s_mul(ua[a], ga[a]);
        }
        if constexpr (t > 6 && sg == 28) { hv[e][0] = pk_bf16(ua[0], ua[1]); hv[e][1] = pk_bf16(ua[2], ua[3]); }
        if constexpr (sg == 30 && (t == 8 || t == 9)) hstore(2 * t - 10);
        if constexpr (t == 10 && sg >= 4 && sg < 4 + 2 * PRE && (sg & 1) == 0)
          hload(std::integral_constant<int, (sg >= 4 ? (sg - 4) >> 1 : 0)>{}, hf[(sg >= 4 ? sg - 4 : 0) >> 1]);
        __builtin_amdgcn_sched_barrier(0);
      });
      if constexpr (t == 10) { epi(10); hstore(9); __builtin_amdgcn_sched_barrier(0); }
      if constexpr (t == 6) wait_vm<0>();
      if constexpr (t == 10) wait_vm<PRE + 1>();
      if constexpr (t == 10) wait_lgkm0();
      bar();
      __builtin_amdgcn_sched_barrier(0);
    });
    TSL(8)
    f32x4 ac2[4] = {{0, 0, 0, 0}, {0, 0, 0, 0}, {0, 0, 0, 0}, {0, 0, 0, 0}};
    u32x4 wf2[3][4];
    auto rd2 = [&](auto U) {
      constexpr int u = decltype(U)::value;
      static_for<0, 4>([&](auto I) {
        constexpr int i = decltype(I)::value;
        wf2[u][i] = lw128(std::integral_constant<int, f_wd(u >> 1) + (u & 1) * 4096 + i * 1024>{});
      });
    };
    rd2(std::integral_constant<int, 0>{});
    rd2(std::integral_constant<int, 1>{});
    __builtin_amdgcn_sched_barrier(0);
    constexpr auto issued = [](int u) {
      int n = PRE;
      for (int v = 1; v <= u; ++v) {
        int lim = v + WIN;
        if (v <= F_U2 && lim > 24) lim = 24;
        if (v <= F_U3 && lim > 35) lim = 35;
        if (lim > NKF) lim = NKF;
        const int rate = v > F_U3 ? 8 : F_RATE;
        n = n + rate < lim ? n + rate : (lim > n ? lim : n);
      }
      return n;
    };
    static_for<0, NKF>([&](auto U) {
      constexpr int u = decltype(U)::value;
      static_assert(issued(u) > u && F_U1 >= 6 && F_U2 >= 12 && F_U2 < 25 && F_U3 < 29, "h window / W_down residency");
      asm volatile("s_waitcnt vmcnt(%1)" : "+v"(hf[u]) : "n"(issued(u) - 1 - u + (u < PRE ? 1 : 0)) : "memory");
      wait_lgkm<(u + 1 < NKF ? 4 : 0)>();
      if constexpr (u == F_U1) { wait_vm<issued(u) - PRE>(); bar(); TSL(9) }
      if constexpr (u == F_U2) bar();
      if constexpr (u == F_U3) { bar(); TSL(10) }
      __builtin_amdgcn_sched_barrier(0);
      static_for<0, 4>([&](auto I) {
        constexpr int i = decltype(I)::value;
        ac2[i] = mfma_bf16(wf2[u % 3][i], hf[u], ac2[i]);
        if constexpr (u + 2 < NKF)
          wf2[(u + 2) % 3][i] = lw128(std::integral_constant<int, f_wd((u + 2) >> 1) + (u & 1) * 4096 + i * 1024>{});
        static_for<issued(u), issued(u + 1)>([&](auto V) {
          constexpr int cnt = issued(u + 1) - issued(u), ix = decltype(V)::value - issued(u);
          if constexpr (ix * 4 / (cnt > 4 ? cnt : 4) == i) hload(V, hf[decltype(V)::value]);
        });
        __builtin_amdgcn_sched_barrier(0);
      });
    });
    TSL(11)
    lane = fresh_lane();
    {
    const int col = lane & 15, g = lane >> 4, q = (lane & 15) >> 2, p = lane & 3;
    const int l = rb64 * 64 + mq * 16 + col;
    u32x2 ov[4];
#pragma unroll
    for (int i = 0; i < 4; ++i) {
      const u32x2 rv = lds_tr32(lds0 + F_RES + (i * 16 + 4 * g + q) * 144 + mq * 32 + p * 8);
      const u32 o0 = pk_bf16(ac2[i][0], ac2[i][1]), o1 = pk_bf16(ac2[i][2], ac2[i][3]);
      ov[i] = u32x2{pk_bf16(bflo(rv[0]) + bflo(o0), bfhi(rv[0]) + bfhi(o0)),
                    pk_bf16(bflo(rv[1]) + bflo(o1), bfhi(rv[1]) + bfhi(o1))};
    }
    if (layer == 0) {
      lds_bar();
      float sa = 0.f, sq = 0.f;
#pragma unroll
      for (int i = 0; i < 4; ++i)
#pragma unroll
        for (int e = 0; e < 2; ++e) { sa = dot2bf(ov[i][e], 0x3f803f80u, sa); sq = dot2bf(ov[i][e], ov[i][e], sq); }
      sa = sum_groups(sa); sq = sum_groups(sq);
      __builtin_amdgcn_raw_buffer_store_b128(u32x4{__float_as_uint(sa), tag, __float_as_uint(sq), tag}, rsw,
                                             OFF_PART + (l * 8 + ct) * 16 + g * 0x10000000u, 0, 16);
#pragma unroll
      for (int i = 0; i < 4; ++i) {
        const u32 give = (col & 1) ? ov[i][0] : ov[i][1];
        const u32 got = (u32)__builtin_amdgcn_update_dpp(0, (int)give, 0xB1, 0xf, 0xf, true);
        const u32x4 gv = {(col & 1) ? got : ov[i][0], tag + 1u, (col & 1) ? ov[i][1] : got, tag + 1u};
        const u32 go = OFF_XG + (ct * 32 + i * 8 + g * 2 + (col & 1)) * XG_ROW + (l & ~1) * 8;
        __builtin_amdgcn_raw_buffer_store_b128(gv, rsw, go, 0, 16);
      }
    } else {
      const u32x4 t0 = tr4(ov[0][0], ov[1][0], ov[2][0], ov[3][0]), t1 = tr4(ov[0][1], ov[1][1], ov[2][1], ov[3][1]);
      const auto rso = __builtin_amdgcn_make_buffer_rsrc(A.out, 0, SL * SD * 2, 0x00020000);
      const u32 oo = (l * SD + ct * 64 + g * 16) * 2;
      __builtin_amdgcn_raw_buffer_store_b128(u32x4{t0[0], t1[0], t0[1], t1[1]}, rso, oo, 0, OUT_AUX);
      __builtin_amdgcn_raw_buffer_store_b128(u32x4{t0[2], t1[2], t0[3], t1[3]}, rso, oo + 16, 0, OUT_AUX);
    }
    }
#pragma unroll
    for (int j = 0; j < 4; ++j) asm volatile("" : "=v"(gr[j]), "=v"(xg[j]));
    TSL(12)
    TSL_FLUSH
  }
  if (b == 0 && w == 0 && fresh_lane() == 0) __hip_atomic_store((gu32*)(ws + OFF_EPOCH), tag + 1u, __ATOMIC_RELAXED, __HIP_MEMORY_SCOPE_AGENT);
}

DEV void publish_ln(char* ws, u32 off, float sum, float sumsq, u32 tag, int b, int lane) {
  float m, sd;
  ln_consts(sum8(sum), sum8(sumsq), m, sd);
  const u32 val = (__float_as_uint(m) >> 16) | (__float_as_uint(sd) & 0xffff0000u);
  __hip_atomic_store((gu64*)(ws + off + (lane & 7) * GR_SZ) + (lane >> 4) * 512 + 2 * b + ((lane >> 3) & 1),
                     ((unsigned long long)tag << 32) | val, __ATOMIC_RELAXED, __HIP_MEMORY_SCOPE_AGENT);
}

DEV void helper_waves(const Args& A, char* smem, int tid, int lw, int b KIDP) {
  TS_DECL
  char* ws = A.ws;
  const int lane = tid & 63;
  const u32 lds0 = (u32)(uintptr_t)smem;
  const auto rsw = __builtin_amdgcn_make_buffer_rsrc(ws, 0, (int)WS_USED, 0x00020000);
  const int kr = lane >> 3, no = lane & 7;
  u32 tag;
  {
    char* img = smem + KG_PACK + (lw - 1) * PK_IMG;
    if (lw <= 1) {
      const u16* src = lw == 0 ? A.x + (size_t)(8 * b + kr) * SD + no * 8
                               : A.x + (size_t)(64 * (b >> 3) + 8 * no) * SD + (b & 7) * 64 + kr * 8;
      const int stp = lw == 0 ? 64 : SD;
      u32x4 v[8];
#pragma unroll
      for (int i = 0; i < 8; ++i) v[i] = *(const u32x4*)(src + i * stp);
      tag = __builtin_amdgcn_raw_buffer_load_b32(rsw, OFF_EPOCH, 0, 16);
      if (lw == 0 && b < 10) __builtin_amdgcn_raw_buffer_store_b32(0u, rsw, OFF_CNT + (b * 64 + lane) * 128, 0, 16);
      __builtin_amdgcn_sched_barrier(0);
      bar();
      __builtin_amdgcn_sched_barrier(0);
      if (lw == 1) {
        tag += 1u;
        if (lane == 0) { lds_st(lds0 + L_MISC + 4, tag); lds_st(lds0 + L_MISC, 1u); }
        const u32 go = OFF_XG + ((b & 7) * 32 + kr * 4) * XG_ROW + (b >> 3) * 128 + no * 16;
#pragma unroll
        for (int i = 0; i < 4; ++i)
#pragma unroll
          for (int e = 0; e < 4; ++e)
            __builtin_amdgcn_raw_buffer_store_b128(u32x4{v[2 * i][e], tag, v[2 * i + 1][e], tag}, rsw,
                                                   go + e * XG_ROW + i * 4096, 0, 16);
        wait_vm<0>();
        if (lane == 0) lds_st(lds0 + L_MISC + 44, 1u);
      } else {
        float a[2] = {0.f, 0.f}, q[2] = {0.f, 0.f};
#pragma unroll
        for (int i = 0; i < 8; ++i)
#pragma unroll
          for (int j = 0; j < 4; ++j) {
            a[j & 1] = dot2bf(v[i][j], 0x3f803f80u, a[j & 1]);
            q[j & 1] = dot2bf(v[i][j], v[i][j], q[j & 1]);
          }
        lds_wait(lds0 + L_MISC, 1u);
        tag = lds_ld(lds0 + L_MISC + 4);
        wait_vm<0>();
        publish_ln(ws, OFF_GRAN, a[0] + a[1], q[0] + q[1], tag, b, lane);
      }
    } else {
      if (lw == 2) {
        const u32 t0 = __builtin_amdgcn_raw_buffer_load_b32(rsw, ((b >> 3) * TLB_N + (lane % TLB_N)) * TLB_STRIDE + TLB_STRIDE - 256, 0, 16);
        const u16 t1 = A.out[((b >> 3) * 32768) + 16384];
        __builtin_amdgcn_sched_barrier(0);
        bar();
        asm volatile("" ::"v"(t0), "v"((u32)t1));
      } else
      bar();
      __builtin_amdgcn_s_sleep(PK_DELAY);
    }
    TS(12)
    const int id = (3 - lw) * 256 + b;
    const int col = lane & 15, g = lane >> 4, q = (lane & 15) >> 2, p = lane & 3;
    const u32 ra = (u32)(uintptr_t)img + (8 * g + q) * 256 + 8 * p;
    const int rkey = q | ((g & 1) << 2);
    u32x4 fr[9];
    u32 so9[9];
#pragma unroll
    for (int i = 0; i < 9; ++i) so9[i] = 0x7ffffff0u;
    if (id < 352) {
      const int ps = id & 15, ng = id >> 4;
      const size_t so = (size_t)(32 * ps + kr) * SF + 8 * (8 * ng + no < 172 ? 8 * ng + no : 0);
      u32x4 gv[4], uv[4];
#pragma unroll
      for (int i = 0; i < 4; ++i) {
        gv[i] = *(const u32x4*)(A.wg + so + (size_t)i * 8 * SF);
        uv[i] = *(const u32x4*)(A.wu + so + (size_t)i * 8 * SF);
      }
#pragma unroll
      for (int i = 0; i < 4; ++i) {
        char* dst = img + (8 * i + kr) * 256 + ((no ^ ((kr & 3) | ((i & 1) << 2))) * 32);
        *(u32x4*)dst = u32x4{gv[i][0], uv[i][0], gv[i][1], uv[i][1]};
        *(u32x4*)(dst + 16) = u32x4{gv[i][2], uv[i][2], gv[i][3], uv[i][3]};
      }
      wait_lgkm0();
      asm volatile("" ::: "memory");
      f32x4 cs = {0, 0, 0, 0};
#pragma unroll
      for (int t8 = 0; t8 < 8; ++t8) {
        const u32x2 lo = lds_tr32(ra + ((t8 ^ rkey) * 32)), hi = lds_tr32(ra + ((t8 ^ rkey) * 32) + 1024);
        fr[t8] = u32x4{lo[0], lo[1], hi[0], hi[1]};
        const int Tt = 8 * ng + t8;
        so9[t8] = lane * 16 + OFF_WGU + wgu_off(Tt / 11, ps, Tt % 11);
        const u32 sel = col == t8 ? 0x3f803f80u : 0u;
        cs = mfma_bf16(fr[t8], u32x4{sel, sel, sel, sel}, cs);
      }
      fr[8] = __builtin_bit_cast(u32x4, cs);
      if (col < 8) so9[8] = OFF_CSP + ((ps * 176 + 8 * ng + col) * 16 + g * 4) * 4;
    } else if (id < 352 + 8 * NKF) {
      const int ct = (id - 352) & 7, kp = (id - 352) >> 3;
      u32x4 gv[4];
#pragma unroll
      for (int i = 0; i < 4; ++i)
        gv[i] = *(const u32x4*)(A.wd + (size_t)(8 * tile_of_oct(4 * kp + i) + kr) * SD + ct * 64 + no * 8);
#pragma unroll
      for (int i = 0; i < 4; ++i)
        *(u32x4*)(img + (8 * i + kr) * 256 + (((no >> 1) ^ ((kr & 3) | ((i & 1) << 2))) * 32) + (no & 1) * 16) = gv[i];
      wait_lgkm0();
      asm volatile("" ::: "memory");
#pragma unroll
      for (int nt = 0; nt < 4; ++nt) {
        const u32x2 lo = lds_tr32(ra + ((nt ^ rkey) * 32)), hi = lds_tr32(ra + ((nt ^ rkey) * 32) + 1024);
        fr[nt] = u32x4{lo[0], lo[1], hi[0], hi[1]};
        so9[nt] = lane * 16 + OFF_WD + ((ct * NKF + kp) * 4 + nt) * 1024;
      }
    }
    if (id < 352) {
#pragma unroll
      for (int i = 0; i < 8; ++i)
        if ((8 * (id >> 4) + i) % 11 < 6) __builtin_amdgcn_raw_buffer_store_b128(fr[i], rsw, so9[i], 0, 16);
      __builtin_amdgcn_raw_buffer_store_b128(fr[8], rsw, so9[8], 0, 16);
    }
    TS(13)
    lds_post(lds0 + L_MISC + 28, lane);
    lds_wait_slow(lds0 + L_MISC + 36, 4u);
    tag = lds_ld(lds0 + L_MISC + 4);
    wait_vm<0>();
    if (id < 352) {
      if (lane == 0)
        __hip_atomic_fetch_add((gu32*)(ws + OFF_CNT) + (96 + (id >> 4)) * 32, 1u, __ATOMIC_RELAXED, __HIP_MEMORY_SCOPE_AGENT);
#pragma unroll
      for (int i = 0; i < 8; ++i)
        if ((8 * (id >> 4) + i) % 11 >= 6) __builtin_amdgcn_raw_buffer_store_b128(fr[i], rsw, so9[i], 0, 16);
    } else if (id < 352 + 8 * NKF) {
#pragma unroll
      for (int i = 0; i < 4; ++i) __builtin_amdgcn_raw_buffer_store_b128(fr[i], rsw, so9[i], 0, 16);
    }
    wait_vm<0>();
    if (lds_arrive(lds0 + L_MISC + 40, lane) == 3u && lane == 0)
      __hip_atomic_fetch_add((gu32*)(ws + OFF_CNT) + (120 + (b & 7)) * 32, 1u, __ATOMIC_RELAXED, __HIP_MEMORY_SCOPE_AGENT);
  }
  TS(1)
  const int xcd = b & 7, idx = b >> 3;
  const int rb = (xcd & 1) * 8 + (idx & 7), c = (xcd >> 1) * 4 + (idx >> 3);
  const int rb64 = xcd * 4 + (idx & 3), ct = idx >> 2;
  const u32 wbase = OFF_WGU + c * (176 * 1024) + lane * 16;
  const u32 wdo = OFF_WD + ct * NKF * 4096 + (lw - 1) * 8192 + lane * 16;
  if (lw == 0) {
    u32x4 kimg[17];
    const u32 ko = lds0 + lane * 16;
#pragma unroll 1
    for (int layer = 0; layer < 2; ++layer) {
      asm volatile("" : "+s"(ws));
      const u32 xin = OFF_X + layer * X_SZ;
      if (layer == 1) {
#pragma unroll
        for (int i = 0; i < 17; ++i)
          if (i < 16 || lane < 32) *(LAS u32x4*)(ko + i * 1024) = kimg[i];
        lds_post(lds0 + L_MISC + 52, lane);
        u32x4 pv;
        __builtin_amdgcn_s_sleep(GR_DELAY);
#pragma unroll 1
        for (int sp = 0; sp < (1 << 18); ++sp) {
          pv = __builtin_amdgcn_raw_buffer_load_b128(rsw, OFF_PART + b * 1024 + lane * 16, 0, XG_AUX);
          if (__builtin_amdgcn_ballot_w64(((pv[1] ^ tag) | (pv[3] ^ tag)) != 0u) == 0ull) break;
          __builtin_amdgcn_s_sleep(3);
        }
        publish_ln(ws, OFF_GRAN, __uint_as_float(pv[0]), __uint_as_float(pv[2]), tag + 1u, b, lane);
        if (lane == 0) lds_st(lds0 + L_MISC + 44, 2u);
        TSL(8)
      } else {
        lds_wait_slow(lds0 + L_MISC + 52, 4u);
#pragma unroll
        for (int i = 0; i < 17; ++i)
          if (i < 16 || lane < 32) kimg[i] = lds_rd128(ko + i * 1024);
        gu32* fp = (gu32*)(ws + OFF_CNT) + (96 + min((c * 11 >> 3) + lane, (c * 11 + 10) >> 3)) * 32;
        lds_wait_slow(lds0 + L_MISC + 28, 4u);
        __builtin_amdgcn_s_sleep(FL_DELAY);
#pragma unroll 1
        for (int sp = 0; sp < (1 << 20); ++sp) {
          u32 v = 16u;
          if (lane < 3) v = __hip_atomic_load(fp, __ATOMIC_RELAXED, __HIP_MEMORY_SCOPE_AGENT);
          if (__builtin_amdgcn_ballot_w64(v < 16u) == 0ull) break;
          __builtin_amdgcn_s_sleep(2);
        }
        asm volatile("" ::: "memory");
        if (lane == 0) lds_st(lds0 + L_MISC + 28, 5u);
        TS(2)
        f32x4 cv[6];
#pragma unroll
        for (int r = 0; r < 6; ++r) {
          const u32 co = OFF_CSP + (no * 176 + c * 11) * 64 + min(r * 8 + kr, 43) * 16;
          cv[r] = __builtin_bit_cast(f32x4, __builtin_amdgcn_raw_buffer_load_b128(rsw, co, 0, 16)) +
                  __builtin_bit_cast(f32x4, __builtin_amdgcn_raw_buffer_load_b128(rsw, co + 8 * 176 * 64, 0, 16));
        }
#pragma unroll
        for (int r = 0; r < 6; ++r) {
          const f32x4 v = {sum8(cv[r][0]), sum8(cv[r][1]), sum8(cv[r][2]), sum8(cv[r][3])};
          if (no == 0) *(f32x4*)(smem + L_CS + min(r * 8 + kr, 43) * 16) = v;
        }
        wait_lgkm0();
#pragma unroll 1
        for (int sp = 0; sp < (1 << 20); ++sp) {
          u32 v = 32u;
          if (lane < 8) v = __hip_atomic_load((gu32*)(ws + OFF_CNT) + (120 + lane) * 32, __ATOMIC_RELAXED, __HIP_MEMORY_SCOPE_AGENT);
          if (__builtin_amdgcn_ballot_w64(v < 32u) == 0ull) break;
          __builtin_amdgcn_s_sleep(8);
        }
        asm volatile("" ::: "memory");
      }
      TSL(3)
      if (lds_ld(lds0 + L_MISC + 24) < 4u * (layer + 1)) {
        lds_wait_slow(lds0 + L_MISC + 24, 4u * (layer + 1));
        __builtin_amdgcn_s_sleep(X_DELAY);
      }
      grid_wait(ws, layer, b, lane);
      __builtin_amdgcn_s_setprio(XPRIO);
      TSL(4)
      u32 vo[2];
#pragma unroll
      for (int o = 0; o < 2; ++o) {
        const int seg = ((lane & 15) >> 1) ^ ((lane >> 4) | (o << 2));
        vo[o] = xin + rb * (SD * 256) + (lane >> 4) * 256 + seg * 32 + (lane & 1) * 16;
      }
      auto stage = [&](int s) {
#pragma unroll
        for (int i = 0; i < 4; ++i)
          asm volatile(
              "s_mov_b32 m0, %3\n\ts_nop 0\n\tglobal_load_lds_dwordx4 %0, %2 sc1\n\tglobal_load_lds_dwordx4 %0, %2 offset:1024 sc1\n\t"
              "global_load_lds_dwordx4 %1, %2 offset:2048 sc1\n\tglobal_load_lds_dwordx4 %1, %2 offset:3072 sc1"
              ::"v"(vo[0]), "v"(vo[1]), "s"(ws + s * 16384 + i * 4096), "s"(lds0 + (s % 3) * 16384 + i * 4096) : "memory");
      };
      stage(0); stage(1);
      static_for<0, 8>([&](auto I) {
        constexpr int i = decltype(I)::value;
        wait_vm<(i < 7 ? 16 : 0)>();
        bar();
        if constexpr (i < 6) stage(i + 2);
      });
      gu32* cs_ = (gu32*)(ws + OFF_CNT) + (layer * 48 + rb * 3) * 32;
      gu32* cw_ = (gu32*)(ws + OFF_CNT) + (layer * 48 + (rb64 >> 1) * 3) * 32;
      bar();
      bar();
      if (lane == 0) __hip_atomic_fetch_add(cs_, 1u, __ATOMIC_RELAXED, __HIP_MEMORY_SCOPE_AGENT);
      bar();
      bar();
      if (lane == 0) poll_ge(cw_, 16u);
      TSL(5)
      bar();
      bar();
      if (lane == 0) __hip_atomic_fetch_add(cs_ + 32, 1u, __ATOMIC_RELAXED, __HIP_MEMORY_SCOPE_AGENT);
      bar();
      if (lane == 0) {
        __hip_atomic_fetch_add(cs_ + 64, 1u, __ATOMIC_RELAXED, __HIP_MEMORY_SCOPE_AGENT);
        poll_ge(cw_ + 32, 16u);
      }
      bar();
      __builtin_amdgcn_s_sleep(AB_DELAY);
      if (lane == 0) poll_ge(cw_ + 64, 16u);
      TSL(6)
      bar();
      TSL(7)
      TSL_FLUSH
      if (layer == 0) bar();
    }
  } else {
    u32x4 px[8], py[8], pq[4];
#pragma unroll 1
    for (int layer = 0; layer < 2; ++layer) {
      asm volatile("" : "+s"(ws));
      const u32 xin = OFF_X + layer * X_SZ;
      lds_wait_slow(layer == 0 ? lds0 + L_MISC + 28 : lds0 + L_MISC + 36, layer == 0 ? 5u : 8u);
      TS(2)
      const u32 wo = wbase + (lw - 1) * 4096;
#pragma unroll
      for (int i = 0; i < 8; ++i) {
        if (lds_ld(lds0 + L_MISC + 48) > 4u * layer) lds_wait_slow(lds0 + L_MISC + 24, 4u * (layer + 1));
        dma4(wo, ws + i * 12288, lds0 + F_W1 + i * 12288 + (lw - 1) * 4096);
      }
      TSL(3)
      static_for<0, 8>([&](auto I) {
        constexpr int i = decltype(I)::value;
        wait_vm<(i == 0 ? 28 : 24)>();
        bar();
        if constexpr (i >= 1)
          dma4(wo, ws + (96 + (i - 1) * 12) * 1024,
               (i == 7 && lw == 3) ? lds0 + F_BASE : lds0 + F_W1 + (i - 1) * 12288 + (lw - 1) * 4096);
      });
      auto wd3 = [&](auto U, int n) {
        constexpr int u0 = decltype(U)::value;
        if (lw <= n) {
          const u32 home = lds0 + (lw == 1 ? f_wd(u0) : lw == 2 ? f_wd(u0 + 1) : f_wd(u0 + 2));
          dma4(wdo, ws + u0 * 8192, home);
          dma4(wdo, ws + u0 * 8192 + 4096, home + 4096);
        }
      };
      using std::integral_constant;
      wait_vm<16>();
      bar();
      wd3(integral_constant<int, 0>{}, 3); wd3(integral_constant<int, 3>{}, 3); wd3(integral_constant<int, 6>{}, 3);
      if (layer == 0) {
#pragma unroll
        for (int i = 0; i < 8; ++i) gload16_sc1(px[i], wdo, ws + 15 * 8192 + i * 1024);
      }
      wait_vm<24>();
      bar();
      wd3(integral_constant<int, 9>{}, 2);
      if (layer == 0) {
#pragma unroll
        for (int i = 0; i < 4; ++i) gload16_sc1(py[i], wdo, ws + 18 * 8192 + i * 1024);
      }
      bar();
      wd3(integral_constant<int, 11>{}, 2);
      if (layer == 0) {
#pragma unroll
        for (int i = 4; i < 8; ++i) gload16_sc1(py[i], wdo, ws + 18 * 8192 + i * 1024);
      }
      bar();
      wd3(integral_constant<int, 13>{}, 2);
      if (layer == 0 && lw == 3) {
#pragma unroll
        for (int i = 0; i < 4; ++i) gload16_sc1(pq[i], wdo, ws + 19 * 8192 + i * 1024);
      }
      bar();
      u32x4 rr[4];
      {
        const u32 ro = xin + ((rb64 >> 1) * SD + ct * 64 + (lw & 1) * 32 + kr) * 256 + (rb64 & 1) * 128 + no * 16;
#pragma unroll
        for (int i = 0; i < 4; ++i) rr[i] = __builtin_amdgcn_raw_buffer_load_b128(rsw, lw == 3 ? 0x7ffffff0u : ro + i * 2048, 0, 16);
      }
      wait_vm<4>();
#pragma unroll
      for (int i = 0; i < 8; ++i) asm volatile("" : "+v"(px[i]), "+v"(py[i]), "+v"(pq[i & 3]));
      bar();
      const u32 hx = lds0 + f_wd(15) + (lw - 1) * 8192 + lane * 16;
#pragma unroll
      for (int i = 0; i < 8; ++i) *(LAS u32x4*)(hx + i * 1024) = px[i];
      bar();
      const u32 hy = lds0 + (lw == 1 ? f_wd(18) : lw == 2 ? f_wd(19) : f_wd(20)) + lane * 16;
#pragma unroll
      for (int i = 0; i < 8; ++i) *(LAS u32x4*)(hy + i * 1024) = py[i];
      bar();
      if (lw == 3) {
#pragma unroll
        for (int i = 0; i < 4; ++i) *(LAS u32x4*)(lds0 + f_wd(21) + lane * 16 + i * 1024) = pq[i];
      } else {
#pragma unroll
        for (int i = 0; i < 4; ++i) *(LAS u32x4*)(lds0 + F_RES + ((lw & 1) * 32 + i * 8 + kr) * 144 + no * 16) = rr[i];
      }
      lds_bar();
      TSL(7)
      TSL_FLUSH
      if (layer == 0) bar();
    }
  }
}

__global__ __launch_bounds__(512, 2) void k_all(Args A) {
  extern __shared__ __attribute__((aligned(16))) char smem[];
  const int tid = threadIdx.x, w = __builtin_amdgcn_readfirstlane(tid >> 6), b = blockIdx.x;
  if (w == 7) {
    const char* pc;
    asm volatile("s_getpc_b64 %0" : "=s"(pc));
    const u32 line = (b >> 3) * CODE_N + (tid & 63) % CODE_N;
    asm volatile("s_mov_b32 m0, %2\n\ts_nop 0\n\tglobal_load_lds_dword %0, %1"
                 ::"v"(line * 128u), "s"(pc), "s"((u32)(uintptr_t)smem + L_DUMP) : "memory");
  }
  if (w < 4) compute_waves(A, smem, tid, w, b KIDA(w));
  else helper_waves(A, smem, tid, w - 4, b KIDA(w));
}

extern "C" void kernel_launch(void* const* d_in, const int* in_sizes, int n_in, void* d_out, int out_size, void* d_ws,
                              size_t ws_size, hipStream_t stream) {
  Args A;
  A.x = (const u16*)d_in[0];
  A.logA = (const u16*)d_in[1];
  A.Aim = (const u16*)d_in[2];
  A.Cre = (const u16*)d_in[3];
  A.Cim = (const u16*)d_in[4];
  A.logdt = (const u16*)d_in[5];
  A.dskip = (const u16*)d_in[6];
  A.wg = (const u16*)d_in[8];
  A.wu = (const u16*)d_in[9];
  A.wd = (const u16*)d_in[10];
  A.out = (u16*)d_out;
  A.ws = (char*)d_ws + ((ws_size / 2) & ~(size_t)0x1fffff);
  (void)hipFuncSetAttribute((const void*)k_all, hipFuncAttributeMaxDynamicSharedMemorySize, LDS_BYTES);
  k_all<<<256, 512, LDS_BYTES, stream>>>(A);
}
```

```cpp
#include <hip/hip_runtime.h>
#include <hip/hip_bf16.h>
#include <stdint.h>
#include <type_traits>

typedef unsigned short u16;
typedef unsigned int u32;
typedef __attribute__((ext_vector_type(8))) __bf16 bf16x8;
typedef __attribute__((ext_vector_type(2))) __bf16 bf16x2;
typedef __attribute__((ext_vector_type(8))) _Float16 f16x8;
typedef __attribute__((ext_vector_type(2))) _Float16 f16x2;
typedef __attribute__((ext_vector_type(4))) float f32x4;
typedef __attribute__((ext_vector_type(2))) float f32x2;
typedef __attribute__((ext_vector_type(4))) u32 u32x4;
typedef __attribute__((ext_vector_type(2))) u32 u32x2;
typedef __attribute__((ext_vector_type(4))) short s16x4;
#define LAS __attribute__((address_space(3)))
#define DEV __device__ __forceinline__

constexpr int SL = 2048, SD = 512, SN = 128, SF = 1376;
constexpr int NKF = 43;
constexpr int KIMG_CH = 8448;
constexpr int KIMG_C1 = 4224;
constexpr int XG_ROW = SL * 8 + 256;

constexpr u32 OFF_WGU = 0;
constexpr u32 OFF_WD = OFF_WGU + 16 * 176 * 1024;
constexpr u32 OFF_CSP = OFF_WD + 8 * NKF * 4096;
constexpr u32 OFF_X = OFF_CSP + 16 * 176 * 64;
constexpr u32 X_SZ = SL * SD * 2;
constexpr u32 OFF_H = OFF_X + 2 * X_SZ;
constexpr int H_SEP = 1;
constexpr u32 H_SZ = H_SEP * 32 * NKF * 4096;
constexpr u32 OFF_GRAN = OFF_H + 2 * 32 * NKF * 4096;
constexpr u32 GR_SZ = SL * 8 + 256;
constexpr u32 OFF_XG = OFF_GRAN + 8 * GR_SZ;
constexpr u32 OFF_PART = OFF_XG + 256 * XG_ROW;
constexpr u32 OFF_CNT = OFF_PART + SL * 128;
constexpr u32 OFF_EPOCH = OFF_CNT + 640 * 128;
constexpr u32 WS_USED = OFF_EPOCH + 256;

DEV float bflo(u32 v) { return __uint_as_float(v << 16); }
DEV float bfhi(u32 v) { return __uint_as_float(v & 0xffff0000u); }
DEV float bf2f(u16 v) { return __uint_as_float((u32)v << 16); }
DEV u32 pk_bf16(float a, float b) {
  f32x2 v = {a, b};
  return __builtin_bit_cast(u32, __builtin_convertvector(v, bf16x2));
}
DEV u32 pk_f16(float a, float b) {
  f32x2 v = {a, b};
  return __builtin_bit_cast(u32, __builtin_convertvector(v, f16x2));
}
DEV float bfr(float a) { return bflo(pk_bf16(a, a)); }
DEV float dot2bf(u32 a, u32 b, float c) {
  return __builtin_amdgcn_fdot2_f32_bf16(__builtin_bit_cast(bf16x2, a), __builtin_bit_cast(bf16x2, b), c, false);
}
DEV f32x4 mfma_bf16(u32x4 a, u32x4 b, f32x4 c) {
  return __builtin_amdgcn_mfma_f32_16x16x32_bf16(__builtin_bit_cast(bf16x8, a), __builtin_bit_cast(bf16x8, b), c, 0, 0, 0);
}
DEV f32x4 mfma_f16(u32x4 a, u32x4 b, f32x4 c) {
  return __builtin_amdgcn_mfma_f32_16x16x32_f16(__builtin_bit_cast(f16x8, a), __builtin_bit_cast(f16x8, b), c, 0, 0, 0);
}
DEV u32x2 lds_tr32(u32 a) {
  return __builtin_bit_cast(u32x2, __builtin_amdgcn_ds_read_tr16_b64_v4i16((LAS s16x4*)a));
}
DEV u32x4 lds_rd128(u32 a) { return *(const LAS u32x4*)a; }
template <int CTRL>
DEV float dpp_f(float v) {
  return __builtin_bit_cast(float, __builtin_amdgcn_update_dpp(0, __builtin_bit_cast(int, v), CTRL, 0xf, 0xf, true));
}
DEV float sum8(float v) {
  v += dpp_f<0xB1>(v);
  v += dpp_f<0x4E>(v);
  v += dpp_f<0x141>(v);
  return v;
}
DEV void ln_consts(float sum, float sumsq, float& m, float& s) {
  float mean = sum * (1.0f / SD);
  float var = fmaxf(sumsq * (1.0f / SD) - mean * mean, 0.0f);
  m = bfr(mean);
  s = bfr(__builtin_amdgcn_sqrtf(bfr(bfr(var) + 1e-5f)));
}

typedef f32x2 cpx;
DEV cpx cmul(cpx a, cpx b) {
  const f32x2 t = a * f32x2{b[0], b[0]};
  return __builtin_elementwise_fma(f32x2{a[1], a[0]}, f32x2{-b[1], b[1]}, t);
}
DEV float exp_acc(float x) {
  const float t = x * 1.44269504088896340736f;
  const float e = fmaf(x, 1.44269504088896340736f, -t) + x * 1.92596299e-8f;
  const float r = __builtin_amdgcn_exp2f(t);
  return fmaf(r, e * 0.69314718056f, r);
}

typedef __attribute__((address_space(1))) u32 gu32;
typedef __attribute__((address_space(1))) unsigned long long gu64;
template <int N>
DEV void wait_vm() { asm volatile("s_waitcnt vmcnt(%0)" ::"n"(N) : "memory"); }
DEV void wait_lgkm0() { __builtin_amdgcn_s_waitcnt(0xC07F); }
DEV void bar() {
  asm volatile("" ::: "memory");
  __builtin_amdgcn_s_barrier();
  asm volatile("" ::: "memory");
}
template <int N>
DEV void wait_lgkm() { __builtin_amdgcn_s_waitcnt(0xC07F | (N << 8)); }
DEV void lds_bar() { wait_lgkm0(); bar(); }
template <int I, int N, class Fn>
DEV void static_for(Fn&& f) {
  if constexpr (I < N) { f(std::integral_constant<int, I>{}); static_for<I + 1, N>(f); }
}
DEV int wgu_off(int c, int s, int t) { return (c * 176 + (t < 6 ? s * 6 + t : 96 + (t - 6) * 16 + s)) << 10; }
DEV int tile_of_oct(int oct) {
  return oct < 32 ? (oct >> 1) * 11 + (oct & 1)
       : oct < 96 ? ((oct - 32) >> 2) * 11 + 2 + ((oct - 32) & 3)
       : oct < 142 ? ((oct - 96) / 3) * 11 + 6 + (oct - 96) % 3 : ((oct - 142) >> 1) * 11 + 9 + ((oct - 142) & 1);
}
DEV float solo(float d) { asm volatile("" : "+v"(d)); return d; }
DEV float s_fma(float a, float b, float c) { return solo(fmaf(a, b, c)); }
DEV float s_nfma(float a, float b, float c) { return solo(fmaf(-a, b, c)); }
DEV float s_div512(float a) { return solo(a * (1.0f / SD)); }
DEV float s_addeps(float a) { return solo(a + 1e-5f); }
DEV float s_mul(float a, float b) { return solo(a * b); }

constexpr int CV_U = 2 * KIMG_CH;
constexpr int CV_UROW = (128 + 1024) * 2;
constexpr int CV_P = CV_U + 4 * CV_UROW;
constexpr int CV_PROW = 2048 * 4 + 64 * 16 + 16;
constexpr int L_MISC = CV_P + 4 * CV_PROW;
constexpr int L_CS = L_MISC + 64;
constexpr int L_DUMP = L_CS + 704;
constexpr int KG_CH = 64 * 512;
constexpr int KG_TAB = KG_CH + 53 * 512;
constexpr int KG_TROW = 672 * 2;
constexpr int KG_PACK = 65536;
constexpr int PK_IMG = 8192;
constexpr int F_W1 = 65536;
constexpr int LDS_BYTES = 163840;
constexpr int F_BASE = 57344;
static_assert(L_DUMP + 256 <= 65536 && KG_TAB + 2 * KG_TROW <= L_MISC, "lds map");
constexpr int f_tile(int t) { return F_W1 + (t - 6) * 16384; }
constexpr int f_wd(int u) {
  return u < 7 ? u * 8192 : u < 9 ? F_W1 + 81920 + (u - 7) * 8192 : u < 19 ? F_W1 + (u - 9) * 8192 : f_wd(u - 19);
}
constexpr int F_RES = f_wd(4);

DEV u32 opq(u32 v) { asm volatile("" : "+s"(v)); return v; }
DEV int fresh_lane() { return __builtin_amdgcn_mbcnt_hi(~0u, __builtin_amdgcn_mbcnt_lo(~0u, opq(0u))); }
DEV int chan_pair(int b) { return (b & 7) * 32 + (b >> 3); }
DEV u32 lds_ld(u32 a) { return *(volatile LAS u32*)opq(a); }
DEV void lds_st(u32 a, u32 v) { *(volatile LAS u32*)a = v; }
DEV u32 lds_arrive(u32 word, int lane) {
  wait_lgkm0();
  asm volatile("" ::: "memory");
  u32 old = 0;
  if (lane == 0) old = __hip_atomic_fetch_add((LAS u32*)opq(word), opq(1u), __ATOMIC_RELAXED, __HIP_MEMORY_SCOPE_WORKGROUP);
  return __builtin_amdgcn_readfirstlane(old);
}
DEV void lds_bump(u32 word) {
  asm volatile("s_mov_b64 exec, 1\n\tds_add_u32 %0, %1\n\ts_mov_b64 exec, -1" ::"v"(opq(word)), "v"(opq(1u)) : "memory");
}
DEV void lds_post(u32 word, int lane) { lds_bump(word); }
DEV void lds_wait(u32 word, u32 want) {
#pragma unroll 1
  for (int sp = 0; sp < (1 << 21); ++sp)
    if (__builtin_amdgcn_readfirstlane(lds_ld(word)) >= want) break;
  asm volatile("" ::: "memory");
}
DEV void lds_wait_slow(u32 word, u32 want) {
#pragma unroll 1
  for (int sp = 0; sp < (1 << 20); ++sp) {
    if (__builtin_amdgcn_readfirstlane(lds_ld(word)) >= want) break;
    __builtin_amdgcn_s_sleep(2);
  }
  asm volatile("" ::: "memory");
}
constexpr int XG_AUX = 16;
constexpr int GR_AUX = 16;
#define TLB_STRIDE 65536
#define TLB_N 12
constexpr int OUT_AUX = 16;
#define CODE_WARM 1
#define CODE_N 10
constexpr int FL_DELAY = 10;
constexpr int G_REP = 32;
constexpr int G_SH = 8;
constexpr int GR_DELAY2 = 6;
constexpr int X_DELAY = 8;
constexpr int GR_SLEEP = 8;
constexpr int GR_DELAY = 8;
DEV void grid_signal(char* ws, int k, int b, int lane) {
  const u32 one = opq(1u), base = OFF_CNT + (128 + k * 256) * 128;
  u32 old = 0;
  if (lane == 0)
    old = __hip_atomic_fetch_add((gu32*)(ws + opq(base + (b % G_SH) * 128)), one, __ATOMIC_RELAXED, __HIP_MEMORY_SCOPE_AGENT);
  if (__builtin_amdgcn_readfirstlane(old) == 256u / G_SH - 1u && lane < G_REP)
    __hip_atomic_fetch_add((gu32*)(ws + (base + (64 + lane) * 128)), one, __ATOMIC_RELAXED, __HIP_MEMORY_SCOPE_AGENT);
}
DEV void grid_wait(char* ws, int k, int b, int lane) {
  gu32* p = (gu32*)(ws + OFF_CNT) + (128 + k * 256 + 64 + (b % G_REP)) * 32;
#pragma unroll 1
  for (int sp = 0; sp < (1 << 20); ++sp) {
    u32 v = G_SH;
    if (lane == 0) v = __hip_atomic_load(p, __ATOMIC_RELAXED, __HIP_MEMORY_SCOPE_AGENT);
    if (__builtin_amdgcn_readfirstlane(v) >= G_SH) break;
  }
  asm volatile("" ::: "memory");
}
DEV void poll_ge(gu32* cp, u32 want) {
#pragma unroll 1
  for (int sp = 0; sp < (1 << 20); ++sp) {
    if (__hip_atomic_load(cp, __ATOMIC_RELAXED, __HIP_MEMORY_SCOPE_AGENT) >= want) break;
  }
}
DEV void gload16_sc1(u32x4& dst, u32 voff, const char* base) {
  asm volatile("global_load_dwordx4 %0, %1, %2 sc1" : "=v"(dst) : "v"(voff), "s"(base) : "memory");
}
template <int OFF>
DEV void gload16_sc1_o(u32x4& dst, u32 voff, const char* base) {
  asm volatile("global_load_dwordx4 %0, %1, %2 offset:%3 sc1" : "=v"(dst) : "v"(voff), "s"(base), "n"(OFF) : "memory");
}
DEV void dma4(u32 voff, const char* base, u32 lds) {
  asm volatile("s_mov_b32 m0, %2\n\ts_nop 0\n\tglobal_load_lds_dwordx4 %0, %1 sc1\n\t"
               "global_load_lds_dwordx4 %0, %1 offset:1024 sc1\n\tglobal_load_lds_dwordx4 %0, %1 offset:2048 sc1\n\t"
               "global_load_lds_dwordx4 %0, %1 offset:3072 sc1" ::"v"(voff), "s"(base), "s"(lds) : "memory");
}
DEV cpx cpow2(float a2, float rh, float rl, float s) {
  const float mag = __builtin_amdgcn_exp2f(a2 * s);
  const float rev = fmaf(rl, s, __builtin_amdgcn_fractf(rh * s));
  return f32x2{__builtin_amdgcn_cosf(rev), __builtin_amdgcn_sinf(rev)} * f32x2{mag, mag};
}
DEV u32 glu_tile(f32x4 acc, f32x4 cs, float m, float s, float c1) {
  const f32x2 nm = {-m, -m};
  const f32x2 gg = __builtin_elementwise_fma(nm, f32x2{cs[0], cs[1]}, f32x2{acc[0], acc[1]}) * f32x2{c1, c1};
  const f32x2 uu = __builtin_elementwise_fma(nm, f32x2{cs[2], cs[3]}, f32x2{acc[2], acc[3]});
  const f32x2 ex = {__builtin_amdgcn_exp2f(gg[0]), __builtin_amdgcn_exp2f(gg[1])};
  const f32x2 dn = __builtin_elementwise_fma(ex, f32x2{s, s}, f32x2{s, s});
  const f32x2 hh = uu * f32x2{__builtin_amdgcn_rcpf(dn[0]), __builtin_amdgcn_rcpf(dn[1])};
  return pk_bf16(hh[0], hh[1]);
}
DEV u32x4 tr4(u32 r0, u32 r1, u32 r2, u32 r3) {
  auto a = __builtin_amdgcn_permlane32_swap(r0, r2, false, false);
  auto b = __builtin_amdgcn_permlane32_swap(r1, r3, false, false);
  auto c = __builtin_amdgcn_permlane16_swap(a[0], b[0], false, false);
  auto d = __builtin_amdgcn_permlane16_swap(a[1], b[1], false, false);
  return u32x4{c[0], c[1], d[0], d[1]};
}
DEV float sum_groups(float v) {
  auto a = __builtin_amdgcn_permlane32_swap(__float_as_uint(v), __float_as_uint(v), false, false);
  v = __uint_as_float(a[0]) + __uint_as_float(a[1]);
  auto b = __builtin_amdgcn_permlane16_swap(__float_as_uint(v), __float_as_uint(v), false, false);
  return __uint_as_float(b[0]) + __uint_as_float(b[1]);
}

constexpr int F_WIN = 18;
constexpr int F_PRE = 8;
constexpr int F_RATE = 2;
constexpr int F_U1 = 6;
constexpr int F_U2 = 16;
constexpr int F_U3 = 26;

struct Args {
  const u16 *x, *logA, *Aim, *Cre, *Cim, *logdt, *dskip, *wg, *wu, *wd;
  u16* out;
  char* ws;
};

DEV u32 conv_ka(u32 lds0, int lane, int w) {
  const int h = lane >> 4, q = (lane & 15) >> 2, p = lane & 3;
  return lds0 + (w >> 1) * KIMG_CH + 2 * 16 + (q & 1) * (KIMG_C1 + 4) + 2 * (48 - 16 * h - 2 * q + 4 * p) - 16;
}
DEV void conv_mfma(u32 lds0, u32 ka, const u32x4 (&k0)[8], int lane, int w) {
  const int np = lane & 15, h = lane >> 4;
  u32 ua = lds0 + CV_U + w * CV_UROW + 256 + 2 * (8 * (np + h) - 24) - 192;
  u32 pp = lds0 + CV_P + w * CV_PROW + 16 + np * 64 + h * 16 + (np >> 1) * 16;
  asm volatile("" : "+v"(ua), "+v"(pp));
  f32x4 acc[8];
#pragma unroll
  for (int j = 0; j < 8; ++j) acc[j] = f32x4{0, 0, 0, 0};
  u32x4 kf[2][8], uf[2][8];
  constexpr auto pair_i = [](int n) { int i = 0; while (n >= 8 - i) { n -= 8 - i; ++i; } return i; };
  constexpr auto pair_j = [](int n) { int i = 0; while (n >= 8 - i) { n -= 8 - i; ++i; } return i + n; };
  auto rd1 = [&](auto D, auto N) {
    constexpr int dl = decltype(D)::value, n = decltype(N)::value;
    if constexpr (n >= 2 && n < 10) uf[dl & 1][n - 2] = lds_rd128(ua + (n - 2) * 256 + (3 - dl) * 64);
    else {
      constexpr int m = n < 2 ? n : n - 8, i = m >> 1;
      const u32x2 v = lds_tr32(ka + (4 * i + dl) * 128 + ((m & 1) ? 0 : 16));
      kf[dl & 1][i][2 * (m & 1)] = v[0]; kf[dl & 1][i][2 * (m & 1) + 1] = v[1];
    }
  };
#pragma unroll
  for (int i = 0; i < 8; ++i) kf[0][i] = k0[i];
  static_for<2, 10>([&](auto N) { rd1(std::integral_constant<int, 0>{}, N); });
  __builtin_amdgcn_sched_barrier(0);
  static_for<0, 4>([&](auto D) {
    constexpr int dl = decltype(D)::value;
    if constexpr (dl > 0) wait_lgkm0();
    if constexpr (dl == 3) lds_bump(lds0 + L_MISC + 48);
    __builtin_amdgcn_sched_barrier(0);
    static_for<0, 36>([&](auto N) {
      constexpr int n = decltype(N)::value, g0 = n < 15 ? 0 : n < 26 ? 15 : n < 33 ? 26 : 33, jh = n < 15 ? 7 : n < 26 ? 5 : n < 33 ? 3 : 1;
      constexpr int i = dl < 3 ? pair_i(n) : (n - g0) >> 1, j = dl < 3 ? pair_j(n) : jh - ((n - g0) & 1);
      acc[j] = mfma_bf16(kf[dl & 1][i], uf[dl & 1][j - i], acc[j]);
      constexpr int r0 = dl == 0 ? 4 : 0;
      if constexpr (dl < 3 && n >= r0 && n < r0 + 24)
        rd1(std::integral_constant<int, dl + 1>{}, std::integral_constant<int, n - r0>{});
      if constexpr (dl == 3 && jh < 7 && n - g0 == 1) *(LAS f32x4*)(pp + (jh + 1) * (1024 + 128)) = acc[jh + 1];
      if constexpr (dl == 3 && jh < 7 && n - g0 == (jh > 1 ? 3 : 2)) *(LAS f32x4*)(pp + (jh + 2) * (1024 + 128)) = acc[jh + 2];
      __builtin_amdgcn_sched_barrier(0);
    });
  });
  *(LAS f32x4*)pp = acc[0];
  *(LAS f32x4*)(pp + 1024 + 128) = acc[1];
}

DEV void compute_waves(const Args& A, char* smem, int tid, int w, int b) {
  char* ws = A.ws;
  int lane = tid & 63;
  const u32 lds0 = (u32)(uintptr_t)(LAS char*)smem;
  const auto rsw = __builtin_amdgcn_make_buffer_rsrc(ws, 0, (int)WS_USED, 0x00020000);
  const int d0 = 2 * chan_pair(b);
  const u32 v_dsk = *(const u32*)(A.dskip + d0);
  u32x4 gr[4], xg[4];
  bool early = false;
  auto try_early = [&]() {
    if (!early && __builtin_amdgcn_readfirstlane(lds_ld(lds0 + L_MISC + 8)) != 0u) {
      early = true;
#pragma unroll
      for (int j = 0; j < 4; ++j)
        xg[j] = __builtin_amdgcn_raw_buffer_load_b128(rsw, OFF_XG + (d0 >> 1) * XG_ROW + tid * 16 + j * 4096, 0, XG_AUX);
#pragma unroll
      for (int j = 0; j < 4; ++j)
        gr[j] = __builtin_amdgcn_raw_buffer_load_b128(rsw, OFF_GRAN + (b & 7) * GR_SZ + tid * 16 + j * 4096, 0, GR_AUX);
    }
  };
  {
    const int ch = w >> 1, n = tid & 127;
    const u32 pi = d0 * SN + tid;
    const u16 l_dt = A.logdt[d0 + ch], l_a = A.logA[pi], l_b = A.Aim[pi];
    const u16 l_cr = A.Cre[pi], l_ci = A.Cim[pi], l_cr2 = A.Cre[pi ^ 64], l_ci2 = A.Cim[pi ^ 64];
    if (tid < 16) *(u32*)(smem + L_MISC + tid * 4) = 0u;
    if (n < 8) *(u32*)(smem + KG_TAB + ch * KG_TROW + n * 4) = 0u;
    __builtin_amdgcn_sched_barrier(0);
    u32 wr[8];
#pragma unroll
    for (int r = 0; r < 8; ++r) { wr[r] = lds0 + ch * KG_CH + r * 512 + ((n * 4) ^ (r << 4)); asm volatile("" : "+v"(wr[r])); }
    const int xt = w & 1, r16 = lane & 15, g = lane >> 4;
    u32 fa[2], fb[2], fc[2];
#pragma unroll
    for (int o = 0; o < 2; ++o) {
      const int rc = min(r16, 4);
      fa[o] = lds0 + ch * KG_CH + (16 * xt + r16) * 512 + (((o * 4 + g) ^ (r16 & 7)) << 4);
      fb[o] = lds0 + ch * KG_CH + (32 + r16) * 512 + (((o * 4 + g) ^ (r16 & 7)) << 4);
      fc[o] = lds0 + ch * KG_CH + (48 + rc) * 512 + (((o * 4 + g) ^ rc) << 4);
      asm volatile("" : "+v"(fa[o]), "+v"(fb[o]), "+v"(fc[o]));
    }
    const int i0 = 16 * xt + 4 * g;
    const int ts2 = r16 < 8 ? 16 : (r16 < 12 ? 8 : 4);
    const int tb = r16 < 8 ? r16 : (r16 < 12 ? 248 + r16 : 384 + 64 * ((r16 >> 1) & 1) + (r16 & 1));
    u32 ta[4];
#pragma unroll
    for (int e = 0; e < 4; ++e) { ta[e] = lds0 + KG_TAB + ch * KG_TROW + 32 + tb * 2 + ts2 * (i0 + e); asm volatile("" : "+v"(ta[e])); }
    u32 tc = lds0 + KG_TAB + ch * KG_TROW + 32 + 2 * ((r16 < 4 || i0 == 0) ? 512 + 32 * min(r16, 4) + i0 : 648);
    asm volatile("" : "+v"(tc));
    u32 ia[10];
    {
      const float f0 = (float)(8 * tid);
#pragma unroll
      for (int j = 0; j < 5; ++j) {
        const u32 bits = pk_bf16(f0 + (float)(2 * j - 2), f0 + (float)(2 * j - 1));
        const u32 lin = lds0 + KG_TAB + (8 * tid + 2 * j - 2 + 16) * 2;
        ia[2 * j] = tid < 24 ? lin : (bits & 0xffffu) * 2 + (lds0 + KG_TAB - (0x4300 - 144) * 2);
        ia[2 * j + 1] = tid < 24 ? lin + 2 : (bits >> 16) * 2 + (lds0 + KG_TAB - (0x4300 - 144) * 2);
        asm volatile("" : "+v"(ia[2 * j]), "+v"(ia[2 * j + 1]));
      }
    }
    __builtin_amdgcn_sched_barrier(0);
    lds_bar();
    __builtin_amdgcn_sched_barrier(0);
    const u32 ea = pk_bf16(exp_acc(bf2f(l_dt)), exp_acc(bf2f(l_a)));
    const float dt = bflo(ea);
    const float a2 = -(bfhi(ea) * dt) * 1.44269504088896340736f;
    const float bi = bf2f(l_b) * dt;
    const float rh = bi * 0.15915494309189533577f;
    const float rl = fmaf(bi, 6.420638e-9f, fmaf(bi, 0.15915494309189533577f, -rh));
    u32 mx = max(max((u32)l_cr & 0x7fffu, (u32)l_ci & 0x7fffu), max((u32)l_cr2 & 0x7fffu, (u32)l_ci2 & 0x7fffu));
    mx = max(mx, (u32)__builtin_amdgcn_update_dpp(0, (int)mx, 0xB1, 0xf, 0xf, true));
    mx = max(mx, (u32)__builtin_amdgcn_update_dpp(0, (int)mx, 0x4E, 0xf, 0xf, true));
    mx = max(mx, (u32)__builtin_amdgcn_update_dpp(0, (int)mx, 0x141, 0xf, 0xf, true));
    mx = max(mx, (u32)__builtin_amdgcn_update_dpp(0, (int)mx, 0x140, 0xf, 0xf, true));
    mx = max(max((u32)__builtin_amdgcn_readlane((int)mx, 0), (u32)__builtin_amdgcn_readlane((int)mx, 16)),
             max((u32)__builtin_amdgcn_readlane((int)mx, 32), (u32)__builtin_amdgcn_readlane((int)mx, 48)));
    const u32 ex = min(max(mx >> 7, 2u), 252u) + 1u;
    const float fs = 2.0f * dt * __uint_as_float(ex << 23), isc = __uint_as_float((254u - ex) << 23);
    cpx z[12];
#pragma unroll
    for (int p = 0; p < 12; p += 3) {
      z[p] = cpow2(a2, rh, rl, (float)(1 << p));
      z[p + 1] = cmul(z[p], z[p]);
      z[p + 2] = cmul(z[p + 1], z[p + 1]);
    }
    {
      cpx V[56];
      auto put = [&](int i) { *(LAS u32*)(wr[i & 7] + (i >> 3) * 4096) = pk_f16(V[i][0], V[i][1]); };
      V[0] = cpx{1.0f, 0.0f};
      V[1] = z[3]; V[2] = z[4]; V[4] = z[5]; V[8] = z[6]; V[16] = z[7];
      V[32] = cpx{bf2f(l_cr) * isc, bf2f(l_ci) * -isc};
      constexpr int OP[47][3] = {
          {3, 1, 4},   {5, 1, 5},   {6, 2, 5},   {33, 32, 0}, {34, 32, 1}, {36, 32, 2}, {40, 32, 8}, {44, 32, 9},
          {7, 3, 5},   {9, 1, 6},   {10, 2, 6},  {11, 3, 6},  {35, 34, 0}, {37, 36, 0}, {38, 36, 1}, {41, 34, 8},
          {12, 4, 6},  {13, 5, 6},  {14, 6, 6},  {15, 7, 6},  {39, 38, 0}, {42, 36, 8}, {43, 38, 8}, {45, 36, 9},
          {17, 1, 7},  {18, 2, 7},  {19, 3, 7},  {20, 4, 7},  {46, 40, 9}, {47, 42, 9}, {48, 32, 10}, {49, 40, 10},
          {21, 5, 7},  {22, 6, 7},  {23, 7, 7},  {24, 8, 7},  {50, 44, 10}, {51, 46, 10}, {52, 32, 11}, {25, 9, 7},
          {26, 10, 7}, {27, 11, 7}, {28, 12, 7}, {29, 13, 7}, {30, 14, 7}, {31, 15, 7}, {0, 0, 0}};
      put(0); put(1); put(2); put(4); put(8); put(16); put(32);
      static_for<0, 13>([&](auto G) {
        constexpr int g4 = decltype(G)::value * 4;
        f32x2 t[4];
#pragma unroll
        for (int k = 0; k < 4; ++k)
          if (g4 + k < 46) t[k] = V[OP[g4 + k][1]] * f32x2{z[OP[g4 + k][2]][0], z[OP[g4 + k][2]][0]};
#pragma unroll
        for (int k = 0; k < 4; ++k)
          if (g4 >= 4 && g4 - 4 + k < 46) put(OP[g4 - 4 + k][0]);
#pragma unroll
        for (int k = 0; k < 4; ++k)
          if (g4 + k < 46) {
            const cpx a = V[OP[g4 + k][1]];
            const float zi = z[OP[g4 + k][2]][1];
            V[OP[g4 + k][0]] = __builtin_elementwise_fma(f32x2{a[1], a[0]}, OP[g4 + k][0] < 32 ? f32x2{-zi, zi} : f32x2{zi, -zi}, t[k]);
          }
        __builtin_amdgcn_sched_barrier(0);
      });
    }
    lds_post(lds0 + L_MISC + 56 + ch * 4, lane);
    lds_wait(lds0 + L_MISC + 56 + ch * 4, 2u);
    {
      f32x4 acc0 = {0, 0, 0, 0}, acc1 = {0, 0, 0, 0};
#pragma unroll
      for (int s = 0; s < 8; ++s) {
        const u32x4 a = lds_rd128(fa[s & 1] + (s >> 1) * 128);
        acc0 = mfma_f16(a, lds_rd128(fb[s & 1] + (s >> 1) * 128), acc0);
        acc1 = mfma_f16(a, lds_rd128(fc[s & 1] + (s >> 1) * 128), acc1);
      }
      try_early();
      acc0 *= fs; acc1 *= fs;
      const u32 v01 = pk_bf16(acc0[0], acc0[1]), v23 = pk_bf16(acc0[2], acc0[3]);
      *(LAS u16*)ta[0] = (u16)v01; *(LAS u16*)ta[1] = (u16)(v01 >> 16);
      *(LAS u16*)ta[2] = (u16)v23; *(LAS u16*)ta[3] = (u16)(v23 >> 16);
      *(LAS u32x2*)tc = u32x2{pk_bf16(acc1[0], acc1[1]), pk_bf16(acc1[2], acc1[3])};
    }
    lds_post(lds0 + L_MISC + 16, lane);
    lds_wait(lds0 + L_MISC + 16, 4u);
    try_early();
    u32 kv[2][5];
#pragma unroll
    for (int cc = 0; cc < 2; ++cc)
#pragma unroll
      for (int j = 0; j < 5; ++j)
        kv[cc][j] = (u32)*(const LAS u16*)(ia[2 * j] + cc * KG_TROW) | ((u32)*(const LAS u16*)(ia[2 * j + 1] + cc * KG_TROW) << 16);
#pragma unroll
    for (int cc = 0; cc < 2; ++cc) {
      char* o0 = smem + cc * KIMG_CH + 32 + tid * 16;
      *(u32x4*)o0 = u32x4{kv[cc][1], kv[cc][2], kv[cc][3], kv[cc][4]};
      *(u32x4*)(o0 + KIMG_C1) = u32x4{kv[cc][0], kv[cc][1], kv[cc][2], kv[cc][3]};
    }
    if (tid < 8) *(u32x4*)(smem + (tid >> 2) * KIMG_CH + ((tid >> 1) & 1) * KIMG_C1 + (tid & 1) * 16) = u32x4{0, 0, 0, 0};
    lds_post(lds0 + L_MISC + 12, lane);
  }
  const u32 l_dsk = __builtin_amdgcn_readfirstlane(v_dsk);
  u32 tag = 0;

#pragma unroll 1
  for (int layer = 0; layer < 2; ++layer) {
    {
      lane = fresh_lane();
      tid = w * 64 + lane;
      const u32 goff = OFF_GRAN + (b & 7) * GR_SZ + tid * 16, xoff = OFF_XG + (d0 >> 1) * XG_ROW + tid * (layer ? 64 : 16);
      u32 out_state = 0;
#pragma unroll 1
      for (int sp = 0; sp < (1 << 20); ++sp) {
        const u32x4 mw = *(volatile LAS u32x4*)opq(lds0 + L_MISC);
        tag = __builtin_amdgcn_readfirstlane(mw[1]);
        out_state = __builtin_amdgcn_readfirstlane(mw[2]);
        if (__builtin_amdgcn_readfirstlane(mw[0]) != 0u && __builtin_amdgcn_readfirstlane(mw[3]) >= 4u + layer) break;
        __builtin_amdgcn_s_sleep(1);
      }
      asm volatile("" ::: "memory");
      u32 ka = conv_ka(lds0, lane, w);
      asm volatile("" : "+v"(ka));
      u32x4 k0[8];
#pragma unroll
      for (int i = 0; i < 8; ++i) {
        const u32x2 lo = lds_tr32(ka + i * 512 + 16), hi = lds_tr32(ka + i * 512);
        k0[i] = u32x4{lo[0], lo[1], hi[0], hi[1]};
      }
      __builtin_amdgcn_sched_barrier(0);
      const u32 ltag = tag + layer;
      auto bad4 = [&](const u32x4 (&v)[4]) {
        u32 lo = min(v[0][1], v[0][3]), hi = max(v[0][1], v[0][3]);
#pragma unroll
        for (int j = 1; j < 4; ++j) { lo = min(min(lo, v[j][1]), v[j][3]); hi = max(max(hi, v[j][1]), v[j][3]); }
        u32 bad = (lo ^ ltag) | (hi ^ ltag);
        asm volatile("" : "+v"(bad));
        return __builtin_amdgcn_ballot_w64(bad != 0u) != 0ull;
      };
      bool needx = true, needg = true;
      if (early) { needx = bad4(xg); needg = bad4(gr); }
      else if (out_state < 1u + layer) lds_wait_slow(lds0 + L_MISC + 8, 1u + layer);
      early = false;
#pragma unroll 1
      for (int sp = 0; sp < (1 << 18) && (needx || needg); ++sp) {
        if (needx) {
#pragma unroll
          for (int j = 0; j < 4; ++j) xg[j] = __builtin_amdgcn_raw_buffer_load_b128(rsw, xoff + j * (layer ? 16 : 4096), 0, XG_AUX);
        }
        if (needg) {
          if (layer) __builtin_amdgcn_s_sleep(GR_DELAY2);
#pragma unroll
          for (int j = 0; j < 4; ++j) gr[j] = __builtin_amdgcn_raw_buffer_load_b128(rsw, goff + j * 4096, 0, GR_AUX);
        }
        needx = bad4(xg); needg = bad4(gr);
        if (needx || needg) __builtin_amdgcn_s_sleep(GR_SLEEP);
      }
      lds_bump(lds0 + L_MISC + 36);
      float base[2][8];
#pragma unroll
      for (int r = 0; r < 2; ++r) {
        u32 up[2][2];
#pragma unroll
        for (int j = 0; j < 2; ++j) {
          const u32 g0 = gr[2 * j][2 * r], g1 = gr[2 * j + 1][2 * r], x0 = xg[2 * j][2 * r], x1 = xg[2 * j + 1][2 * r];
          const f32x2 rs = {__builtin_amdgcn_rcpf(bfhi(g0)), __builtin_amdgcn_rcpf(bfhi(g1))};
          const f32x2 mm = {bflo(g0), bflo(g1)};
#pragma unroll
          for (int cc = 0; cc < 2; ++cc) {
            const f32x2 xx = cc ? f32x2{bfhi(x0), bfhi(x1)} : f32x2{bflo(x0), bflo(x1)};
            const float dk = cc ? bfhi(l_dsk) : bflo(l_dsk);
            const f32x2 u = (xx - mm) * rs;
            up[cc][j] = pk_bf16(u[0], u[1]);
            const f32x2 bs = __builtin_elementwise_fma(u, f32x2{dk, dk}, xx);
            base[cc][4 * j + r] = bs[0]; base[cc][4 * j + r + 2] = bs[1];
          }
        }
#pragma unroll
        for (int cc = 0; cc < 2; ++cc)
          *(u32x2*)(smem + CV_U + (cc * 2 + r) * CV_UROW + 256 + tid * 8) = u32x2{up[cc][0], up[cc][1]};
      }
      {
        const u32 zz = opq(0u);
        *(u32*)(smem + CV_U + w * CV_UROW + lane * 4) = zz;
        *(u32*)(smem + CV_P + w * CV_PROW + 12) = zz;
      }
      lds_post(lds0 + L_MISC + 32, lane);
      lds_wait(lds0 + L_MISC + 32, 4u * (layer + 1));
      conv_mfma(lds0, ka, k0, lane, w);
      lds_post(lds0 + L_MISC + 20, lane);
      lds_wait(lds0 + L_MISC + 20, 4u * (layer + 1));
      const int off = CV_P + 16 + tid * 32 + (tid >> 2) * 16;
      const int offm = off + CV_PROW - 4 - (((tid & 3) == 0 && tid > 0) ? (int)opq(16u) : 0);
      const u32 xo = OFF_X + layer * X_SZ + ((tid >> 4) * SD + d0) * 256 + (tid & 15) * 16;
#pragma unroll
      for (int cc = 0; cc < 2; ++cc) {
        const char* p0 = smem + off + (2 * cc) * CV_PROW;
        const f32x4 a0 = *(const f32x4*)p0, a1 = *(const f32x4*)(p0 + 16);
        const f32x4 b0 = *(const f32x4*)(p0 + CV_PROW), b1 = *(const f32x4*)(p0 + CV_PROW + 16);
        const float bm = *(const float*)(smem + offm + (2 * cc) * CV_PROW);
        const float o[8] = {base[cc][0] + a0[0] + bm,    base[cc][1] + a0[1] + b0[0], base[cc][2] + a0[2] + b0[1],
                            base[cc][3] + a0[3] + b0[2], base[cc][4] + a1[0] + b0[3], base[cc][5] + a1[1] + b1[0],
                            base[cc][6] + a1[2] + b1[1], base[cc][7] + a1[3] + b1[2]};
        const u32x4 ov = {pk_bf16(o[0], o[1]), pk_bf16(o[2], o[3]), pk_bf16(o[4], o[5]), pk_bf16(o[6], o[7])};
        __builtin_amdgcn_raw_buffer_store_b128(ov, rsw, xo + cc * 256, 0, 16);
      }
      wait_vm<0>();
      if (lds_arrive(lds0 + L_MISC + 24, lane) == 4u * layer + 3u) grid_signal(ws, layer, b, lane);
    }
    lane = fresh_lane();
    asm volatile("" : "+s"(ws));
    const int bq = opq(b), mq = opq(w);
    const int xcd = bq & 7, idx = bq >> 3;
    const int rb = (xcd & 1) * 8 + (idx & 7), c = (xcd >> 1) * 4 + (idx >> 3);
    const int rb64 = xcd * 4 + (idx & 3), ct = idx >> 2;
    int col = lane & 15, g = lane >> 4, q = (lane & 15) >> 2, p = lane & 3;
    const u32 hoff = OFF_H + layer * H_SZ;
    const int key = q | ((g & 1) << 2);
    u32 xa[2], wa[3];
#pragma unroll
    for (int j = 0; j < 2; ++j) {
      xa[j] = lds0 + (8 * g + q) * 256 + (((2 * mq + j) ^ key) * 32) + 8 * p;
      asm volatile("" : "+v"(xa[j]));
    }
#pragma unroll
    for (int r = 0; r < 3; ++r) {
      wa[r] = lds0 + r * F_BASE + lane * 16;
      asm volatile("" : "+v"(wa[r]));
    }
    auto lw128 = [&](auto O) { constexpr int o = decltype(O)::value; return lds_rd128(wa[o / F_BASE] + o % F_BASE); };
    f32x4 acc[11][2];
#pragma unroll
    for (int t = 0; t < 11; ++t)
#pragma unroll
      for (int j = 0; j < 2; ++j) acc[t][j] = f32x4{0, 0, 0, 0};
    f32x4 s1[2] = {{0, 0, 0, 0}, {0, 0, 0, 0}}, s2[2] = {{0, 0, 0, 0}, {0, 0, 0, 0}};
    const u32 one2 = opq(0x3f803f80u);
    const u32x4 ones = {one2, one2, one2, one2};
    u32x4 xf[16][2], wf[2][6];
    auto rd = [&](auto K) {
      constexpr int k = decltype(K)::value, o = ((k >> 1) % 3) * 16384 + (k & 1) * 8192;
#pragma unroll
      for (int j = 0; j < 2; ++j) {
        const u32x2 lo = lds_tr32(xa[j] + o), hi = lds_tr32(xa[j] + o + 1024);
        xf[k][j] = u32x4{lo[0], lo[1], hi[0], hi[1]};
      }
      static_for<0, 6>([&](auto T) {
        constexpr int t = decltype(T)::value;
        wf[k & 1][t] = lw128(std::integral_constant<int, F_W1 + (k * 6 + t) * 1024>{});
      });
    };
    auto mm = [&](auto K) {
      constexpr int k = decltype(K)::value;
#pragma unroll
      for (int j = 0; j < 2; ++j) {
        s2[j] = mfma_bf16(xf[k][j], xf[k][j], s2[j]);
        s1[j] = mfma_bf16(ones, xf[k][j], s1[j]);
      }
#pragma unroll
      for (int t = 0; t < 6; ++t)
#pragma unroll
        for (int j = 0; j < 2; ++j) acc[t][j] = mfma_bf16(wf[k & 1][t], xf[k][j], acc[t][j]);
    };
    auto weave = [&]() {
#pragma unroll
      for (int k = 0; k < 10; ++k) {
        __builtin_amdgcn_sched_group_barrier(0x008, 1, 0);
        __builtin_amdgcn_sched_group_barrier(0x100, 1, 0);
      }
      __builtin_amdgcn_sched_group_barrier(0x008, 6, 0);
      __builtin_amdgcn_sched_barrier(0);
    };
    static_for<0, 8>([&](auto I) {
      constexpr int i = decltype(I)::value;
      wait_lgkm0();
      __builtin_amdgcn_sched_barrier(0);
      bar();
      rd(std::integral_constant<int, 2 * i>{});
      if constexpr (i > 0) mm(std::integral_constant<int, 2 * i - 1>{});
      weave();
      rd(std::integral_constant<int, 2 * i + 1>{});
      mm(std::integral_constant<int, 2 * i>{});
      weave();
    });
    wait_lgkm0();
    __builtin_amdgcn_sched_barrier(0);
    bar();
#pragma unroll
    for (int j = 0; j < 2; ++j) {
      s2[j] = mfma_bf16(xf[15][j], xf[15][j], s2[j]);
      s1[j] = mfma_bf16(ones, xf[15][j], s1[j]);
    }
    lane = fresh_lane();
    col = lane & 15; g = lane >> 4; q = (lane & 15) >> 2; p = lane & 3;
    const u32 csa = lds0 + L_CS + g * 16;
    f32x4 cs6[6];
    __builtin_amdgcn_sched_barrier(0);
    u32 so[6];
    float lm[2], ls[2], lc[2], dv[2];
    u32 pk0, pk1;
    static_for<0, 12>([&](auto N) {
      constexpr int n = decltype(N)::value;
      acc[n >> 1][n & 1] = mfma_bf16(wf[1][n >> 1], xf[15][n & 1], acc[n >> 1][n & 1]);
      if constexpr (n >= 6) cs6[n - 6] = __builtin_bit_cast(f32x4, lds_rd128(csa + (n - 6) * 64));
      if constexpr (n < 3) {
        const int lt = rb * 8 + 2 * mq + (g & 1);
        const u32 hb = hoff + lt * (NKF * 1024) + col * 16;
#pragma unroll
        for (int i = 2 * n; i < 2 * n + 2; ++i) {
          const int o0 = i == 0 ? c * 2 : i < 3 ? 32 + c * 4 + 2 * (i - 1) : i < 5 ? 96 + c * 3 + 2 * (i - 3) : 142 + c * 2, o1 = o0 + 1;
          const u32 f0 = (i < 4 || c < 15) ? o0 * 256 : 0x70000000u;
          const u32 f1 = (i < 3 || (i != 4 && c < 15)) ? o1 * 256 : 0x70000000u;
          so[i] = hb + f0 + (g >> 1) * (f1 - f0);
        }
      }
      if constexpr (n == 3 || n == 4) {
        constexpr int j = n - 3;
        const float e0 = s2[j][0], e1 = s2[j][1], e2 = s2[j][2], e3 = s2[j][3];
        const float d01 = (lane & 1) ? e1 : e0, d23 = (lane & 1) ? e3 : e2, dd = (lane & 2) ? d23 : d01;
        dv[j] = (g == q) ? dd : 0.f;
      }
      if constexpr (n == 5 || n == 6) dv[n - 5] = sum_groups(dv[n - 5]);
      if constexpr (n == 7) {
        const float m0 = s_div512(s1[0][0]), m1 = s_div512(s1[1][0]);
        pk0 = pk_bf16(m0, m1);
        dv[0] = s_nfma(m0, m0, s_div512(dv[0]));
        dv[1] = s_nfma(m1, m1, s_div512(dv[1]));
      }
      if constexpr (n == 8) { pk1 = pk_bf16(fmaxf(dv[0], 0.f), fmaxf(dv[1], 0.f)); lm[0] = bflo(pk0); lm[1] = bfhi(pk0); }
      if constexpr (n == 9) pk1 = pk_bf16(s_addeps(bflo(pk1)), s_addeps(bfhi(pk1)));
      if constexpr (n == 10) pk1 = pk_bf16(__builtin_amdgcn_sqrtf(bflo(pk1)), __builtin_amdgcn_sqrtf(bfhi(pk1)));
      if constexpr (n == 11) {
        ls[0] = bflo(pk1); ls[1] = bfhi(pk1);
        lc[0] = __builtin_amdgcn_rcpf(ls[0]); lc[1] = __builtin_amdgcn_rcpf(ls[1]);
      }
      __builtin_amdgcn_sched_barrier(0);
    });
    lc[0] *= -1.44269504088896340736f; lc[1] *= -1.44269504088896340736f;
    u32x4 wv[2][16];
    static_for<0, 4>([&](auto K) { wv[0][decltype(K)::value] = lw128(std::integral_constant<int, f_tile(6) + decltype(K)::value * 1024>{}); });
    u32 hv[11][2];
    auto hstore = [&](int t) {
      const int t1 = t == 8 ? 8 : t + 1;
      __builtin_amdgcn_raw_buffer_store_b128(tr4(hv[t][0], hv[t][1], hv[t1][0], hv[t1][1]), rsw, so[(t + 1) >> 1], 0, 16);
    };
    auto epi = [&](int t) {
      const f32x4 cs = __builtin_bit_cast(f32x4, lds_rd128(csa + t * 64));
#pragma unroll
      for (int j = 0; j < 2; ++j) hv[t][j] = glu_tile(acc[t][j], cs, lm[j], ls[j], lc[j]);
    };
#pragma unroll
    for (int t = 0; t < 6; ++t) {
#pragma unroll
      for (int j = 0; j < 2; ++j) hv[t][j] = glu_tile(acc[t][j], cs6[t], lm[j], ls[j], lc[j]);
      if (t & 1) hstore(t - 1);
      if (t == 1) __builtin_amdgcn_sched_barrier(0);
    }
    static_for<4, 8>([&](auto K) { wv[0][decltype(K)::value] = lw128(std::integral_constant<int, f_tile(6) + decltype(K)::value * 1024>{}); });
    __builtin_amdgcn_sched_barrier(0);
    constexpr int PRE = F_PRE, WIN = F_WIN;
    const u32 ho = hoff + (rb64 * 4 + mq) * (NKF * 1024) + lane * 16;
    auto hload = [&](auto V, u32x4& dst) {
      constexpr int v = decltype(V)::value;
      gload16_sc1_o<(v & 7) * 1024 - 4096>(dst, ho, ws + (v >> 3) * 8192 + 4096);
    };
    u32x4 hf[NKF];
    static_for<6, 11>([&](auto T) {
      constexpr int t = decltype(T)::value, e = t - 1, wb = t & 1;
      f32x4 cv;
      float ga[4], ua[4];
      static_for<0, 32>([&](auto S) {
        constexpr int sg = decltype(S)::value, k = sg >> 1, j = sg & 1;
        if constexpr ((sg & 7) == 0) {
          wait_lgkm<(t == 10 && sg == 24) ? 0 : 4>();
          __builtin_amdgcn_sched_barrier(0);
        }
        acc[t][j] = mfma_bf16(wv[wb][k], xf[k][j], acc[t][j]);
        if constexpr (t > 6 && sg == 0) cv = __builtin_bit_cast(f32x4, lds_rd128(csa + e * 64));
        if constexpr ((sg & 7) < 4) {
          constexpr int kk = 8 + (sg >> 3) * 4 + (sg & 3);
          if constexpr (kk < 16) wv[wb][kk] = lw128(std::integral_constant<int, f_tile(t) + kk * 1024>{});
          else if constexpr (t < 10) wv[wb ^ 1][kk - 16] = lw128(std::integral_constant<int, f_tile(t + 1) + (kk - 16) * 1024>{});
        }
        if constexpr (t > 6) {
          constexpr int st = sg >> 2, a = sg & 3, jj = a >> 1, ee = a & 1;
          if constexpr (st == 1) { ga[a] = s_nfma(lm[jj], cv[ee], acc[e][jj][ee]); ua[a] = s_nfma(lm[jj], cv[2 + ee], acc[e][jj][2 + ee]); }
          if constexpr (st == 2) ga[a] = s_mul(ga[a], lc[jj]);
          if constexpr (st == 3) ga[a] = __builtin_amdgcn_exp2f(ga[a]);
          if constexpr (st == 4) ga[a] = s_fma(ga[a], ls[jj], ls[jj]);
          if constexpr (st == 5) ga[a] = __builtin_amdgcn_rcpf(ga[a]);
          if constexpr (st == 6) ua[a] = s_mul(ua[a], ga[a]);
        }
        if constexpr (t > 6 && sg == 28) { hv[e][0] = pk_bf16(ua[0], ua[1]); hv[e][1] = pk_bf16(ua[2], ua[3]); }
        if constexpr (sg == 30 && (t == 8 || t == 9)) hstore(2 * t - 10);
        if constexpr (t == 10 && sg >= 4 && sg < 4 + 2 * PRE && (sg & 1) == 0)
          hload(std::integral_constant<int, (sg >= 4 ? (sg - 4) >> 1 : 0)>{}, hf[(sg >= 4 ? sg - 4 : 0) >> 1]);
        __builtin_amdgcn_sched_barrier(0);
      });
      if constexpr (t == 10) { epi(10); hstore(9); __builtin_amdgcn_sched_barrier(0); }
      if constexpr (t == 6) wait_vm<2>();
      if constexpr (t == 7) wait_vm<0>();
      if constexpr (t == 10) wait_vm<PRE + 1>();
      if constexpr (t == 10) wait_lgkm0();
      bar();
      __builtin_amdgcn_sched_barrier(0);
    });
    f32x4 ac2[4] = {{0, 0, 0, 0}, {0, 0, 0, 0}, {0, 0, 0, 0}, {0, 0, 0, 0}};
    u32x4 wf2[3][4];
    auto rd2 = [&](auto U) {
      constexpr int u = decltype(U)::value;
      static_for<0, 4>([&](auto I) {
        constexpr int i = decltype(I)::value;
        wf2[u][i] = lw128(std::integral_constant<int, f_wd(u >> 1) + (u & 1) * 4096 + i * 1024>{});
      });
    };
    rd2(std::integral_constant<int, 0>{});
    rd2(std::integral_constant<int, 1>{});
    __builtin_amdgcn_sched_barrier(0);
    constexpr auto issued = [](int u) {
      int n = PRE;
      for (int v = 1; v <= u; ++v) {
        int lim = v + WIN;
        if (v <= F_U2 && lim > 24) lim = 24;
        if (v <= F_U3 && lim > 35) lim = 35;
        if (lim > NKF) lim = NKF;
        const int rate = v > F_U3 ? 8 : F_RATE;
        n = n + rate < lim ? n + rate : (lim > n ? lim : n);
      }
      return n;
    };
    static_for<0, NKF>([&](auto U) {
      constexpr int u = decltype(U)::value;
      static_assert(PRE <= 8 && issued(u) > u && F_U1 >= 6 && F_U2 >= 12 && F_U2 < 25 && F_U3 < 29, "h window / W_down residency");
      asm volatile("s_waitcnt vmcnt(%1)" : "+v"(hf[u]) : "n"(issued(u) - 1 - u + (u < PRE ? 1 : 0)) : "memory");
      wait_lgkm<(u + 1 < NKF ? 4 : 0)>();
      if constexpr (u == F_U1) { wait_vm<issued(u) - PRE>(); bar(); }
      if constexpr (u == F_U2) bar();
      if constexpr (u == F_U3) { bar(); }
      __builtin_amdgcn_sched_barrier(0);
      static_for<0, 4>([&](auto I) {
        constexpr int i = decltype(I)::value;
        ac2[i] = mfma_bf16(wf2[u % 3][i], hf[u], ac2[i]);
        if constexpr (u + 2 < NKF)
          wf2[(u + 2) % 3][i] = lw128(std::integral_constant<int, f_wd((u + 2) >> 1) + (u & 1) * 4096 + i * 1024>{});
        static_for<issued(u), issued(u + 1)>([&](auto V) {
          constexpr int cnt = issued(u + 1) - issued(u), ix = decltype(V)::value - issued(u);
          if constexpr (ix * 4 / (cnt > 4 ? cnt : 4) == i) hload(V, hf[decltype(V)::value]);
        });
        __builtin_amdgcn_sched_barrier(0);
      });
    });
    lane = fresh_lane();
    {
    const int col = lane & 15, g = lane >> 4, q = (lane & 15) >> 2, p = lane & 3;
    const int l = rb64 * 64 + mq * 16 + col;
    u32x2 ov[4];
#pragma unroll
    for (int i = 0; i < 4; ++i) {
      const u32x2 rv = lds_tr32(lds0 + F_RES + (i * 16 + 4 * g + q) * 144 + mq * 32 + p * 8);
      const u32 o0 = pk_bf16(ac2[i][0], ac2[i][1]), o1 = pk_bf16(ac2[i][2], ac2[i][3]);
      ov[i] = u32x2{pk_bf16(bflo(rv[0]) + bflo(o0), bfhi(rv[0]) + bfhi(o0)),
                    pk_bf16(bflo(rv[1]) + bflo(o1), bfhi(rv[1]) + bfhi(o1))};
    }
    if (layer == 0) {
      lds_bar();
      float sa = 0.f, sq = 0.f;
#pragma unroll
      for (int i = 0; i < 4; ++i)
#pragma unroll
        for (int e = 0; e < 2; ++e) { sa = dot2bf(ov[i][e], 0x3f803f80u, sa); sq = dot2bf(ov[i][e], ov[i][e], sq); }
      sa = sum_groups(sa); sq = sum_groups(sq);
      __builtin_amdgcn_raw_buffer_store_b128(u32x4{__float_as_uint(sa), tag, __float_as_uint(sq), tag}, rsw,
                                             OFF_PART + (ct * SL + l) * 16 + g * 0x10000000u, 0, 16);
#pragma unroll
      for (int i = 0; i < 4; ++i) {
        const u32 give = (col & 1) ? ov[i][0] : ov[i][1];
        const u32 got = (u32)__builtin_amdgcn_update_dpp(0, (int)give, 0xB1, 0xf, 0xf, true);
        const u32x4 gv = {(col & 1) ? got : ov[i][0], tag + 1u, (col & 1) ? ov[i][1] : got, tag + 1u};
        const u32 go = OFF_XG + (ct * 32 + i * 8 + g * 2 + (col & 1)) * XG_ROW + (l & ~1) * 8;
        __builtin_amdgcn_raw_buffer_store_b128(gv, rsw, go, 0, 16);
      }
    } else {
      const u32 ob = lds0 + f_wd(9) + mq * 2304;
#pragma unroll
      for (int i = 0; i < 4; ++i) *(LAS u32x2*)(ob + col * 144 + i * 32 + g * 8) = ov[i];
      asm volatile("" ::: "memory");
      const u32x4 s0 = lds_rd128(ob + (lane >> 3) * 144 + (lane & 7) * 16), s1 = lds_rd128(ob + ((lane >> 3) + 8) * 144 + (lane & 7) * 16);
      const auto rso = __builtin_amdgcn_make_buffer_rsrc(A.out, 0, SL * SD * 2, 0x00020000);
      const u32 oo = ((rb64 * 64 + mq * 16 + (lane >> 3)) * SD + ct * 64) * 2 + (lane & 7) * 16;
      __builtin_amdgcn_raw_buffer_store_b128(s0, rso, oo, 0, OUT_AUX);
      __builtin_amdgcn_raw_buffer_store_b128(s1, rso, oo + 8 * SD * 2, 0, OUT_AUX);
    }
    }
#pragma unroll
    for (int j = 0; j < 4; ++j) asm volatile("" : "=v"(gr[j]), "=v"(xg[j]));
  }
  if (b == 0 && w == 0 && fresh_lane() == 0) __hip_atomic_store((gu32*)(ws + OFF_EPOCH), tag + 1u, __ATOMIC_RELAXED, __HIP_MEMORY_SCOPE_AGENT);
}

DEV void publish_ln(char* ws, u32 off, float sum, float sumsq, u32 tag, int b, int lane) {
  float m, sd;
  ln_consts(sum8(sum), sum8(sumsq), m, sd);
  const u32 val = (__float_as_uint(m) >> 16) | (__float_as_uint(sd) & 0xffff0000u);
  __hip_atomic_store((gu64*)(ws + off + (lane & 7) * GR_SZ) + (lane >> 4) * 512 + 2 * b + ((lane >> 3) & 1),
                     ((unsigned long long)tag << 32) | val, __ATOMIC_RELAXED, __HIP_MEMORY_SCOPE_AGENT);
}

DEV void helper_waves(const Args& A, char* smem, int tid, int lw, int b) {
  char* ws = A.ws;
  const int lane = tid & 63;
  const u32 lds0 = (u32)(uintptr_t)(LAS char*)smem;
  const auto rsw = __builtin_amdgcn_make_buffer_rsrc(ws, 0, (int)WS_USED, 0x00020000);
  const int kr = lane >> 3, no = lane & 7;
  u32 tag;
  {
    char* img = smem + KG_PACK + (lw - 1) * PK_IMG;
    if (lw <= 1) {
      tag = __builtin_amdgcn_raw_buffer_load_b32(rsw, lw == 1 ? OFF_EPOCH : 0x7ffffff0u, 0, 16);
      __builtin_amdgcn_sched_barrier(0);
      const int dg = (b >> 3) & 7, lb = (b & 7) * 4 + (b >> 6);
      const u16* src = lw == 0 ? A.x + (size_t)(8 * b + kr) * SD + no * 8
                               : A.x + (size_t)(64 * lb + 8 * no) * SD + dg * 64 + kr * 8;
      const int stp = lw == 0 ? 64 : SD;
      u32x4 v[8];
#pragma unroll
      for (int i = 0; i < 8; ++i) v[i] = *(const u32x4*)(src + i * stp);
      if (lw == 0 && b < 10) __builtin_amdgcn_raw_buffer_store_b32(0u, rsw, OFF_CNT + (b * 64 + lane) * 128, 0, 16);
      __builtin_amdgcn_sched_barrier(0);
      bar();
      __builtin_amdgcn_sched_barrier(0);
      if (lw == 1) {
        tag += 1u;
        if (lane == 0) { lds_st(lds0 + L_MISC + 4, tag); lds_st(lds0 + L_MISC, 1u); }
        const u32 go = OFF_XG + (dg * 32 + kr * 4) * XG_ROW + lb * 128 + no * 16;
#pragma unroll
        for (int i = 0; i < 4; ++i)
#pragma unroll
          for (int e = 0; e < 4; ++e)
            __builtin_amdgcn_raw_buffer_store_b128(u32x4{v[2 * i][e], tag, v[2 * i + 1][e], tag}, rsw,
                                                   go + e * XG_ROW + i * 4096, 0, 16);
        wait_vm<0>();
        if (lane == 0) lds_st(lds0 + L_MISC + 8, 1u);
      } else {
        float a[2] = {0.f, 0.f}, q[2] = {0.f, 0.f};
#pragma unroll
        for (int i = 0; i < 8; ++i)
#pragma unroll
          for (int j = 0; j < 4; ++j) {
            a[j & 1] = dot2bf(v[i][j], 0x3f803f80u, a[j & 1]);
            q[j & 1] = dot2bf(v[i][j], v[i][j], q[j & 1]);
          }
        lds_wait(lds0 + L_MISC, 1u);
        tag = lds_ld(lds0 + L_MISC + 4);
        wait_vm<0>();
        publish_ln(ws, OFF_GRAN, a[0] + a[1], q[0] + q[1], tag, b, lane);
      }
    } else {
      if (lw == 2) {
        const u32 to = min((u32)(((b >> 3) * TLB_N + (lane % TLB_N)) * TLB_STRIDE + TLB_STRIDE - 256), WS_USED - 4);
        asm volatile("s_mov_b32 m0, %4\n\ts_nop 0\n\tglobal_load_lds_dword %0, %1 sc1\n\tglobal_load_lds_dword %2, %3 sc1"
                     ::"v"(to), "s"(ws), "v"((u32)((b >> 3) * 65536 + 32768)), "s"(A.out), "s"(lds0 + L_DUMP) : "memory");
      }
      bar();
    }
    const int id = (3 - lw) * 256 + b;
    const int col = lane & 15, g = lane >> 4, q = (lane & 15) >> 2, p = lane & 3;
    const u32 ra = (u32)(uintptr_t)(LAS char*)img + (8 * g + q) * 256 + 8 * p;
    const int rkey = q | ((g & 1) << 2);
    u32x4 fr[9];
    u32 so9[9];
#pragma unroll
    for (int i = 0; i < 9; ++i) so9[i] = 0x7ffffff0u;
    if (id < 352) {
      const int ps = id & 15, ng = id >> 4;
      const size_t so = (size_t)(32 * ps + kr) * SF + 8 * (8 * ng + no < 172 ? 8 * ng + no : 0);
      u32x4 gv[4], uv[4];
#pragma unroll
      for (int i = 0; i < 4; ++i) {
        gv[i] = *(const u32x4*)(A.wg + so + (size_t)i * 8 * SF);
        uv[i] = *(const u32x4*)(A.wu + so + (size_t)i * 8 * SF);
      }
#pragma unroll
      for (int i = 0; i < 4; ++i) {
        char* dst = img + (8 * i + kr) * 256 + ((no ^ ((kr & 3) | ((i & 1) << 2))) * 32);
        *(u32x4*)dst = u32x4{gv[i][0], uv[i][0], gv[i][1], uv[i][1]};
        *(u32x4*)(dst + 16) = u32x4{gv[i][2], uv[i][2], gv[i][3], uv[i][3]};
      }
      wait_lgkm0();
      asm volatile("" ::: "memory");
      f32x4 cs = {0, 0, 0, 0};
#pragma unroll
      for (int t8 = 0; t8 < 8; ++t8) {
        const u32x2 lo = lds_tr32(ra + ((t8 ^ rkey) * 32)), hi = lds_tr32(ra + ((t8 ^ rkey) * 32) + 1024);
        fr[t8] = u32x4{lo[0], lo[1], hi[0], hi[1]};
        const int Tt = 8 * ng + t8;
        so9[t8] = lane * 16 + OFF_WGU + wgu_off(Tt / 11, ps, Tt % 11);
        const u32 sel = col == t8 ? 0x3f803f80u : 0u;
        cs = mfma_bf16(fr[t8], u32x4{sel, sel, sel, sel}, cs);
      }
      fr[8] = __builtin_bit_cast(u32x4, cs);
      if (col < 8) so9[8] = OFF_CSP + ((ps * 176 + 8 * ng + col) * 16 + g * 4) * 4;
    } else if (id < 352 + 8 * NKF) {
      const int ct = (id - 352) & 7, kp = (id - 352) >> 3;
      u32x4 gv[4];
#pragma unroll
      for (int i = 0; i < 4; ++i)
        gv[i] = *(const u32x4*)(A.wd + (size_t)(8 * tile_of_oct(4 * kp + i) + kr) * SD + ct * 64 + no * 8);
#pragma unroll
      for (int i = 0; i < 4; ++i)
        *(u32x4*)(img + (8 * i + kr) * 256 + (((no >> 1) ^ ((kr & 3) | ((i & 1) << 2))) * 32) + (no & 1) * 16) = gv[i];
      wait_lgkm0();
      asm volatile("" ::: "memory");
#pragma unroll
      for (int nt = 0; nt < 4; ++nt) {
        const u32x2 lo = lds_tr32(ra + ((nt ^ rkey) * 32)), hi = lds_tr32(ra + ((nt ^ rkey) * 32) + 1024);
        fr[nt] = u32x4{lo[0], lo[1], hi[0], hi[1]};
        so9[nt] = lane * 16 + OFF_WD + ((ct * NKF + kp) * 4 + nt) * 1024;
      }
    }
    if (id < 352) {
#pragma unroll
      for (int i = 0; i < 8; ++i)
        if ((8 * (id >> 4) + i) % 11 < 6) __builtin_amdgcn_raw_buffer_store_b128(fr[i], rsw, so9[i], 0, 16);
      __builtin_amdgcn_raw_buffer_store_b128(fr[8], rsw, so9[8], 0, 16);
    }
    lds_post(lds0 + L_MISC + 28, lane);
    lds_wait_slow(lds0 + L_MISC + 36, 4u);
    tag = lds_ld(lds0 + L_MISC + 4);
    wait_vm<0>();
    if (id < 352) {
      if (lane == 0)
        __hip_atomic_fetch_add((gu32*)(ws + OFF_CNT) + (256 + (id >> 4)) * 32, 1u, __ATOMIC_RELAXED, __HIP_MEMORY_SCOPE_AGENT);
#pragma unroll
      for (int i = 0; i < 8; ++i)
        if ((8 * (id >> 4) + i) % 11 >= 6) __builtin_amdgcn_raw_buffer_store_b128(fr[i], rsw, so9[i], 0, 16);
    } else if (id < 352 + 8 * NKF) {
#pragma unroll
      for (int i = 0; i < 4; ++i) __builtin_amdgcn_raw_buffer_store_b128(fr[i], rsw, so9[i], 0, 16);
    }
    wait_vm<0>();
    if (lds_arrive(lds0 + L_MISC + 40, lane) == 3u && lane == 0)
      __hip_atomic_fetch_add((gu32*)(ws + OFF_CNT) + (288 + (b & 7)) * 32, 1u, __ATOMIC_RELAXED, __HIP_MEMORY_SCOPE_AGENT);
  }
  const int xcd = b & 7, idx = b >> 3;
  const int rb = (xcd & 1) * 8 + (idx & 7), c = (xcd >> 1) * 4 + (idx >> 3);
  const int rb64 = xcd * 4 + (idx & 3), ct = idx >> 2;
  const u32 wbase = OFF_WGU + c * (176 * 1024) + lane * 16;
  const u32 wdo = OFF_WD + ct * NKF * 4096 + (lw - 1) * 8192 + lane * 16;
  if (lw == 0) {
    u32x4 kimg[17];
    const u32 ko = lds0 + lane * 16;
#pragma unroll 1
    for (int layer = 0; layer < 2; ++layer) {
      asm volatile("" : "+s"(ws));
      const u32 xin = OFF_X + layer * X_SZ;
      if (layer == 1) {
#pragma unroll
        for (int i = 0; i < 17; ++i)
          if (i < 16 || lane < 32) *(LAS u32x4*)(ko + i * 1024) = kimg[i];
        lds_post(lds0 + L_MISC + 12, lane);
        u32x4 pv;
        __builtin_amdgcn_s_sleep(GR_DELAY);
#pragma unroll 1
        for (int sp = 0; sp < (1 << 18); ++sp) {
          pv = __builtin_amdgcn_raw_buffer_load_b128(rsw, OFF_PART + (no * SL + 8 * b + kr) * 16, 0, XG_AUX);
          if (__builtin_amdgcn_ballot_w64(((pv[1] ^ tag) | (pv[3] ^ tag)) != 0u) == 0ull) break;
          __builtin_amdgcn_s_sleep(3);
        }
        publish_ln(ws, OFF_GRAN, __uint_as_float(pv[0]), __uint_as_float(pv[2]), tag + 1u, b, lane);
        if (lane == 0) lds_st(lds0 + L_MISC + 8, 2u);
      } else {
        lds_wait_slow(lds0 + L_MISC + 12, 4u);
#pragma unroll
        for (int i = 0; i < 17; ++i)
          if (i < 16 || lane < 32) kimg[i] = lds_rd128(ko + i * 1024);
        gu32* fp = (gu32*)(ws + OFF_CNT) + (256 + min((c * 11 >> 3) + lane, (c * 11 + 10) >> 3)) * 32;
        lds_wait_slow(lds0 + L_MISC + 28, 4u);
        __builtin_amdgcn_s_sleep(FL_DELAY);
#pragma unroll 1
        for (int sp = 0; sp < (1 << 20); ++sp) {
          u32 v = 16u;
          if (lane < 3) v = __hip_atomic_load(fp, __ATOMIC_RELAXED, __HIP_MEMORY_SCOPE_AGENT);
          if (__builtin_amdgcn_ballot_w64(v < 16u) == 0ull) break;
          __builtin_amdgcn_s_sleep(2);
        }
        asm volatile("" ::: "memory");
        if (lane == 0) lds_st(lds0 + L_MISC + 28, 5u);
        f32x4 cv[6];
#pragma unroll
        for (int r = 0; r < 6; ++r) {
          const u32 co = OFF_CSP + (no * 176 + c * 11) * 64 + min(r * 8 + kr, 43) * 16;
          cv[r] = __builtin_bit_cast(f32x4, __builtin_amdgcn_raw_buffer_load_b128(rsw, co, 0, 16)) +
                  __builtin_bit_cast(f32x4, __builtin_amdgcn_raw_buffer_load_b128(rsw, co + 8 * 176 * 64, 0, 16));
        }
#pragma unroll
        for (int r = 0; r < 6; ++r) {
          const f32x4 v = {sum8(cv[r][0]), sum8(cv[r][1]), sum8(cv[r][2]), sum8(cv[r][3])};
          if (no == 0) *(f32x4*)(smem + L_CS + min(r * 8 + kr, 43) * 16) = v;
        }
        wait_lgkm0();
#pragma unroll 1
        for (int sp = 0; sp < (1 << 20); ++sp) {
          u32 v = 32u;
          if (lane < 8) v = __hip_atomic_load((gu32*)(ws + OFF_CNT) + (288 + lane) * 32, __ATOMIC_RELAXED, __HIP_MEMORY_SCOPE_AGENT);
          if (__builtin_amdgcn_ballot_w64(v < 32u) == 0ull) break;
          __builtin_amdgcn_s_sleep(8);
        }
        asm volatile("" ::: "memory");
      }
      if (lds_ld(lds0 + L_MISC + 24) < 4u * (layer + 1)) {
        lds_wait_slow(lds0 + L_MISC + 24, 4u * (layer + 1));
        __builtin_amdgcn_s_sleep(X_DELAY);
      }
      grid_wait(ws, layer, b, lane);
      u32 vo[2];
#pragma unroll
      for (int o = 0; o < 2; ++o) {
        const int seg = ((lane & 15) >> 1) ^ ((lane >> 4) | (o << 2));
        vo[o] = xin + rb * (SD * 256) + (lane >> 4) * 256 + seg * 32 + (lane & 1) * 16;
      }
      auto stage = [&](int s) {
#pragma unroll
        for (int i = 0; i < 4; ++i)
          asm volatile(
              "s_mov_b32 m0, %3\n\ts_nop 0\n\tglobal_load_lds_dwordx4 %0, %2 sc1\n\tglobal_load_lds_dwordx4 %0, %2 offset:1024 sc1\n\t"
              "global_load_lds_dwordx4 %1, %2 offset:2048 sc1\n\tglobal_load_lds_dwordx4 %1, %2 offset:3072 sc1"
              ::"v"(vo[0]), "v"(vo[1]), "s"(ws + s * 16384 + i * 4096), "s"(lds0 + (s % 3) * 16384 + i * 4096) : "memory");
      };
      stage(0); stage(1);
      static_for<0, 8>([&](auto I) {
        constexpr int i = decltype(I)::value;
        wait_vm<(i < 7 ? 16 : 0)>();
        bar();
        if constexpr (i < 6) stage(i + 2);
      });
      gu32* cs_ = (gu32*)(ws + OFF_CNT) + (layer * 64 + rb * 4) * 32;
      gu32* cw_ = (gu32*)(ws + OFF_CNT) + (layer * 64 + (rb64 >> 1) * 4) * 32;
      bar();
      bar();
      if (lane == 0) __hip_atomic_fetch_add(cs_ + 96, 1u, __ATOMIC_RELAXED, __HIP_MEMORY_SCOPE_AGENT);
      bar();
      if (lane == 0) __hip_atomic_fetch_add(cs_, 1u, __ATOMIC_RELAXED, __HIP_MEMORY_SCOPE_AGENT);
      bar();
      if (lane == 0) poll_ge(cw_ + 96, 16u);
      bar();
      if (lane == 0) poll_ge(cw_, 16u);
      bar();
      if (lane == 0) __hip_atomic_fetch_add(cs_ + 32, 1u, __ATOMIC_RELAXED, __HIP_MEMORY_SCOPE_AGENT);
      bar();
      if (lane == 0) {
        __hip_atomic_fetch_add(cs_ + 64, 1u, __ATOMIC_RELAXED, __HIP_MEMORY_SCOPE_AGENT);
        poll_ge(cw_ + 32, 16u);
      }
      bar();
      if (lane == 0) poll_ge(cw_ + 64, 16u);
      bar();
      if (layer == 0) bar();
    }
  } else {
    u32x4 px[8], py[8], pq[4];
#pragma unroll 1
    for (int layer = 0; layer < 2; ++layer) {
      asm volatile("" : "+s"(ws));
      const u32 xin = OFF_X + layer * X_SZ;
      lds_wait_slow(layer == 0 ? lds0 + L_MISC + 28 : lds0 + L_MISC + 36, layer == 0 ? 5u : 8u);
      const u32 wo = wbase + (lw - 1) * 4096;
#pragma unroll
      for (int i = 0; i < 8; ++i) {
        if (lds_ld(lds0 + L_MISC + 48) > 4u * layer) lds_wait_slow(lds0 + L_MISC + 24, 4u * (layer + 1));
        dma4(wo, ws + i * 12288, lds0 + F_W1 + i * 12288 + (lw - 1) * 4096);
      }
      static_for<0, 8>([&](auto I) {
        constexpr int i = decltype(I)::value;
        wait_vm<(i == 0 ? 28 : 24)>();
        bar();
        if constexpr (i >= 1)
          dma4(wo, ws + (96 + (i - 1) * 12) * 1024,
               (i == 7 && lw == 3) ? lds0 + F_BASE : lds0 + F_W1 + (i - 1) * 12288 + (lw - 1) * 4096);
      });
      auto wd3 = [&](auto U, int n) {
        constexpr int u0 = decltype(U)::value;
        if (lw <= n) {
          const u32 home = lds0 + (lw == 1 ? f_wd(u0) : lw == 2 ? f_wd(u0 + 1) : f_wd(u0 + 2));
          dma4(wdo, ws + u0 * 8192, home);
          dma4(wdo, ws + u0 * 8192 + 4096, home + 4096);
        }
      };
      using std::integral_constant;
      wait_vm<16>();
      bar();
      wd3(integral_constant<int, 0>{}, 3); wd3(integral_constant<int, 3>{}, 3); wd3(integral_constant<int, 6>{}, 3);
      if (layer == 0) {
#pragma unroll
        for (int i = 0; i < 8; ++i) gload16_sc1(px[i], wdo, ws + 15 * 8192 + i * 1024);
      }
      wait_vm<24>();
      bar();
      wd3(integral_constant<int, 9>{}, 2);
      if (layer == 0) {
#pragma unroll
        for (int i = 0; i < 4; ++i) gload16_sc1(py[i], wdo, ws + 18 * 8192 + i * 1024);
      }
      bar();
      wd3(integral_constant<int, 11>{}, 2);
      if (layer == 0) {
#pragma unroll
        for (int i = 4; i < 8; ++i) gload16_sc1(py[i], wdo, ws + 18 * 8192 + i * 1024);
      }
      bar();
      wd3(integral_constant<int, 13>{}, 2);
      if (layer == 0 && lw == 3) {
#pragma unroll
        for (int i = 0; i < 4; ++i) gload16_sc1(pq[i], wdo, ws + 19 * 8192 + i * 1024);
      }
      bar();
      u32x4 rr[4];
      {
        const u32 ro = xin + ((rb64 >> 1) * SD + ct * 64 + (lw & 1) * 32 + kr) * 256 + (rb64 & 1) * 128 + no * 16;
#pragma unroll
        for (int i = 0; i < 4; ++i) rr[i] = __builtin_amdgcn_raw_buffer_load_b128(rsw, lw == 3 ? 0x7ffffff0u : ro + i * 2048, 0, 16);
      }
      wait_vm<4>();
#pragma unroll
      for (int i = 0; i < 8; ++i) asm volatile("" : "+v"(px[i]), "+v"(py[i]), "+v"(pq[i & 3]));
      bar();
      const u32 hx = lds0 + f_wd(15) + (lw - 1) * 8192 + lane * 16;
#pragma unroll
      for (int i = 0; i < 8; ++i) *(LAS u32x4*)(hx + i * 1024) = px[i];
      bar();
      const u32 hy = lds0 + (lw == 1 ? f_wd(18) : lw == 2 ? f_wd(19) : f_wd(20)) + lane * 16;
#pragma unroll
      for (int i = 0; i < 8; ++i) *(LAS u32x4*)(hy + i * 1024) = py[i];
      bar();
      if (lw == 3) {
#pragma unroll
        for (int i = 0; i < 4; ++i) *(LAS u32x4*)(lds0 + f_wd(21) + lane * 16 + i * 1024) = pq[i];
      } else {
#pragma unroll
        for (int i = 0; i < 4; ++i) *(LAS u32x4*)(lds0 + F_RES + ((lw & 1) * 32 + i * 8 + kr) * 144 + no * 16) = rr[i];
      }
      lds_bar();
      if (layer == 0) bar();
    }
  }
}

__global__ __launch_bounds__(512, 2) void k_all(Args A) {
  extern __shared__ __attribute__((aligned(16))) char smem[];
  const int tid = threadIdx.x, w = __builtin_amdgcn_readfirstlane(tid >> 6), b = blockIdx.x;
  if (w == 7) {
    const char* pc;
    asm volatile("s_getpc_b64 %0" : "=s"(pc));
    const u32 line = (b >> 3) * CODE_N + (tid & 63) % CODE_N;
    asm volatile("s_mov_b32 m0, %2\n\ts_nop 0\n\tglobal_load_lds_dword %0, %1"
                 ::"v"(line * 128u), "s"(pc), "s"((u32)(uintptr_t)(LAS char*)smem + L_DUMP) : "memory");
  }
  if (w < 4) compute_waves(A, smem, tid, w, b);
  else helper_waves(A, smem, tid, w - 4, b);
}

extern "C" void kernel_launch(void* const* d_in, const int* in_sizes, int n_in, void* d_out, int out_size, void* d_ws,
                              size_t ws_size, hipStream_t stream) {
  Args A;
  A.x = (const u16*)d_in[0];
  A.logA = (const u16*)d_in[1];
  A.Aim = (const u16*)d_in[2];
  A.Cre = (const u16*)d_in[3];
  A.Cim = (const u16*)d_in[4];
  A.logdt = (const u16*)d_in[5];
  A.dskip = (const u16*)d_in[6];
  A.wg = (const u16*)d_in[8];
  A.wu = (const u16*)d_in[9];
  A.wd = (const u16*)d_in[10];
  A.out = (u16*)d_out;
  A.ws = (char*)d_ws;
  (void)hipFuncSetAttribute((const void*)k_all, hipFuncAttributeMaxDynamicSharedMemorySize, LDS_BYTES);
  k_all<<<256, 512, LDS_BYTES, stream>>>(A);
}
```
